```python
import torch

def _softmax(x, axis=-1):
    return torch.softmax(x, dim=axis)
def _gelu(x):
    return torch.nn.functional.gelu(x, approximate="tanh")
def _silu(x):
    return torch.nn.functional.silu(x)
def _sigmoid(x):
    return torch.sigmoid(x)
def _layernorm(x, axis=-1, eps=1e-5):
    m = x.mean(dim=axis, keepdim=True)
    v = x.var(dim=axis, keepdim=True, unbiased=False)
    return (x - m) / torch.sqrt(v + eps)
def _scatter_add(tgt, idx, src, axis=0):
    return tgt.clone().index_add_(axis, idx, src)
def _segment_sum(src, seg, n):
    out = torch.zeros((n,) + src.shape[1:], dtype=src.dtype)
    return out.index_add_(0, seg, src)
def _pad(x, pw):
    flat = tuple(v for pair in reversed(pw) for v in pair)
    return torch.nn.functional.pad(x, flat)

kernel_name = 'recurrent_scan__mha_block_cached__mixer_channel_mlp__decode'

def setup_inputs():

    _w = (lambda a: (lambda t: t.to(torch.bfloat16) if t.is_floating_point() else t)(torch.as_tensor(a)))
    def _A_setup():
        B, Sq, D = 4, 1, 512
        H, Sk, Dh = 32, 4096, 128
        return {
            "x": _w((lambda *s: torch.randn(*s, dtype=torch.bfloat16) * 0.1)(B, Sq, D)),
            "k_cache": _w((lambda *s: torch.randn(*s, dtype=torch.bfloat16) * 0.8)(B, H, Sk, Dh)),
            "v_cache": _w((lambda *s: torch.randn(*s, dtype=torch.bfloat16) * 3.2)(B, H, Sk, Dh)),
            "wq": _w((lambda *s: torch.randn(*s, dtype=torch.bfloat16) * 0.8)(D, H * Dh) * (D ** -0.5)),
            "wo": _w((lambda *s: torch.randn(*s, dtype=torch.bfloat16) * 3.2)(H * Dh, D) * ((H * Dh) ** -0.5)),
        }
    _A_in = _A_setup()

    def _B_setup():
        B, S, D, F = 4, 1, 512, 1792
        return {
            "x": _w((lambda *s: torch.randn(*s, dtype=torch.bfloat16) * 0.1)(B, S, D)),
            "w1": _w((lambda *s: torch.randn(*s, dtype=torch.bfloat16) * 3.2)(D, F) * (D ** -0.5)),
            "b1": _w((lambda *s: torch.randn(*s, dtype=torch.bfloat16) * 3.2)(F)),
            "w2": _w((lambda *s: torch.randn(*s, dtype=torch.bfloat16) * 3.2)(F, D) * (F ** -0.5)),
            "b2": _w((lambda *s: torch.randn(*s, dtype=torch.bfloat16) * 3.2)(D)),
        }
    _B_in = _B_setup()

    return {
        "A_x": _A_in["x"],
        "A_k_cache": _A_in["k_cache"],
        "A_v_cache": _A_in["v_cache"],
        "A_wq": _A_in["wq"],
        "A_wo": _A_in["wo"],
        "B_x": _B_in["x"],
        "B_w1": _B_in["w1"],
        "B_b1": _B_in["b1"],
        "B_w2": _B_in["w2"],
        "B_b2": _B_in["b2"],
    }

def reference(A_x, A_k_cache, A_v_cache, A_wq, A_wo, B_x, B_w1, B_b1, B_w2, B_b2):
    def _A_ref(x, k_cache, v_cache, wq, wo):
        B, Sq, D = x.shape
        H, Dh = 32, 128
        q = (lambda x, *d: x.permute(*d))((x @ wq).reshape(B, Sq, H, Dh), 0, 2, 1, 3)
        scores = torch.einsum("bhqd,bhkd->bhqk", q, k_cache) * (Dh ** -0.5)
        p = _softmax(scores, axis=-1)
        o = torch.einsum("bhqk,bhkd->bhqd", p, v_cache)
        return (lambda x, *d: x.permute(*d))(o, 0, 2, 1, 3).reshape(B, Sq, H * Dh) @ wo

    def _B_ref(x, w1, b1, w2, b2):
        y = _layernorm(x, axis=-1)
        h = _gelu(torch.einsum("bsd,df->bsf", y, w1) + b1)
        return torch.einsum("bsf,fd->bsd", h, w2) + b2

    x = A_x
    xn = _layernorm(x, axis=-1)
    s = _A_ref(xn, A_k_cache, A_v_cache, A_wq, A_wo)
    g = _silu(_B_ref(xn, B_w1, B_b1, B_w2, B_b2))
    return x + s * g
```

```cpp
#include <hip/hip_runtime.h>
#include <hip/hip_bf16.h>
#include <stdint.h>

#define KV_AUX 2

typedef unsigned v4u __attribute__((ext_vector_type(4)));
typedef unsigned v2u __attribute__((ext_vector_type(2)));
typedef float f32x4 __attribute__((ext_vector_type(4)));
typedef float f32x2 __attribute__((ext_vector_type(2)));
typedef __bf16 bf16x2 __attribute__((ext_vector_type(2)));

constexpr int NB = 4, DM = 512, NH = 32, SK = 4096, DH = 128, FF = 1792;
constexpr int RSRC_W3 = 0x00020000;

__device__ __forceinline__ float dot2(unsigned a, unsigned b, float acc) {
  return __builtin_amdgcn_fdot2_f32_bf16(__builtin_bit_cast(bf16x2, a), __builtin_bit_cast(bf16x2, b), acc, false);
}
__device__ __forceinline__ float bflo(unsigned u) { return __builtin_bit_cast(float, u << 16); }
__device__ __forceinline__ float bfhi(unsigned u) { return __builtin_bit_cast(float, u & 0xffff0000u); }
__device__ __forceinline__ unsigned pk_bf16(float lo, float hi) {
  bf16x2 r; r.x = (__bf16)lo; r.y = (__bf16)hi; return __builtin_bit_cast(unsigned, r);
}
__device__ __forceinline__ float rbf(float x) { return bflo((unsigned)__builtin_bit_cast(unsigned short, (__bf16)x)); }
template <int CTRL> __device__ __forceinline__ float dpp(float x) {
  return __builtin_bit_cast(float, __builtin_amdgcn_mov_dpp(__builtin_bit_cast(int, x), CTRL, 0xf, 0xf, true));
}
constexpr int XOR1 = 0xB1, XOR2 = 0x4E, XOR7 = 0x141, XOR8 = 0x128, BCAST = 0x150;
__device__ __forceinline__ float tr32(float a, float b) {
  auto s = __builtin_amdgcn_permlane32_swap(__builtin_bit_cast(unsigned, a), __builtin_bit_cast(unsigned, b), false, false);
  return __uint_as_float(s[0]) + __uint_as_float(s[1]);
}
__device__ __forceinline__ float tr16(float a, float b) {
  auto s = __builtin_amdgcn_permlane16_swap(__builtin_bit_cast(unsigned, a), __builtin_bit_cast(unsigned, b), false, false);
  return __uint_as_float(s[0]) + __uint_as_float(s[1]);
}
__device__ __forceinline__ float wave_sum(float x) {
  x += dpp<XOR1>(x); x += dpp<XOR2>(x); x += dpp<XOR7>(x); x += dpp<XOR8>(x);
  x = tr16(x, x); x = tr32(x, x);
  return x;
}
__device__ __forceinline__ float wave_max(float x) {
  x = fmaxf(x, dpp<XOR1>(x)); x = fmaxf(x, dpp<XOR2>(x)); x = fmaxf(x, dpp<XOR7>(x)); x = fmaxf(x, dpp<XOR8>(x));
  auto s = __builtin_amdgcn_permlane16_swap(__builtin_bit_cast(unsigned, x), __builtin_bit_cast(unsigned, x), false, false);
  x = fmaxf(__uint_as_float(s[0]), __uint_as_float(s[1]));
  auto t = __builtin_amdgcn_permlane32_swap(__builtin_bit_cast(unsigned, x), __builtin_bit_cast(unsigned, x), false, false);
  return fmaxf(__uint_as_float(t[0]), __uint_as_float(t[1]));
}
__device__ __forceinline__ __amdgpu_buffer_rsrc_t mk_rsrc(const void* p, unsigned bytes) {
  return __builtin_amdgcn_make_buffer_rsrc(const_cast<void*>(p), 0, bytes, RSRC_W3);
}
__device__ __forceinline__ void ln_stats(const float (&v)[8], float& mean, float& sd) {
  float s = 0.f;
#pragma unroll
  for (int j = 0; j < 8; ++j) s += v[j];
  const float mu = wave_sum(s) * (1.0f / DM);
  float q = 0.f;
#pragma unroll
  for (int j = 0; j < 8; ++j) { const float d = v[j] - mu; q = fmaf(d, d, q); }
  const float var = wave_sum(q) * (1.0f / DM);
  mean = rbf(mu);
  sd = rbf(sqrtf(rbf(rbf(var) + 1e-5f)));
}
__device__ __forceinline__ float ln_apply(float x, float mean, float sd) { return rbf(rbf(x - mean) / sd); }

constexpr size_t WS_T = 0;
constexpr size_t WS_ML = 256 * 512;
constexpr size_t WS_H = WS_ML + 512;

struct Params {
  const unsigned short* x; const unsigned short* k; const unsigned short* v; const unsigned short* wq; const unsigned short* wo;
  const unsigned short* w1; const unsigned short* b1; const unsigned short* w2; const unsigned short* b2;
  unsigned short* out; float* ws;
};

__global__ void __launch_bounds__(512, 2) k_attn(Params p) {
  __shared__ __attribute__((aligned(16))) float lds[8 * 128 + 4 * 512 + 8 * 32 + 8 * 128 + 8 * 128 + 32];
  float* l_q = lds;
  float* l_y = l_q + 8 * 128;
  float* l_h = l_y + 4 * 512;
  float* l_o = l_h + 8 * 32;
  float* l_p = l_o + 8 * 128;
  float* l_ml = l_p + 8 * 128;
  const int blk = blockIdx.x, tid = threadIdx.x, lane = tid & 63, w = __builtin_amdgcn_readfirstlane(tid >> 6);
  const int idx = blk >> 3, h = (blk & 7) * 4 + (idx & 3), bs = idx >> 2, b = bs >> 1, sp = bs & 1;
  const int r = lane >> 4, c = lane & 15;

  const __amdgpu_buffer_rsrc_t rx = mk_rsrc(p.x, NB * DM * 2);
  const v4u xa = __builtin_amdgcn_raw_buffer_load_b128(rx, lane * 16, b * DM * 2, 0);
  const v4u xm = __builtin_amdgcn_raw_buffer_load_b128(rx, lane * 16, (w & 3) * DM * 2, 0);
  v4u xr[2];
  xr[0] = __builtin_amdgcn_raw_buffer_load_b128(rx, r * 32, b * DM * 2 + w * 128, 0);
  xr[1] = __builtin_amdgcn_raw_buffer_load_b128(rx, r * 32 + 16, b * DM * 2 + w * 128, 0);
  v4u wt[16];
  {
    const __amdgpu_buffer_rsrc_t rq = mk_rsrc(p.wq, DM * NH * DH * 2);
    const int voff = r * 16 * 8192 + c * 16;
#pragma unroll
    for (int i = 0; i < 16; ++i) wt[i] = __builtin_amdgcn_raw_buffer_load_b128(rq, voff, (w * 64 + i) * 8192 + h * 256, 0);
  }
  const bool has_mlp = idx < 28;
  const int u = (blk & 7) * 28 + idx;
  v4u w1v = {0u, 0u, 0u, 0u};
  if (has_mlp) w1v = __builtin_amdgcn_raw_buffer_load_b128(mk_rsrc(p.w1, DM * FF * 2), tid * (FF * 2), u * 16, 0);
  const size_t kvoff = ((size_t)(b * NH + h) * SK + sp * 2048 + w * 256) * DH;
  const __amdgpu_buffer_rsrc_t rk = mk_rsrc(p.k + kvoff, 65536), rv = mk_rsrc(p.v + kvoff, 65536);
  v4u win[16];
#pragma unroll
  for (int i = 0; i < 16; ++i) win[i] = __builtin_amdgcn_raw_buffer_load_b128(rk, lane * 16, i * 1024, KV_AUX);
  __builtin_amdgcn_sched_barrier(0);

  float mean, sd;
  {
    float v[8];
#pragma unroll
    for (int j = 0; j < 4; ++j) { v[2 * j] = bflo(xa[j]); v[2 * j + 1] = bfhi(xa[j]); }
    ln_stats(v, mean, sd);
  }
  if (w < 4) {
    float v[8], m1, s1, m2, s2;
#pragma unroll
    for (int j = 0; j < 4; ++j) { v[2 * j] = bflo(xm[j]); v[2 * j + 1] = bfhi(xm[j]); }
    ln_stats(v, m1, s1);
#pragma unroll
    for (int j = 0; j < 8; ++j) v[j] = ln_apply(v[j], m1, s1);
    ln_stats(v, m2, s2);
#pragma unroll
    for (int j = 0; j < 8; ++j) v[j] = ln_apply(v[j], m2, s2);
    *(f32x4*)(l_y + w * 512 + lane * 8) = f32x4{v[0], v[1], v[2], v[3]};
    *(f32x4*)(l_y + w * 512 + lane * 8 + 4) = f32x4{v[4], v[5], v[6], v[7]};
  }
  unsigned xn[8];
#pragma unroll
  for (int j = 0; j < 8; ++j) {
    const unsigned t = xr[j >> 2][j & 3];
    xn[j] = pk_bf16(ln_apply(bflo(t), mean, sd), ln_apply(bfhi(t), mean, sd));
  }
  {
    float a[8];
#pragma unroll
    for (int j = 0; j < 8; ++j) a[j] = 0.f;
#pragma unroll
    for (int i = 0; i < 8; ++i)
#pragma unroll
      for (int j = 0; j < 4; ++j) {
        const unsigned e = wt[2 * i][j], o = wt[2 * i + 1][j];
        a[2 * j] = dot2(__builtin_amdgcn_perm(o, e, 0x05040100u), xn[i], a[2 * j]);
        a[2 * j + 1] = dot2(__builtin_amdgcn_perm(o, e, 0x07060302u), xn[i], a[2 * j + 1]);
      }
    float t4[4];
#pragma unroll
    for (int j = 0; j < 4; ++j) t4[j] = tr32(a[j], a[j + 4]);
    const float t0 = tr16(t4[0], t4[2]), t1 = tr16(t4[1], t4[3]);
    *(f32x2*)(l_q + w * 128 + 8 * c + 2 * r) = f32x2{t0, t1};
  }
  __syncthreads();
  unsigned q[4];
  {
    f32x4 s0 = {0.f, 0.f, 0.f, 0.f}, s1 = s0;
#pragma unroll
    for (int ww = 0; ww < 8; ++ww) { s0 += *(const f32x4*)(l_q + ww * 128 + 8 * c); s1 += *(const f32x4*)(l_q + ww * 128 + 8 * c + 4); }
    q[0] = pk_bf16(s0[0], s0[1]); q[1] = pk_bf16(s0[2], s0[3]); q[2] = pk_bf16(s1[0], s1[1]); q[3] = pk_bf16(s1[2], s1[3]);
  }
  {
    float a[32];
#pragma unroll
    for (int bb = 0; bb < 4; ++bb) {
      const float y = l_y[bb * 512 + tid];
#pragma unroll
      for (int j = 0; j < 4; ++j) { a[bb * 8 + 2 * j] = y * bflo(w1v[j]); a[bb * 8 + 2 * j + 1] = y * bfhi(w1v[j]); }
    }
#pragma unroll
    for (int j = 0; j < 16; ++j) a[j] = tr32(a[j], a[j + 16]);
#pragma unroll
    for (int j = 0; j < 8; ++j) a[j] = tr16(a[j], a[j + 8]);
#define TR_STEP(HALF, CTRL, BIT) _Pragma("unroll") for (int j = 0; j < (HALF); ++j) { \
      const float lo_ = a[j], hi_ = a[j + (HALF)]; const float t1_ = lo_ + dpp<CTRL>(lo_), t2_ = hi_ + dpp<CTRL>(hi_); a[j] = (lane & (BIT)) ? t2_ : t1_; }
    TR_STEP(4, XOR8, 8) TR_STEP(2, XOR7, 4) TR_STEP(1, XOR2, 2)
    const float z = a[0] + dpp<XOR1>(a[0]);
    if ((lane & 1) == 0) l_h[w * 32 + (lane >> 1)] = z;
  }
  const int rg = lane >> 3, cg = lane & 7;
  {
    const __amdgpu_buffer_rsrc_t ro = mk_rsrc(p.wo, NH * DH * DM * 2);
    const int voff = rg * 16 * 1024 + cg * 16;
#pragma unroll
    for (int i = 0; i < 16; ++i) wt[i] = __builtin_amdgcn_raw_buffer_load_b128(ro, voff, (h * 128 + i) * 1024 + w * 128, 0);
  }
  __builtin_amdgcn_sched_barrier(0);

  float sc[4];
#pragma unroll
  for (int g = 0; g < 4; ++g) {
    float a[16];
#pragma unroll
    for (int i = 0; i < 16; ++i) {
      const v4u t = win[i];
      float d = dot2(t[0], q[0], 0.f); d = dot2(t[1], q[1], d); d = dot2(t[2], q[2], d); d = dot2(t[3], q[3], d);
      a[i] = d;
      if (g < 3) win[i] = __builtin_amdgcn_raw_buffer_load_b128(rk, lane * 16, (g + 1) * 16384 + i * 1024, KV_AUX);
      else win[i] = __builtin_amdgcn_raw_buffer_load_b128(rv, lane * 16, i * 1024, KV_AUX);
      __builtin_amdgcn_sched_barrier(0);
    }
    TR_STEP(8, XOR8, 8) TR_STEP(4, XOR7, 4) TR_STEP(2, XOR2, 2) TR_STEP(1, XOR1, 1)
    sc[g] = a[0];
    __builtin_amdgcn_sched_barrier(0);
  }
  constexpr float C2 = 0.088388347648318440f * 1.4426950408889634f;
  const float mw = wave_max(fmaxf(fmaxf(sc[0], sc[1]), fmaxf(sc[2], sc[3]))) * C2;
#pragma unroll
  for (int g = 0; g < 4; ++g) sc[g] = __builtin_amdgcn_exp2f(fmaf(sc[g], C2, -mw));
  const float lw = wave_sum((sc[0] + sc[1]) + (sc[2] + sc[3]));
  if (lane == 0) *(f32x2*)(l_ml + 2 * w) = f32x2{mw, lw};

  float acc[8];
#pragma unroll
  for (int j = 0; j < 8; ++j) acc[j] = 0.f;
#define PV_STEP(I) { const v4u t = win[I]; const float pb = dpp<BCAST + (I)>(sc[g]); \
    _Pragma("unroll") for (int j = 0; j < 4; ++j) { acc[2 * j] = fmaf(pb, bflo(t[j]), acc[2 * j]); acc[2 * j + 1] = fmaf(pb, bfhi(t[j]), acc[2 * j + 1]); } \
    if (g < 3) win[I] = __builtin_amdgcn_raw_buffer_load_b128(rv, lane * 16, (g + 1) * 16384 + (I) * 1024, KV_AUX); \
    __builtin_amdgcn_sched_barrier(0); }
#pragma unroll
  for (int g = 0; g < 4; ++g) {
    PV_STEP(0) PV_STEP(1) PV_STEP(2) PV_STEP(3) PV_STEP(4) PV_STEP(5) PV_STEP(6) PV_STEP(7)
    PV_STEP(8) PV_STEP(9) PV_STEP(10) PV_STEP(11) PV_STEP(12) PV_STEP(13) PV_STEP(14) PV_STEP(15)
  }
  {
    float t4[4];
#pragma unroll
    for (int j = 0; j < 4; ++j) t4[j] = tr32(acc[j], acc[j + 4]);
    const float t0 = tr16(t4[0], t4[2]), t1 = tr16(t4[1], t4[3]);
    *(f32x2*)(l_o + w * 128 + 8 * c + 2 * r) = f32x2{t0, t1};
  }
  __syncthreads();
  float M = -INFINITY;
  float mm[8], ll[8];
#pragma unroll
  for (int ww = 0; ww < 8; ++ww) { const f32x2 t = *(const f32x2*)(l_ml + 2 * ww); mm[ww] = t[0]; ll[ww] = t[1]; M = fmaxf(M, t[0]); }
  float L = 0.f; f32x2 o2 = {0.f, 0.f};
#pragma unroll
  for (int ww = 0; ww < 8; ++ww) {
    const float f = __builtin_amdgcn_exp2f(mm[ww] - M);
    L = fmaf(f, ll[ww], L);
    const f32x2 t = *(const f32x2*)(l_o + ww * 128 + 2 * lane);
    o2[0] = fmaf(f, t[0], o2[0]); o2[1] = fmaf(f, t[1], o2[1]);
  }
  *(f32x2*)(l_p + w * 128 + 2 * lane) = o2;
  __builtin_amdgcn_fence(__ATOMIC_RELEASE, "wavefront");
  __builtin_amdgcn_wave_barrier();
  __builtin_amdgcn_fence(__ATOMIC_ACQUIRE, "wavefront");
  float a[8];
#pragma unroll
  for (int j = 0; j < 8; ++j) a[j] = 0.f;
#pragma unroll
  for (int i4 = 0; i4 < 4; ++i4) {
    const f32x4 ov = *(const f32x4*)(l_p + w * 128 + 16 * rg + 4 * i4);
#pragma unroll
    for (int ii = 0; ii < 4; ++ii) {
      const v4u t = wt[4 * i4 + ii];
#pragma unroll
      for (int j = 0; j < 4; ++j) { a[2 * j] = fmaf(ov[ii], bflo(t[j]), a[2 * j]); a[2 * j + 1] = fmaf(ov[ii], bfhi(t[j]), a[2 * j + 1]); }
    }
  }
#pragma unroll
  for (int j = 0; j < 4; ++j) a[j] = tr32(a[j], a[j + 4]);
#pragma unroll
  for (int j = 0; j < 2; ++j) a[j] = tr16(a[j], a[j + 2]);
  TR_STEP(1, XOR8, 8)
  const int slot = (b * NH + h) * 2 + sp;
  p.ws[WS_T + (size_t)slot * 512 + 64 * w + 8 * cg + rg] = a[0];
  if (tid == 0) { p.ws[WS_ML + 2 * slot] = M; p.ws[WS_ML + 2 * slot + 1] = L; }
  if (has_mlp && tid < 32) {
    float z = 0.f;
#pragma unroll
    for (int ww = 0; ww < 8; ++ww) z += l_h[ww * 32 + tid];
    const int col = u * 8 + (tid & 7);
    z = rbf(rbf(z) + bflo(p.b1[col]));
    const float inner = 0.7978845608028654f * (z + 0.044715f * z * z * z);
    const float g = 0.5f * z * (1.0f + tanhf(inner));
    p.ws[WS_H + (tid >> 3) * FF + col] = rbf(g);
  }
}

__global__ void __launch_bounds__(512) k_final(Params p) {
  __shared__ float l_s[8][8], l_m[8][8];
  const int blk = blockIdx.x, tid = threadIdx.x, lane = tid & 63, w = tid >> 6;
  const int idx = blk >> 3, b = idx >> 3, j8 = (blk & 7) * 8 + (idx & 7);
  float a[8];
#pragma unroll
  for (int j = 0; j < 8; ++j) a[j] = 0.f;
#pragma unroll
  for (int i = 0; i < 4; ++i) {
    const int row = tid + 512 * i;
    if (row < FF) {
      const v4u t = *(const v4u*)(p.w2 + (size_t)row * DM + j8 * 8);
      const float hv = p.ws[WS_H + b * FF + row];
#pragma unroll
      for (int j = 0; j < 4; ++j) { a[2 * j] = fmaf(hv, bflo(t[j]), a[2 * j]); a[2 * j + 1] = fmaf(hv, bfhi(t[j]), a[2 * j + 1]); }
    }
  }
#pragma unroll
  for (int j = 0; j < 4; ++j) a[j] = tr32(a[j], a[j + 4]);
#pragma unroll
  for (int j = 0; j < 2; ++j) a[j] = tr16(a[j], a[j + 2]);
  TR_STEP(1, XOR8, 8)
  float z = a[0]; z += dpp<XOR7>(z); z += dpp<XOR2>(z); z += dpp<XOR1>(z);
  if ((lane & 7) == 0) l_m[w][lane >> 3] = z;
  const int pr = tid >> 3, col = tid & 7, slot = b * 64 + pr;
  const float m0 = p.ws[WS_ML + 2 * slot], l0 = p.ws[WS_ML + 2 * slot + 1];
  const float m1 = p.ws[WS_ML + 2 * (slot ^ 1)], l1 = p.ws[WS_ML + 2 * (slot ^ 1) + 1];
  const float M = fmaxf(m0, m1), f0 = __builtin_amdgcn_exp2f(m0 - M), f1 = __builtin_amdgcn_exp2f(m1 - M);
  float t = p.ws[WS_T + (size_t)slot * 512 + j8 * 8 + col] * (f0 / (f0 * l0 + f1 * l1));
  t += dpp<XOR8>(t); t = tr16(t, t); t = tr32(t, t);
  if (lane < 8) l_s[w][lane] = t;
  __syncthreads();
  if (tid < 8) {
    float s = 0.f, m = 0.f;
#pragma unroll
    for (int ww = 0; ww < 8; ++ww) { s += l_s[ww][tid]; m += l_m[ww][tid]; }
    const int cc = j8 * 8 + tid;
    s = rbf(s);
    m = rbf(rbf(m) + bflo(p.b2[cc]));
    const float g = rbf(m / (1.0f + __expf(-m)));
    const float o = bflo(p.x[b * DM + cc]) + rbf(s * g);
    p.out[b * DM + cc] = __builtin_bit_cast(unsigned short, (__bf16)o);
  }
}

extern "C" void kernel_launch(void* const* d_in, const int* in_sizes, int n_in, void* d_out, int out_size, void* d_ws, size_t ws_size,
                              hipStream_t stream) {
  Params p;
  p.x = (const unsigned short*)d_in[0]; p.k = (const unsigned short*)d_in[1]; p.v = (const unsigned short*)d_in[2];
  p.wq = (const unsigned short*)d_in[3]; p.wo = (const unsigned short*)d_in[4];
  p.w1 = (const unsigned short*)d_in[6]; p.b1 = (const unsigned short*)d_in[7]; p.w2 = (const unsigned short*)d_in[8]; p.b2 = (const unsigned short*)d_in[9];
  p.out = (unsigned short*)d_out; p.ws = (float*)d_ws;
  k_attn<<<256, 512, 0, stream>>>(p);
  k_final<<<256, 512, 0, stream>>>(p);
}
```

```cpp
#include <hip/hip_runtime.h>
#include <hip/hip_bf16.h>
#include <stdint.h>
#include <utility>

#define P_D 4
#define P_RA 2
#define P_RB 3
#define AUX_KV 2
#define AUX_WQ 2
#define AUX_WO 2
#define AUX_W12 0
#define WS_AT_END 0

typedef unsigned v4u __attribute__((ext_vector_type(4)));
typedef unsigned v2u __attribute__((ext_vector_type(2)));
typedef float f32x4 __attribute__((ext_vector_type(4)));
typedef float f32x2 __attribute__((ext_vector_type(2)));
typedef __bf16 bf16x2 __attribute__((ext_vector_type(2)));

constexpr int NB = 4, DM = 512, NH = 32, SK = 4096, DH = 128, FF = 1792;
constexpr int AUX_SC1 = 16;
constexpr unsigned SENT = 0xFFFFFFFFu;
constexpr unsigned SPIN_CAP = 1u << 18;

template <class F, int... I> __device__ __forceinline__ void static_for_impl(F&& f, std::integer_sequence<int, I...>) { (f(std::integral_constant<int, I>{}), ...); }
template <int N, class F> __device__ __forceinline__ void static_for(F&& f) { static_for_impl(f, std::make_integer_sequence<int, N>{}); }

__device__ __forceinline__ float dot2(unsigned a, unsigned b, float acc) {
  return __builtin_amdgcn_fdot2_f32_bf16(__builtin_bit_cast(bf16x2, a), __builtin_bit_cast(bf16x2, b), acc, false);
}
__device__ __forceinline__ float bflo(unsigned u) { return __uint_as_float(u << 16); }
__device__ __forceinline__ float bfhi(unsigned u) { return __uint_as_float(u & 0xffff0000u); }
__device__ __forceinline__ unsigned pk_bf16(float lo, float hi) { bf16x2 r; r.x = (__bf16)lo; r.y = (__bf16)hi; return __builtin_bit_cast(unsigned, r); }
__device__ __forceinline__ unsigned bf_bits(float x) { return (unsigned)__builtin_bit_cast(unsigned short, (__bf16)x); }
__device__ __forceinline__ float rbf(float x) { return bflo(bf_bits(x)); }
template <int CTRL> __device__ __forceinline__ float dpp(float x) { return __int_as_float(__builtin_amdgcn_mov_dpp(__float_as_int(x), CTRL, 0xf, 0xf, true)); }
constexpr int XOR1 = 0xB1, XOR2 = 0x4E, XOR7 = 0x141, XOR8 = 0x128, BCAST = 0x150;
__device__ __forceinline__ float tr32(float a, float b) {
  auto s = __builtin_amdgcn_permlane32_swap(__float_as_uint(a), __float_as_uint(b), false, false);
  return __uint_as_float(s[0]) + __uint_as_float(s[1]);
}
__device__ __forceinline__ float tr16(float a, float b) {
  auto s = __builtin_amdgcn_permlane16_swap(__float_as_uint(a), __float_as_uint(b), false, false);
  return __uint_as_float(s[0]) + __uint_as_float(s[1]);
}
template <int CTRL> __device__ __forceinline__ float trd(float lo, float hi, bool up) {
  const float t1 = lo + dpp<CTRL>(lo), t2 = hi + dpp<CTRL>(hi);
  return up ? t2 : t1;
}
__device__ __forceinline__ float wave_sum(float x) {
  x += dpp<XOR1>(x); x += dpp<XOR2>(x); x += dpp<XOR7>(x); x += dpp<XOR8>(x);
  x = tr16(x, x); x = tr32(x, x);
  return x;
}
__device__ __forceinline__ float wave_max(float x) {
  x = fmaxf(x, dpp<XOR1>(x)); x = fmaxf(x, dpp<XOR2>(x)); x = fmaxf(x, dpp<XOR7>(x)); x = fmaxf(x, dpp<XOR8>(x));
  auto s = __builtin_amdgcn_permlane16_swap(__float_as_uint(x), __float_as_uint(x), false, false);
  x = fmaxf(__uint_as_float(s[0]), __uint_as_float(s[1]));
  auto t = __builtin_amdgcn_permlane32_swap(__float_as_uint(x), __float_as_uint(x), false, false);
  return fmaxf(__uint_as_float(t[0]), __uint_as_float(t[1]));
}
__device__ __forceinline__ __amdgpu_buffer_rsrc_t mk_rsrc(const void* p, unsigned bytes) {
  return __builtin_amdgcn_make_buffer_rsrc(const_cast<void*>(p), 0, bytes, 0x00020000);
}
__device__ __forceinline__ void ln_stats(const float (&v)[8], float& mean, float& rs) {
  float s = 0.f;
#pragma unroll
  for (int j = 0; j < 8; ++j) s += v[j];
  mean = wave_sum(s) * (1.0f / DM);
  float q = 0.f;
#pragma unroll
  for (int j = 0; j < 8; ++j) { const float d = v[j] - mean; q = fmaf(d, d, q); }
  rs = __builtin_amdgcn_rsqf(wave_sum(q) * (1.0f / DM) + 1e-5f);
}
#define TOUCH(x) asm volatile("" :: "v"(x))
#define PIN(x) asm volatile("" : "+v"(x))
#define FENCE() __builtin_amdgcn_sched_barrier(0)
#define LDS_BARRIER() do { asm volatile("s_waitcnt lgkmcnt(0)\n\ts_barrier" ::: "memory"); } while (0)

constexpr unsigned WS_E = 0;
constexpr unsigned WS_MSG = 4096;
constexpr unsigned WS_HG = 16384;
constexpr unsigned WS_TB = 131072;
constexpr unsigned WS_BYTES = WS_TB + 4 * 64 * 64 * 8 * 4;

struct Params {
  const unsigned short* x; const unsigned short* k; const unsigned short* v; const unsigned short* wq; const unsigned short* wo;
  const unsigned short* w1; const unsigned short* b1; const unsigned short* w2; const unsigned short* b2;
  unsigned short* out; unsigned char* ws;
};

constexpr int D = P_D, RA = P_RA, RB = P_RB;
constexpr int NQ = 15 * RA + D;
constexpr int N_W1 = NQ + 4;
constexpr int N_WO = NQ + 6;
constexpr int N_W2 = 92;
constexpr int N_G = 100;
static_assert(N_WO + 15 * RB < 124, "wo loads land before the end");

__global__ void __launch_bounds__(512, 2) k_fused(Params p) {
  __shared__ __attribute__((aligned(16))) float lds[8 * 128 + 4 * 512 + 8 * 32 + 8 * 128 + 8 * 128 + 16 + 64];
  float* l_q = lds;
  float* l_y = l_q + 8 * 128;
  float* l_h = l_y + 4 * 512;
  float* l_o = l_h + 8 * 32;
  float* l_p = l_o + 8 * 128;
  float* l_ml = l_p + 8 * 128;
  float* l_m = l_ml + 16;
  const int blk = blockIdx.x, tid = threadIdx.x, lane = tid & 63, w = __builtin_amdgcn_readfirstlane(tid >> 6);
  const int idx = blk >> 3, h = (blk & 7) * 4 + (idx & 3), b = idx >> 3, sp = (idx >> 2) & 1;
  const int prod = 2 * h + sp;
  const int cons = (blk & 7) * 8 + (idx & 7);
  const int r = lane >> 4, c = lane & 15, rg = lane >> 3, cg = lane & 7;
  const bool has_mlp = idx < 28, row3 = w < 4;
  const int u = (blk & 7) * 28 + idx;

  const __amdgpu_buffer_rsrc_t rws = mk_rsrc(p.ws, WS_BYTES);
  const __amdgpu_buffer_rsrc_t rx = mk_rsrc(p.x, NB * DM * 2);
  const __amdgpu_buffer_rsrc_t rq = mk_rsrc(p.wq + (size_t)w * 64 * (NH * DH) + h * DH, 64 * NH * DH * 2);
  const size_t kvoff = ((size_t)(b * NH + h) * SK + sp * 2048 + w * 4) * DH;
  const __amdgpu_buffer_rsrc_t rk = mk_rsrc(p.k + kvoff, 524288 - w * 1024), rv = mk_rsrc(p.v + kvoff, 524288 - w * 1024);
  const int l16 = lane * 16;
  const int vq = r * 16 * 8192 + c * 16;
  const int vo = rg * 16 * 1024 + cg * 16;
  const unsigned tb_off = (lane & 24) ? 0x80000000u : WS_TB + (unsigned)(((b * 64 + 8 * w + cg) * 64 + prod) * 32 + (lane >> 5) * 16);

  v4u S[128], Wq[16], Wo[16], W2[4];
  const v2u ep = __builtin_amdgcn_raw_buffer_load_b64(rws, 0, WS_E, AUX_SC1);
  const v4u xa = __builtin_amdgcn_raw_buffer_load_b128(rx, l16, b * DM * 2, 0);
  const v4u xr0 = __builtin_amdgcn_raw_buffer_load_b128(rx, r * 32, b * DM * 2 + w * 128, 0);
  const v4u xr1 = __builtin_amdgcn_raw_buffer_load_b128(rx, r * 32 + 16, b * DM * 2 + w * 128, 0);
  const v4u xm = __builtin_amdgcn_raw_buffer_load_b128(rx, l16, (w & 3) * DM * 2, 0);
  FENCE();

  unsigned xn[8], q[4], tagA = 0, tagB = 0;
  float qa[8], sc[4], ka[16], acc[8], coef = 0.f;
  v4u w1v; v2u hg[4]; v4u msg; unsigned b1v = 0, b2v = 0, xv = 0;
  constexpr float C2 = 0.088388347648318440f * 1.4426950408889634f;

  auto kdot = [&](auto I) { constexpr int i = I; const v4u t = S[i];
    float d = dot2(t[0], q[0], 0.f); d = dot2(t[1], q[1], d); d = dot2(t[2], q[2], d); d = dot2(t[3], q[3], d); PIN(d); ka[i & 15] = d;
    if constexpr ((i & 15) == 15) {
#pragma unroll
      for (int j = 0; j < 8; ++j) ka[j] = trd<XOR8>(ka[j], ka[j + 8], lane & 8);
#pragma unroll
      for (int j = 0; j < 4; ++j) ka[j] = trd<XOR7>(ka[j], ka[j + 4], lane & 4);
#pragma unroll
      for (int j = 0; j < 2; ++j) ka[j] = trd<XOR2>(ka[j], ka[j + 2], lane & 2);
      sc[i >> 4] = trd<XOR1>(ka[0], ka[1], lane & 1); PIN(sc[i >> 4]);
    } };
  auto pv = [&](auto I) { constexpr int i = I; const v4u t = S[64 + i]; const float pb = dpp<BCAST + (i & 15)>(sc[i >> 4]);
#pragma unroll
    for (int j = 0; j < 4; ++j) { acc[2 * j] = fmaf(pb, bflo(t[j]), acc[2 * j]); acc[2 * j + 1] = fmaf(pb, bfhi(t[j]), acc[2 * j + 1]); }
    asm volatile("" : "+v"(acc[0]), "+v"(acc[1]), "+v"(acc[2]), "+v"(acc[3]), "+v"(acc[4]), "+v"(acc[5]), "+v"(acc[6]), "+v"(acc[7])); };

  static_for<128 + D>([&](auto N) {
    constexpr int n = N;
    if constexpr (n >= D) TOUCH(S[n - D]);
    FENCE();
    if constexpr (n % RA == 0 && n / RA < 16) Wq[n / RA] = __builtin_amdgcn_raw_buffer_load_b128(rq, vq, (n / RA) * 8192, AUX_WQ);
    if constexpr (n >= N_WO && (n - N_WO) % RB == 0 && (n - N_WO) / RB < 16)
      Wo[(n - N_WO) / RB] = __builtin_amdgcn_raw_buffer_load_b128(mk_rsrc(p.wo + (size_t)h * DH * DM + w * 64, DH * DM * 2), vo, ((n - N_WO) / RB) * 1024, AUX_WO);
    if constexpr (n == 1) __builtin_amdgcn_raw_buffer_store_b128(v4u{SENT, SENT, SENT, SENT}, rws, tb_off, 0, AUX_SC1);
    if constexpr (n == N_W1 - D - 1) {
      w1v = __builtin_amdgcn_raw_buffer_load_b128(mk_rsrc(p.w1, has_mlp ? DM * FF * 2 : 0), tid * (FF * 2), u * 16, AUX_W12);
      b1v = __builtin_amdgcn_raw_buffer_load_b16(mk_rsrc(p.b1, has_mlp ? FF * 2 : 0), (lane & 7) * 2, u * 16, 0);
    }
    if constexpr (n >= N_W2 && n < N_W2 + 4) W2[n - N_W2] = __builtin_amdgcn_raw_buffer_load_b128(mk_rsrc(p.w2, FF * DM * 2), (n == N_W2 + 3 && !row3) ? 0x80000000u : (unsigned)(tid * (DM * 2)), (n - N_W2) * 512 * (DM * 2) + cons * 16, AUX_W12);
    if constexpr (n == N_G) {
#pragma unroll
      for (int i = 0; i < 4; ++i) hg[i] = __builtin_amdgcn_raw_buffer_load_b64(rws, tid * 8, WS_HG + (b * FF + i * 512) * 8, AUX_SC1);
      msg = __builtin_amdgcn_raw_buffer_load_b128(rws, l16, WS_MSG + ((b + w) & 3) * 1024, AUX_SC1);
      b2v = __builtin_amdgcn_raw_buffer_load_b16(mk_rsrc(p.b2, DM * 2), rg * 2, cons * 16, 0);
      xv = __builtin_amdgcn_raw_buffer_load_b16(rx, rg * 2, b * DM * 2 + cons * 16, 0);
    }
    if constexpr (n < 64) S[n] = __builtin_amdgcn_raw_buffer_load_b128(rk, l16, n * 8192, AUX_KV);
    else if constexpr (n < 128) S[n] = __builtin_amdgcn_raw_buffer_load_b128(rv, l16, (n - 64) * 8192, AUX_KV);
    FENCE();
    if constexpr (n == D - 1) {
      float v[8], mean, rs;
#pragma unroll
      for (int j = 0; j < 4; ++j) { v[2 * j] = bflo(xa[j]); v[2 * j + 1] = bfhi(xa[j]); }
      ln_stats(v, mean, rs);
#pragma unroll
      for (int j = 0; j < 8; ++j) { const unsigned t = j < 4 ? xr0[j & 3] : xr1[j & 3]; xn[j] = pk_bf16((bflo(t) - mean) * rs, (bfhi(t) - mean) * rs); }
#pragma unroll
      for (int j = 0; j < 8; ++j) { qa[j] = 0.f; acc[j] = 0.f; }
      tagA = __builtin_amdgcn_readfirstlane(ep[0]) + 1u; tagB = __builtin_amdgcn_readfirstlane(ep[1]) + 1u;
    }
    if constexpr (n == D + 1) {
      float v[8], mean, rs;
#pragma unroll
      for (int j = 0; j < 4; ++j) { v[2 * j] = bflo(xm[j]); v[2 * j + 1] = bfhi(xm[j]); }
      ln_stats(v, mean, rs);
#pragma unroll
      for (int j = 0; j < 8; ++j) v[j] = rbf((v[j] - mean) * rs);
      ln_stats(v, mean, rs);
#pragma unroll
      for (int j = 0; j < 8; ++j) v[j] = rbf((v[j] - mean) * rs);
      if (w < 4) {
        *(f32x4*)(l_y + w * 512 + lane * 8) = f32x4{v[0], v[1], v[2], v[3]};
        *(f32x4*)(l_y + w * 512 + lane * 8 + 4) = f32x4{v[4], v[5], v[6], v[7]};
      }
    }
    if constexpr (n >= D && (n - D) % RA == 0 && ((n - D) / RA) % 2 == 1 && (n - D) / RA < 16) {
      constexpr int j = (n - D) / RA;
#pragma unroll
      for (int k = 0; k < 4; ++k) {
        const unsigned e = Wq[j - 1][k], o = Wq[j][k];
        qa[2 * k] = dot2(__builtin_amdgcn_perm(o, e, 0x05040100u), xn[j >> 1], qa[2 * k]);
        qa[2 * k + 1] = dot2(__builtin_amdgcn_perm(o, e, 0x07060302u), xn[j >> 1], qa[2 * k + 1]);
      }
    }
    if constexpr (n == NQ) {
      float t4[4];
#pragma unroll
      for (int j = 0; j < 4; ++j) t4[j] = tr32(qa[j], qa[j + 4]);
      *(f32x2*)(l_q + w * 128 + 8 * c + 2 * r) = f32x2{tr16(t4[0], t4[2]), tr16(t4[1], t4[3])};
      LDS_BARRIER();
      f32x4 s0 = *(const f32x4*)(l_q + (2 * r) * 128 + 8 * c) + *(const f32x4*)(l_q + (2 * r + 1) * 128 + 8 * c);
      f32x4 s1 = *(const f32x4*)(l_q + (2 * r) * 128 + 8 * c + 4) + *(const f32x4*)(l_q + (2 * r + 1) * 128 + 8 * c + 4);
#pragma unroll
      for (int j = 0; j < 4; ++j) { s0[j] = tr16(s0[j], s0[j]); s0[j] = tr32(s0[j], s0[j]); s1[j] = tr16(s1[j], s1[j]); s1[j] = tr32(s1[j], s1[j]); }
      q[0] = pk_bf16(s0[0], s0[1]); q[1] = pk_bf16(s0[2], s0[3]); q[2] = pk_bf16(s1[0], s1[1]); q[3] = pk_bf16(s1[2], s1[3]);
    }
    if constexpr (n > NQ && n - D < 64) {
      constexpr int hi = (n - NQ) * 16 < n - D + 1 ? (n - NQ) * 16 : n - D + 1;
      constexpr int lo = n - 1 == NQ ? 0 : ((n - 1 - NQ) * 16 < n - D ? (n - 1 - NQ) * 16 : n - D);
      static_for<hi - lo>([&](auto J) { kdot(std::integral_constant<int, lo + J>{}); });
    }
    if constexpr (n == N_W1) {
      float a[32];
#pragma unroll
      for (int bb = 0; bb < 4; ++bb) {
        const float y = l_y[bb * 512 + tid];
#pragma unroll
        for (int j = 0; j < 4; ++j) { a[bb * 8 + 2 * j] = y * bflo(w1v[j]); a[bb * 8 + 2 * j + 1] = y * bfhi(w1v[j]); }
      }
#pragma unroll
      for (int j = 0; j < 16; ++j) a[j] = tr32(a[j], a[j + 16]);
#pragma unroll
      for (int j = 0; j < 8; ++j) a[j] = tr16(a[j], a[j + 8]);
#pragma unroll
      for (int j = 0; j < 4; ++j) a[j] = trd<XOR8>(a[j], a[j + 4], lane & 8);
#pragma unroll
      for (int j = 0; j < 2; ++j) a[j] = trd<XOR7>(a[j], a[j + 2], lane & 4);
      float z = trd<XOR2>(a[0], a[1], lane & 2);
      z += dpp<XOR1>(z);
      if ((lane & 1) == 0) l_h[w * 32 + (lane >> 1)] = z;
    }
    if constexpr (n == 63 + D) {
      const float mw = wave_max(fmaxf(fmaxf(sc[0], sc[1]), fmaxf(sc[2], sc[3]))) * C2;
#pragma unroll
      for (int g = 0; g < 4; ++g) sc[g] = __builtin_amdgcn_exp2f(fmaf(sc[g], C2, -mw));
      const float lw = wave_sum((sc[0] + sc[1]) + (sc[2] + sc[3]));
      if (lane == 0) *(f32x2*)(l_ml + 2 * w) = f32x2{mw, lw};
      LDS_BARRIER();
      const f32x2 ml = *(const f32x2*)(l_ml + 2 * (lane & 7));
      float M = ml[0];
      M = fmaxf(M, dpp<XOR1>(M)); M = fmaxf(M, dpp<XOR2>(M)); M = fmaxf(M, dpp<XOR7>(M));
      float L = ml[1] * __builtin_amdgcn_exp2f(ml[0] - M);
      L += dpp<XOR1>(L); L += dpp<XOR2>(L); L += dpp<XOR7>(L);
      const float f = __builtin_amdgcn_exp2f(mw - M) * __builtin_amdgcn_rcpf(L);
#pragma unroll
      for (int g = 0; g < 4; ++g) sc[g] *= f;
      const float lse = M + __builtin_amdgcn_logf(L);
      __builtin_amdgcn_raw_buffer_store_b128(v4u{__float_as_uint(lse), tagA, 0u, tagB}, rws, tid == 0 ? 0u : 0x80000000u, WS_MSG + (b * 64 + prod) * 16, AUX_SC1);
      float z = 0.f;
#pragma unroll
      for (int ww = 0; ww < 8; ++ww) z += l_h[ww * 32 + (lane & 31)];
      z = rbf(rbf(z) + bflo(b1v));
      const float e2 = __builtin_amdgcn_exp2f((-2.0f * 0.7978845608028654f * 1.4426950408889634f) * (z + 0.044715f * z * z * z));
      const float g = z * __builtin_amdgcn_rcpf(1.0f + e2);
      __builtin_amdgcn_raw_buffer_store_b64(v2u{bf_bits(g) | (tagB << 16), tagA}, rws,
          (has_mlp && w == 1 && lane < 32) ? (unsigned)(((lane >> 3) * FF + (lane & 7)) * 8) : 0x80000000u, WS_HG + u * 64, AUX_SC1);
    }
    if constexpr (n - D >= 64) pv(std::integral_constant<int, n - D - 64>{});
    if constexpr (n == N_G + D) {
      auto stale = [&]() {
        unsigned bad = (msg[1] ^ tagA) | (msg[3] ^ tagB);
#pragma unroll
        for (int i = 0; i < 4; ++i) { const unsigned d = (hg[i][1] ^ tagA) | ((hg[i][0] ^ (tagB << 16)) >> 16); bad |= (i == 3 && !row3) ? 0u : d; }
        return __any(bad != 0u); };
      TOUCH(msg);
      if (__builtin_expect(stale(), 0)) {
#pragma unroll 1
        for (unsigned spin = 0; spin < SPIN_CAP; ++spin) {
          __builtin_amdgcn_s_sleep(8);
          asm volatile("" ::: "memory");
#pragma unroll
          for (int i = 0; i < 4; ++i) hg[i] = __builtin_amdgcn_raw_buffer_load_b64(rws, tid * 8, WS_HG + (b * FF + i * 512) * 8, AUX_SC1);
          msg = __builtin_amdgcn_raw_buffer_load_b128(rws, l16, WS_MSG + ((b + w) & 3) * 1024, AUX_SC1);
          TOUCH(hg[0]); TOUCH(hg[1]); TOUCH(hg[2]); TOUCH(hg[3]); TOUCH(msg);
          if (!stale()) break;
        }
      }
      const float lse = __uint_as_float(msg[0]);
      coef = __builtin_amdgcn_rcpf(1.0f + __builtin_amdgcn_exp2f(dpp<XOR1>(lse) - lse));
      PIN(coef);
      float a[8];
#pragma unroll
      for (int j = 0; j < 8; ++j) a[j] = 0.f;
#pragma unroll
      for (int i = 0; i < 4; ++i) {
        const float hv = (i == 3 && !row3) ? 0.f : bflo(hg[i][0]);
#pragma unroll
        for (int j = 0; j < 4; ++j) { a[2 * j] = fmaf(hv, bflo(W2[i][j]), a[2 * j]); a[2 * j + 1] = fmaf(hv, bfhi(W2[i][j]), a[2 * j + 1]); }
      }
#pragma unroll
      for (int j = 0; j < 4; ++j) a[j] = tr32(a[j], a[j + 4]);
#pragma unroll
      for (int j = 0; j < 2; ++j) a[j] = tr16(a[j], a[j + 2]);
      float z = trd<XOR8>(a[0], a[1], lane & 8);
      z += dpp<XOR7>(z); z += dpp<XOR2>(z); z += dpp<XOR1>(z);
      if (cg == 0) l_m[w * 8 + rg] = z;
    }
    FENCE();
  });

  {
    float t4[4];
#pragma unroll
    for (int j = 0; j < 4; ++j) t4[j] = tr32(acc[j], acc[j + 4]);
    *(f32x2*)(l_o + w * 128 + 8 * c + 2 * r) = f32x2{tr16(t4[0], t4[2]), tr16(t4[1], t4[3])};
  }
  LDS_BARRIER();
  {
    f32x2 o2 = *(const f32x2*)(l_o + 2 * lane);
#pragma unroll
    for (int ww = 1; ww < 8; ++ww) o2 += *(const f32x2*)(l_o + ww * 128 + 2 * lane);
    *(f32x2*)(l_p + w * 128 + 2 * lane) = o2;
    asm volatile("s_waitcnt lgkmcnt(0)" ::: "memory");
    float a[8];
#pragma unroll
    for (int j = 0; j < 8; ++j) a[j] = 0.f;
#pragma unroll
    for (int i4 = 0; i4 < 4; ++i4) {
      const f32x4 ov = *(const f32x4*)(l_p + w * 128 + 16 * rg + 4 * i4);
#pragma unroll
      for (int ii = 0; ii < 4; ++ii) {
        const v4u t = Wo[4 * i4 + ii];
#pragma unroll
        for (int j = 0; j < 4; ++j) { a[2 * j] = fmaf(ov[ii], bflo(t[j]), a[2 * j]); a[2 * j + 1] = fmaf(ov[ii], bfhi(t[j]), a[2 * j + 1]); }
      }
    }
    v4u tv;
#pragma unroll
    for (int j = 0; j < 4; ++j) { float t = tr32(a[j], a[j + 4]); t = tr16(t, t); t += dpp<XOR8>(t); tv[j] = min(__float_as_uint(t), SENT - 1u); }
    __builtin_amdgcn_raw_buffer_store_b128(tv, rws, tb_off, 0, AUX_SC1);
  }
  if (w != 0) return;
  if (blk == 0) __builtin_amdgcn_raw_buffer_store_b64(v2u{tagA, tagB}, rws, lane == 0 ? 0u : 0x80000000u, WS_E, AUX_SC1);
  float gate;
  {
    float m = l_m[cg * 8 + rg];
    m += dpp<XOR1>(m); m += dpp<XOR2>(m); m += dpp<XOR7>(m);
    m = rbf(rbf(m) + bflo(b2v));
    gate = rbf(m * __builtin_amdgcn_rcpf(1.0f + __builtin_amdgcn_exp2f(-1.4426950408889634f * m)));
    PIN(gate);
  }
  v4u t0, t1;
#pragma unroll 1
  for (unsigned spin = 0; spin < SPIN_CAP; ++spin) {
    t0 = __builtin_amdgcn_raw_buffer_load_b128(rws, lane * 32, WS_TB + (b * 64 + cons) * 2048, AUX_SC1);
    t1 = __builtin_amdgcn_raw_buffer_load_b128(rws, lane * 32 + 16, WS_TB + (b * 64 + cons) * 2048, AUX_SC1);
    const unsigned mx = max(max(max(t0[0], t0[1]), max(t0[2], t0[3])), max(max(t1[0], t1[1]), max(t1[2], t1[3])));
    if (__all(mx != SENT)) break;
    __builtin_amdgcn_s_sleep(1);
    asm volatile("" ::: "memory");
  }
  float a[4];
#pragma unroll
  for (int j = 0; j < 4; ++j) a[j] = tr32(__uint_as_float(t0[j]) * coef, __uint_as_float(t1[j]) * coef);
  a[0] = tr16(a[0], a[2]); a[1] = tr16(a[1], a[3]);
  float s = trd<XOR8>(a[0], a[1], lane & 8);
  s += dpp<XOR7>(s); s += dpp<XOR2>(s); s += dpp<XOR1>(s);
  const float o = bflo(xv) + rbf(rbf(s) * gate);
  __builtin_amdgcn_raw_buffer_store_b16((unsigned short)bf_bits(o), mk_rsrc(p.out, NB * DM * 2), cg == 0 ? (unsigned)(rg * 2) : 0x80000000u, b * DM * 2 + cons * 16, 0);
}

extern "C" void kernel_launch(void* const* d_in, const int* in_sizes, int n_in, void* d_out, int out_size, void* d_ws, size_t ws_size,
                              hipStream_t stream) {
  Params p;
  p.x = (const unsigned short*)d_in[0]; p.k = (const unsigned short*)d_in[1]; p.v = (const unsigned short*)d_in[2];
  p.wq = (const unsigned short*)d_in[3]; p.wo = (const unsigned short*)d_in[4];
  p.w1 = (const unsigned short*)d_in[6]; p.b1 = (const unsigned short*)d_in[7]; p.w2 = (const unsigned short*)d_in[8]; p.b2 = (const unsigned short*)d_in[9];
  p.out = (unsigned short*)d_out;
  p.ws = (unsigned char*)d_ws + (WS_AT_END ? ((ws_size - (1u << 20)) & ~(size_t)4095) : 0);
  k_fused<<<256, 512, 0, stream>>>(p);
}
```

```cpp
#include <hip/hip_runtime.h>
#include <hip/hip_bf16.h>
#include <stdint.h>
#include <utility>

#define P_D 4
#define P_RA 2
#define P_RB 3
#define AUX_KV 2
#define AUX_WQ 2
#define AUX_WO 2
#define AUX_W12 0
#define DIAG 0
#define DIAG_BLK 0
#define WS_AT_END 0

typedef unsigned v4u __attribute__((ext_vector_type(4)));
typedef unsigned v2u __attribute__((ext_vector_type(2)));
typedef float f32x4 __attribute__((ext_vector_type(4)));
typedef float f32x2 __attribute__((ext_vector_type(2)));
typedef __bf16 bf16x2 __attribute__((ext_vector_type(2)));

constexpr int NB = 4, DM = 512, NH = 32, SK = 4096, DH = 128, FF = 1792;
constexpr int AUX_SC1 = 16;
constexpr unsigned SENT = 0xFFFFFFFFu;
constexpr unsigned SPIN_CAP = 1u << 18;

template <class F, int... I> __device__ __forceinline__ void static_for_impl(F&& f, std::integer_sequence<int, I...>) { (f(std::integral_constant<int, I>{}), ...); }
template <int N, class F> __device__ __forceinline__ void static_for(F&& f) { static_for_impl(f, std::make_integer_sequence<int, N>{}); }

__device__ __forceinline__ float dot2(unsigned a, unsigned b, float acc) {
  return __builtin_amdgcn_fdot2_f32_bf16(__builtin_bit_cast(bf16x2, a), __builtin_bit_cast(bf16x2, b), acc, false);
}
__device__ __forceinline__ float bflo(unsigned u) { return __uint_as_float(u << 16); }
__device__ __forceinline__ float bfhi(unsigned u) { return __uint_as_float(u & 0xffff0000u); }
__device__ __forceinline__ unsigned pk_bf16(float lo, float hi) { bf16x2 r; r.x = (__bf16)lo; r.y = (__bf16)hi; return __builtin_bit_cast(unsigned, r); }
__device__ __forceinline__ unsigned bf_bits(float x) { return (unsigned)__builtin_bit_cast(unsigned short, (__bf16)x); }
__device__ __forceinline__ float rbf(float x) { return bflo(bf_bits(x)); }
template <int CTRL> __device__ __forceinline__ float dpp(float x) { return __int_as_float(__builtin_amdgcn_mov_dpp(__float_as_int(x), CTRL, 0xf, 0xf, true)); }
constexpr int XOR1 = 0xB1, XOR2 = 0x4E, XOR7 = 0x141, XOR8 = 0x128, BCAST = 0x150;
__device__ __forceinline__ float tr32(float a, float b) {
  auto s = __builtin_amdgcn_permlane32_swap(__float_as_uint(a), __float_as_uint(b), false, false);
  return __uint_as_float(s[0]) + __uint_as_float(s[1]);
}
__device__ __forceinline__ float tr16(float a, float b) {
  auto s = __builtin_amdgcn_permlane16_swap(__float_as_uint(a), __float_as_uint(b), false, false);
  return __uint_as_float(s[0]) + __uint_as_float(s[1]);
}
template <int CTRL> __device__ __forceinline__ float trd(float lo, float hi, bool up) {
  const float t1 = lo + dpp<CTRL>(lo), t2 = hi + dpp<CTRL>(hi);
  return up ? t2 : t1;
}
__device__ __forceinline__ float wave_sum(float x) {
  x += dpp<XOR1>(x); x += dpp<XOR2>(x); x += dpp<XOR7>(x); x += dpp<XOR8>(x);
  x = tr16(x, x); x = tr32(x, x);
  return x;
}
__device__ __forceinline__ float wave_max(float x) {
  x = fmaxf(x, dpp<XOR1>(x)); x = fmaxf(x, dpp<XOR2>(x)); x = fmaxf(x, dpp<XOR7>(x)); x = fmaxf(x, dpp<XOR8>(x));
  auto s = __builtin_amdgcn_permlane16_swap(__float_as_uint(x), __float_as_uint(x), false, false);
  x = fmaxf(__uint_as_float(s[0]), __uint_as_float(s[1]));
  auto t = __builtin_amdgcn_permlane32_swap(__float_as_uint(x), __float_as_uint(x), false, false);
  return fmaxf(__uint_as_float(t[0]), __uint_as_float(t[1]));
}
__device__ __forceinline__ __amdgpu_buffer_rsrc_t mk_rsrc(const void* p, unsigned bytes) {
  return __builtin_amdgcn_make_buffer_rsrc(const_cast<void*>(p), 0, bytes, 0x00020000);
}
__device__ __forceinline__ void ln_stats(const float (&v)[8], float& mean, float& rs) {
  float s = 0.f;
#pragma unroll
  for (int j = 0; j < 8; ++j) s += v[j];
  mean = wave_sum(s) * (1.0f / DM);
  float q = 0.f;
#pragma unroll
  for (int j = 0; j < 8; ++j) { const float d = v[j] - mean; q = fmaf(d, d, q); }
  rs = __builtin_amdgcn_rsqf(wave_sum(q) * (1.0f / DM) + 1e-5f);
}
#define TOUCH(x) asm volatile("" :: "v"(x))
#define PIN(x) asm volatile("" : "+v"(x))
#define MARK(k) do { if (DIAG == (k)) { FENCE(); tmark = __builtin_amdgcn_s_memrealtime(); FENCE(); } } while (0)
#define FENCE() __builtin_amdgcn_sched_barrier(0)
#define LDS_BARRIER() do { asm volatile("s_waitcnt lgkmcnt(0)\n\ts_barrier" ::: "memory"); } while (0)

constexpr unsigned WS_E = 0;
constexpr unsigned WS_MSG = 4096;
constexpr unsigned WS_HG = 16384;
constexpr unsigned WS_TB = 131072;
constexpr unsigned WS_BYTES = WS_TB + 4 * 64 * 64 * 8 * 4;

struct Params {
  const unsigned short* x; const unsigned short* k; const unsigned short* v; const unsigned short* wq; const unsigned short* wo;
  const unsigned short* w1; const unsigned short* b1; const unsigned short* w2; const unsigned short* b2;
  unsigned short* out; unsigned char* ws;
};

constexpr int D = P_D, RA = P_RA, RB = P_RB;
constexpr int NQ = 15 * RA + D;
constexpr int N_W1 = NQ + 4;
constexpr int N_WO = NQ + 6;
constexpr int N_W2 = 92;
constexpr int N_G = 100;
static_assert(N_WO + 15 * RB < 124, "wo loads land before the end");

__global__ void __launch_bounds__(512, 2) k_fused(Params p) {
  __shared__ __attribute__((aligned(16))) float lds[8 * 128 + 4 * 512 + 8 * 32 + 8 * 128 + 8 * 128 + 16 + 64];
  float* l_q = lds;
  float* l_y = l_q + 8 * 128;
  float* l_h = l_y + 4 * 512;
  float* l_o = l_h + 8 * 32;
  float* l_p = l_o + 8 * 128;
  float* l_ml = l_p + 8 * 128;
  float* l_m = l_ml + 16;
  unsigned long long t0s = 0, tmark = 0;
  if (DIAG) { t0s = __builtin_amdgcn_s_memrealtime(); tmark = t0s; }
  const int blk = blockIdx.x, tid = threadIdx.x, lane = tid & 63, w = __builtin_amdgcn_readfirstlane(tid >> 6);
  const int idx = blk >> 3, h = (blk & 7) * 4 + (idx & 3), b = idx >> 3, sp = (idx >> 2) & 1;
  const int prod = 2 * h + sp;
  const int cons = (blk & 7) * 8 + (idx & 7);
  const int r = lane >> 4, c = lane & 15, rg = lane >> 3, cg = lane & 7;
  const bool has_mlp = idx < 28, row3 = w < 4;
  const int u = (blk & 7) * 28 + idx;

  const __amdgpu_buffer_rsrc_t rws = mk_rsrc(p.ws, WS_BYTES);
  const __amdgpu_buffer_rsrc_t rx = mk_rsrc(p.x, NB * DM * 2);
  const __amdgpu_buffer_rsrc_t rq = mk_rsrc(p.wq + (size_t)w * 64 * (NH * DH) + h * DH, 64 * NH * DH * 2);
  const size_t kvoff = ((size_t)(b * NH + h) * SK + sp * 2048 + w * 4) * DH;
  const __amdgpu_buffer_rsrc_t rk = mk_rsrc(p.k + kvoff, 524288 - w * 1024), rv = mk_rsrc(p.v + kvoff, 524288 - w * 1024);
  const int l16 = lane * 16;
  const int vq = r * 16 * 8192 + c * 16;
  const int vo = rg * 16 * 1024 + cg * 16;
  const unsigned tb_off = (lane & 24) ? 0x80000000u : WS_TB + (unsigned)(((b * 64 + 8 * w + cg) * 64 + prod) * 32 + (lane >> 5) * 16);

  v4u S[128], Wq[16], Wo[16], W2[4];
  const v2u ep = __builtin_amdgcn_raw_buffer_load_b64(rws, 0, WS_E, AUX_SC1);
  const v4u xa = __builtin_amdgcn_raw_buffer_load_b128(rx, l16, b * DM * 2, 0);
  const v4u xr0 = __builtin_amdgcn_raw_buffer_load_b128(rx, r * 32, b * DM * 2 + w * 128, 0);
  const v4u xr1 = __builtin_amdgcn_raw_buffer_load_b128(rx, r * 32 + 16, b * DM * 2 + w * 128, 0);
  const v4u xm = __builtin_amdgcn_raw_buffer_load_b128(rx, l16, (w & 3) * DM * 2, 0);
  FENCE();

  unsigned xn[8], q[4], tagA = 0, tagB = 0;
  float qa[8], sc[4], ka[16], acc[8], coef = 0.f;
  v4u w1v; v2u hg[4]; v4u msg; unsigned b1v = 0, b2v = 0, xv = 0;
  constexpr float C2 = 0.088388347648318440f * 1.4426950408889634f;

  auto kdot = [&](auto I) { constexpr int i = I; const v4u t = S[i];
    float d = dot2(t[0], q[0], 0.f); d = dot2(t[1], q[1], d); d = dot2(t[2], q[2], d); d = dot2(t[3], q[3], d); PIN(d); ka[i & 15] = d;
    if constexpr ((i & 15) == 15) {
#pragma unroll
      for (int j = 0; j < 8; ++j) ka[j] = trd<XOR8>(ka[j], ka[j + 8], lane & 8);
#pragma unroll
      for (int j = 0; j < 4; ++j) ka[j] = trd<XOR7>(ka[j], ka[j + 4], lane & 4);
#pragma unroll
      for (int j = 0; j < 2; ++j) ka[j] = trd<XOR2>(ka[j], ka[j + 2], lane & 2);
      sc[i >> 4] = trd<XOR1>(ka[0], ka[1], lane & 1); PIN(sc[i >> 4]);
    } };
  auto pv = [&](auto I) { constexpr int i = I; const v4u t = S[64 + i]; const float pb = dpp<BCAST + (i & 15)>(sc[i >> 4]);
#pragma unroll
    for (int j = 0; j < 4; ++j) { acc[2 * j] = fmaf(pb, bflo(t[j]), acc[2 * j]); acc[2 * j + 1] = fmaf(pb, bfhi(t[j]), acc[2 * j + 1]); }
    asm volatile("" : "+v"(acc[0]), "+v"(acc[1]), "+v"(acc[2]), "+v"(acc[3]), "+v"(acc[4]), "+v"(acc[5]), "+v"(acc[6]), "+v"(acc[7])); };

  static_for<128 + D>([&](auto N) {
    constexpr int n = N;
    if constexpr (n >= D) TOUCH(S[n - D]);
    FENCE();
    if constexpr (n == D) MARK(1);
    if constexpr (n == NQ) MARK(2);
    if constexpr (n == 63 + D) MARK(3);
    if constexpr (n == 127 + D) MARK(4);
    if constexpr (n == N_G + D) MARK(7);
    if constexpr (n % RA == 0 && n / RA < 16) Wq[n / RA] = __builtin_amdgcn_raw_buffer_load_b128(rq, vq, (n / RA) * 8192, AUX_WQ);
    if constexpr (n >= N_WO && (n - N_WO) % RB == 0 && (n - N_WO) / RB < 16)
      Wo[(n - N_WO) / RB] = __builtin_amdgcn_raw_buffer_load_b128(mk_rsrc(p.wo + (size_t)h * DH * DM + w * 64, DH * DM * 2), vo, ((n - N_WO) / RB) * 1024, AUX_WO);
    if constexpr (n == 1) __builtin_amdgcn_raw_buffer_store_b128(v4u{SENT, SENT, SENT, SENT}, rws, tb_off, 0, AUX_SC1);
    if constexpr (n == N_W1 - D - 1) {
      w1v = __builtin_amdgcn_raw_buffer_load_b128(mk_rsrc(p.w1, has_mlp ? DM * FF * 2 : 0), tid * (FF * 2), u * 16, AUX_W12);
      b1v = __builtin_amdgcn_raw_buffer_load_b16(mk_rsrc(p.b1, has_mlp ? FF * 2 : 0), (lane & 7) * 2, u * 16, 0);
    }
    if constexpr (n >= N_W2 && n < N_W2 + 4) W2[n - N_W2] = __builtin_amdgcn_raw_buffer_load_b128(mk_rsrc(p.w2, FF * DM * 2), (n == N_W2 + 3 && !row3) ? 0x80000000u : (unsigned)(tid * (DM * 2)), (n - N_W2) * 512 * (DM * 2) + cons * 16, AUX_W12);
    if constexpr (n == N_G) {
#pragma unroll
      for (int i = 0; i < 4; ++i) hg[i] = __builtin_amdgcn_raw_buffer_load_b64(rws, tid * 8, WS_HG + (b * FF + i * 512) * 8, AUX_SC1);
      msg = __builtin_amdgcn_raw_buffer_load_b128(rws, l16, WS_MSG + ((b + w) & 3) * 1024, AUX_SC1);
      b2v = __builtin_amdgcn_raw_buffer_load_b16(mk_rsrc(p.b2, DM * 2), rg * 2, cons * 16, 0);
      xv = __builtin_amdgcn_raw_buffer_load_b16(rx, rg * 2, b * DM * 2 + cons * 16, 0);
    }
    if constexpr (n < 64) S[n] = __builtin_amdgcn_raw_buffer_load_b128(rk, l16, n * 8192, AUX_KV);
    else if constexpr (n < 128) S[n] = __builtin_amdgcn_raw_buffer_load_b128(rv, l16, (n - 64) * 8192, AUX_KV);
    FENCE();
    if constexpr (n == D - 1) {
      float v[8], mean, rs;
#pragma unroll
      for (int j = 0; j < 4; ++j) { v[2 * j] = bflo(xa[j]); v[2 * j + 1] = bfhi(xa[j]); }
      ln_stats(v, mean, rs);
#pragma unroll
      for (int j = 0; j < 8; ++j) { const unsigned t = j < 4 ? xr0[j & 3] : xr1[j & 3]; xn[j] = pk_bf16((bflo(t) - mean) * rs, (bfhi(t) - mean) * rs); }
#pragma unroll
      for (int j = 0; j < 8; ++j) { qa[j] = 0.f; acc[j] = 0.f; }
      tagA = __builtin_amdgcn_readfirstlane(ep[0]) + 1u; tagB = __builtin_amdgcn_readfirstlane(ep[1]) + 1u;
    }
    if constexpr (n == D + 1) {
      float v[8], mean, rs;
#pragma unroll
      for (int j = 0; j < 4; ++j) { v[2 * j] = bflo(xm[j]); v[2 * j + 1] = bfhi(xm[j]); }
      ln_stats(v, mean, rs);
#pragma unroll
      for (int j = 0; j < 8; ++j) v[j] = rbf((v[j] - mean) * rs);
      ln_stats(v, mean, rs);
#pragma unroll
      for (int j = 0; j < 8; ++j) v[j] = rbf((v[j] - mean) * rs);
      if (w < 4) {
        *(f32x4*)(l_y + w * 512 + lane * 8) = f32x4{v[0], v[1], v[2], v[3]};
        *(f32x4*)(l_y + w * 512 + lane * 8 + 4) = f32x4{v[4], v[5], v[6], v[7]};
      }
    }
    if constexpr (n >= D && (n - D) % RA == 0 && ((n - D) / RA) % 2 == 1 && (n - D) / RA < 16) {
      constexpr int j = (n - D) / RA;
#pragma unroll
      for (int k = 0; k < 4; ++k) {
        const unsigned e = Wq[j - 1][k], o = Wq[j][k];
        qa[2 * k] = dot2(__builtin_amdgcn_perm(o, e, 0x05040100u), xn[j >> 1], qa[2 * k]);
        qa[2 * k + 1] = dot2(__builtin_amdgcn_perm(o, e, 0x07060302u), xn[j >> 1], qa[2 * k + 1]);
      }
    }
    if constexpr (n == NQ) {
      float t4[4];
#pragma unroll
      for (int j = 0; j < 4; ++j) t4[j] = tr32(qa[j], qa[j + 4]);
      *(f32x2*)(l_q + w * 128 + 8 * c + 2 * r) = f32x2{tr16(t4[0], t4[2]), tr16(t4[1], t4[3])};
      LDS_BARRIER();
      f32x4 s0 = *(const f32x4*)(l_q + (2 * r) * 128 + 8 * c) + *(const f32x4*)(l_q + (2 * r + 1) * 128 + 8 * c);
      f32x4 s1 = *(const f32x4*)(l_q + (2 * r) * 128 + 8 * c + 4) + *(const f32x4*)(l_q + (2 * r + 1) * 128 + 8 * c + 4);
#pragma unroll
      for (int j = 0; j < 4; ++j) { s0[j] = tr16(s0[j], s0[j]); s0[j] = tr32(s0[j], s0[j]); s1[j] = tr16(s1[j], s1[j]); s1[j] = tr32(s1[j], s1[j]); }
      q[0] = pk_bf16(s0[0], s0[1]); q[1] = pk_bf16(s0[2], s0[3]); q[2] = pk_bf16(s1[0], s1[1]); q[3] = pk_bf16(s1[2], s1[3]);
    }
    if constexpr (n > NQ && n - D < 64) {
      constexpr int hi = (n - NQ) * 16 < n - D + 1 ? (n - NQ) * 16 : n - D + 1;
      constexpr int lo = n - 1 == NQ ? 0 : ((n - 1 - NQ) * 16 < n - D ? (n - 1 - NQ) * 16 : n - D);
      static_for<hi - lo>([&](auto J) { kdot(std::integral_constant<int, lo + J>{}); });
    }
    if constexpr (n == N_W1) {
      float a[32];
#pragma unroll
      for (int bb = 0; bb < 4; ++bb) {
        const float y = l_y[bb * 512 + tid];
#pragma unroll
        for (int j = 0; j < 4; ++j) { a[bb * 8 + 2 * j] = y * bflo(w1v[j]); a[bb * 8 + 2 * j + 1] = y * bfhi(w1v[j]); }
      }
#pragma unroll
      for (int j = 0; j < 16; ++j) a[j] = tr32(a[j], a[j + 16]);
#pragma unroll
      for (int j = 0; j < 8; ++j) a[j] = tr16(a[j], a[j + 8]);
#pragma unroll
      for (int j = 0; j < 4; ++j) a[j] = trd<XOR8>(a[j], a[j + 4], lane & 8);
#pragma unroll
      for (int j = 0; j < 2; ++j) a[j] = trd<XOR7>(a[j], a[j + 2], lane & 4);
      float z = trd<XOR2>(a[0], a[1], lane & 2);
      z += dpp<XOR1>(z);
      if ((lane & 1) == 0) l_h[w * 32 + (lane >> 1)] = z;
    }
    if constexpr (n == 63 + D) {
      const float mw = wave_max(fmaxf(fmaxf(sc[0], sc[1]), fmaxf(sc[2], sc[3]))) * C2;
#pragma unroll
      for (int g = 0; g < 4; ++g) sc[g] = __builtin_amdgcn_exp2f(fmaf(sc[g], C2, -mw));
      const float lw = wave_sum((sc[0] + sc[1]) + (sc[2] + sc[3]));
      if (lane == 0) *(f32x2*)(l_ml + 2 * w) = f32x2{mw, lw};
      LDS_BARRIER();
      const f32x2 ml = *(const f32x2*)(l_ml + 2 * (lane & 7));
      float M = ml[0];
      M = fmaxf(M, dpp<XOR1>(M)); M = fmaxf(M, dpp<XOR2>(M)); M = fmaxf(M, dpp<XOR7>(M));
      float L = ml[1] * __builtin_amdgcn_exp2f(ml[0] - M);
      L += dpp<XOR1>(L); L += dpp<XOR2>(L); L += dpp<XOR7>(L);
      const float f = __builtin_amdgcn_exp2f(mw - M) * __builtin_amdgcn_rcpf(L);
#pragma unroll
      for (int g = 0; g < 4; ++g) sc[g] *= f;
      const float lse = M + __builtin_amdgcn_logf(L);
      __builtin_amdgcn_raw_buffer_store_b128(v4u{__float_as_uint(lse), tagA, 0u, tagB}, rws, tid == 0 ? 0u : 0x80000000u, WS_MSG + (b * 64 + prod) * 16, AUX_SC1);
      float z = 0.f;
#pragma unroll
      for (int ww = 0; ww < 8; ++ww) z += l_h[ww * 32 + (lane & 31)];
      z = rbf(rbf(z) + bflo(b1v));
      const float e2 = __builtin_amdgcn_exp2f((-2.0f * 0.7978845608028654f * 1.4426950408889634f) * (z + 0.044715f * z * z * z));
      const float g = z * __builtin_amdgcn_rcpf(1.0f + e2);
      __builtin_amdgcn_raw_buffer_store_b64(v2u{bf_bits(g) | (tagB << 16), tagA}, rws,
          (has_mlp && w == 1 && lane < 32) ? (unsigned)(((lane >> 3) * FF + (lane & 7)) * 8) : 0x80000000u, WS_HG + u * 64, AUX_SC1);
    }
    if constexpr (n - D >= 64) pv(std::integral_constant<int, n - D - 64>{});
    if constexpr (n == N_G + D) {
      auto stale = [&]() {
        unsigned bad = (msg[1] ^ tagA) | (msg[3] ^ tagB);
#pragma unroll
        for (int i = 0; i < 4; ++i) { const unsigned d = (hg[i][1] ^ tagA) | ((hg[i][0] ^ (tagB << 16)) >> 16); bad |= (i == 3 && !row3) ? 0u : d; }
        return __any(bad != 0u); };
      TOUCH(msg);
      if (__builtin_expect(stale(), 0)) {
#pragma unroll 1
        for (unsigned spin = 0; spin < SPIN_CAP; ++spin) {
          __builtin_amdgcn_s_sleep(8);
          asm volatile("" ::: "memory");
#pragma unroll
          for (int i = 0; i < 4; ++i) hg[i] = __builtin_amdgcn_raw_buffer_load_b64(rws, tid * 8, WS_HG + (b * FF + i * 512) * 8, AUX_SC1);
          msg = __builtin_amdgcn_raw_buffer_load_b128(rws, l16, WS_MSG + ((b + w) & 3) * 1024, AUX_SC1);
          TOUCH(hg[0]); TOUCH(hg[1]); TOUCH(hg[2]); TOUCH(hg[3]); TOUCH(msg);
          if (!stale()) break;
        }
      }
      const float lse = __uint_as_float(msg[0]);
      coef = __builtin_amdgcn_rcpf(1.0f + __builtin_amdgcn_exp2f(dpp<XOR1>(lse) - lse));
      PIN(coef);
      float a[8];
#pragma unroll
      for (int j = 0; j < 8; ++j) a[j] = 0.f;
#pragma unroll
      for (int i = 0; i < 4; ++i) {
        const float hv = (i == 3 && !row3) ? 0.f : bflo(hg[i][0]);
#pragma unroll
        for (int j = 0; j < 4; ++j) { a[2 * j] = fmaf(hv, bflo(W2[i][j]), a[2 * j]); a[2 * j + 1] = fmaf(hv, bfhi(W2[i][j]), a[2 * j + 1]); }
      }
#pragma unroll
      for (int j = 0; j < 4; ++j) a[j] = tr32(a[j], a[j + 4]);
#pragma unroll
      for (int j = 0; j < 2; ++j) a[j] = tr16(a[j], a[j + 2]);
      float z = trd<XOR8>(a[0], a[1], lane & 8);
      z += dpp<XOR7>(z); z += dpp<XOR2>(z); z += dpp<XOR1>(z);
      if (cg == 0) l_m[w * 8 + rg] = z;
    }
    FENCE();
  });

  {
    float t4[4];
#pragma unroll
    for (int j = 0; j < 4; ++j) t4[j] = tr32(acc[j], acc[j + 4]);
    *(f32x2*)(l_o + w * 128 + 8 * c + 2 * r) = f32x2{tr16(t4[0], t4[2]), tr16(t4[1], t4[3])};
  }
  LDS_BARRIER();
  {
    f32x2 o2 = *(const f32x2*)(l_o + 2 * lane);
#pragma unroll
    for (int ww = 1; ww < 8; ++ww) o2 += *(const f32x2*)(l_o + ww * 128 + 2 * lane);
    *(f32x2*)(l_p + w * 128 + 2 * lane) = o2;
    asm volatile("s_waitcnt lgkmcnt(0)" ::: "memory");
    float a[8];
#pragma unroll
    for (int j = 0; j < 8; ++j) a[j] = 0.f;
#pragma unroll
    for (int i4 = 0; i4 < 4; ++i4) {
      const f32x4 ov = *(const f32x4*)(l_p + w * 128 + 16 * rg + 4 * i4);
#pragma unroll
      for (int ii = 0; ii < 4; ++ii) {
        const v4u t = Wo[4 * i4 + ii];
#pragma unroll
        for (int j = 0; j < 4; ++j) { a[2 * j] = fmaf(ov[ii], bflo(t[j]), a[2 * j]); a[2 * j + 1] = fmaf(ov[ii], bfhi(t[j]), a[2 * j + 1]); }
      }
    }
    v4u tv;
#pragma unroll
    for (int j = 0; j < 4; ++j) { float t = tr32(a[j], a[j + 4]); t = tr16(t, t); t += dpp<XOR8>(t); tv[j] = min(__float_as_uint(t), SENT - 1u); }
    __builtin_amdgcn_raw_buffer_store_b128(tv, rws, tb_off, 0, AUX_SC1);
  }
  if (w != 0) return;
  MARK(5);
  if (blk == 0) __builtin_amdgcn_raw_buffer_store_b64(v2u{tagA, tagB}, rws, lane == 0 ? 0u : 0x80000000u, WS_E, AUX_SC1);
  float gate;
  {
    float m = l_m[cg * 8 + rg];
    m += dpp<XOR1>(m); m += dpp<XOR2>(m); m += dpp<XOR7>(m);
    m = rbf(rbf(m) + bflo(b2v));
    gate = rbf(m * __builtin_amdgcn_rcpf(1.0f + __builtin_amdgcn_exp2f(-1.4426950408889634f * m)));
    PIN(gate);
  }
  v4u t0, t1;
#pragma unroll 1
  for (unsigned spin = 0; spin < SPIN_CAP; ++spin) {
    t0 = __builtin_amdgcn_raw_buffer_load_b128(rws, lane * 32, WS_TB + (b * 64 + cons) * 2048, AUX_SC1);
    t1 = __builtin_amdgcn_raw_buffer_load_b128(rws, lane * 32 + 16, WS_TB + (b * 64 + cons) * 2048, AUX_SC1);
    const unsigned mx = max(max(max(t0[0], t0[1]), max(t0[2], t0[3])), max(max(t1[0], t1[1]), max(t1[2], t1[3])));
    if (__all(mx != SENT)) break;
    __builtin_amdgcn_s_sleep(1);
    asm volatile("" ::: "memory");
  }
  float a[4];
#pragma unroll
  for (int j = 0; j < 4; ++j) a[j] = tr32(__uint_as_float(t0[j]) * coef, __uint_as_float(t1[j]) * coef);
  a[0] = tr16(a[0], a[2]); a[1] = tr16(a[1], a[3]);
  float s = trd<XOR8>(a[0], a[1], lane & 8);
  s += dpp<XOR7>(s); s += dpp<XOR2>(s); s += dpp<XOR1>(s);
  const float o = bflo(xv) + rbf(rbf(s) * gate);
  __builtin_amdgcn_raw_buffer_store_b16((unsigned short)bf_bits(o), mk_rsrc(p.out, NB * DM * 2), cg == 0 ? (unsigned)(rg * 2) : 0x80000000u, b * DM * 2 + cons * 16, 0);
  MARK(6);
  if (DIAG && blk == DIAG_BLK) { const unsigned long long te = __builtin_amdgcn_s_memrealtime(); while (__builtin_amdgcn_s_memrealtime() - te < tmark - t0s + 2000) __builtin_amdgcn_s_sleep(1); }
}

extern "C" void kernel_launch(void* const* d_in, const int* in_sizes, int n_in, void* d_out, int out_size, void* d_ws, size_t ws_size,
                              hipStream_t stream) {
  Params p;
  p.x = (const unsigned short*)d_in[0]; p.k = (const unsigned short*)d_in[1]; p.v = (const unsigned short*)d_in[2];
  p.wq = (const unsigned short*)d_in[3]; p.wo = (const unsigned short*)d_in[4];
  p.w1 = (const unsigned short*)d_in[6]; p.b1 = (const unsigned short*)d_in[7]; p.w2 = (const unsigned short*)d_in[8]; p.b2 = (const unsigned short*)d_in[9];
  p.out = (unsigned short*)d_out;
  p.ws = (unsigned char*)d_ws + (WS_AT_END ? ((ws_size - (1u << 20)) & ~(size_t)4095) : 0);
  k_fused<<<256, 512, 0, stream>>>(p);
}
```

```cpp
#include <hip/hip_runtime.h>
#include <hip/hip_bf16.h>
#include <stdint.h>
#include <utility>

#define P_D 4
#define P_RA 2
#define P_RB 3
#define AUX_KV 2
#define AUX_WQ 2
#define AUX_WO 2
#define AUX_W12 0
#define DIAG 0
#define DIAG_BLK 0
#define AUX_OUT 0
#define WS_AT_END 0

typedef unsigned v4u __attribute__((ext_vector_type(4)));
typedef unsigned v2u __attribute__((ext_vector_type(2)));
typedef float f32x4 __attribute__((ext_vector_type(4)));
typedef float f32x2 __attribute__((ext_vector_type(2)));
typedef __bf16 bf16x2 __attribute__((ext_vector_type(2)));

constexpr int NB = 4, DM = 512, NH = 32, SK = 4096, DH = 128, FF = 1792;
constexpr int AUX_SC1 = 16;
constexpr unsigned SENT = 0xFFFFFFFFu;
constexpr unsigned SPIN_CAP = 1u << 18;

template <class F, int... I> __device__ __forceinline__ void static_for_impl(F&& f, std::integer_sequence<int, I...>) { (f(std::integral_constant<int, I>{}), ...); }
template <int N, class F> __device__ __forceinline__ void static_for(F&& f) { static_for_impl(f, std::make_integer_sequence<int, N>{}); }

__device__ __forceinline__ float dot2(unsigned a, unsigned b, float acc) {
  return __builtin_amdgcn_fdot2_f32_bf16(__builtin_bit_cast(bf16x2, a), __builtin_bit_cast(bf16x2, b), acc, false);
}
__device__ __forceinline__ float bflo(unsigned u) { return __uint_as_float(u << 16); }
__device__ __forceinline__ float bfhi(unsigned u) { return __uint_as_float(u & 0xffff0000u); }
__device__ __forceinline__ unsigned pk_bf16(float lo, float hi) { bf16x2 r; r.x = (__bf16)lo; r.y = (__bf16)hi; return __builtin_bit_cast(unsigned, r); }
__device__ __forceinline__ unsigned bf_bits(float x) { return (unsigned)__builtin_bit_cast(unsigned short, (__bf16)x); }
__device__ __forceinline__ float rbf(float x) { return bflo(bf_bits(x)); }
template <int CTRL> __device__ __forceinline__ float dpp(float x) { return __int_as_float(__builtin_amdgcn_mov_dpp(__float_as_int(x), CTRL, 0xf, 0xf, true)); }
constexpr int XOR1 = 0xB1, XOR2 = 0x4E, XOR7 = 0x141, XOR8 = 0x128, BCAST = 0x150;
__device__ __forceinline__ float tr32(float a, float b) {
  auto s = __builtin_amdgcn_permlane32_swap(__float_as_uint(a), __float_as_uint(b), false, false);
  return __uint_as_float(s[0]) + __uint_as_float(s[1]);
}
__device__ __forceinline__ float tr16(float a, float b) {
  auto s = __builtin_amdgcn_permlane16_swap(__float_as_uint(a), __float_as_uint(b), false, false);
  return __uint_as_float(s[0]) + __uint_as_float(s[1]);
}
template <int CTRL> __device__ __forceinline__ float trd(float lo, float hi, bool up) {
  const float t1 = lo + dpp<CTRL>(lo), t2 = hi + dpp<CTRL>(hi);
  return up ? t2 : t1;
}
__device__ __forceinline__ float wave_sum(float x) {
  x += dpp<XOR1>(x); x += dpp<XOR2>(x); x += dpp<XOR7>(x); x += dpp<XOR8>(x);
  x = tr16(x, x); x = tr32(x, x);
  return x;
}
__device__ __forceinline__ float wave_max(float x) {
  x = fmaxf(x, dpp<XOR1>(x)); x = fmaxf(x, dpp<XOR2>(x)); x = fmaxf(x, dpp<XOR7>(x)); x = fmaxf(x, dpp<XOR8>(x));
  auto s = __builtin_amdgcn_permlane16_swap(__float_as_uint(x), __float_as_uint(x), false, false);
  x = fmaxf(__uint_as_float(s[0]), __uint_as_float(s[1]));
  auto t = __builtin_amdgcn_permlane32_swap(__float_as_uint(x), __float_as_uint(x), false, false);
  return fmaxf(__uint_as_float(t[0]), __uint_as_float(t[1]));
}
__device__ __forceinline__ __amdgpu_buffer_rsrc_t mk_rsrc(const void* p, unsigned bytes) {
  return __builtin_amdgcn_make_buffer_rsrc(const_cast<void*>(p), 0, bytes, 0x00020000);
}
__device__ __forceinline__ void ln_stats(const float (&v)[8], float& mean, float& rs) {
  float s = 0.f;
#pragma unroll
  for (int j = 0; j < 8; ++j) s += v[j];
  mean = wave_sum(s) * (1.0f / DM);
  float q = 0.f;
#pragma unroll
  for (int j = 0; j < 8; ++j) { const float d = v[j] - mean; q = fmaf(d, d, q); }
  rs = __builtin_amdgcn_rsqf(wave_sum(q) * (1.0f / DM) + 1e-5f);
}
#define TOUCH(x) asm volatile("" :: "v"(x))
#define PIN(x) asm volatile("" : "+v"(x))
#define MARK(k) do { if (DIAG == (k)) { FENCE(); tmark = __builtin_amdgcn_s_memrealtime(); FENCE(); } } while (0)
#define FENCE() __builtin_amdgcn_sched_barrier(0)
#define LDS_BARRIER() do { asm volatile("s_waitcnt lgkmcnt(0)\n\ts_barrier" ::: "memory"); } while (0)

constexpr unsigned WS_E = 0;
constexpr unsigned WS_MSG = 4096;
constexpr unsigned WS_HG = 16384;
constexpr unsigned WS_TB = 131072;
constexpr unsigned WS_BYTES = WS_TB + 4 * 64 * 64 * 8 * 4;

struct Params {
  const unsigned short* x; const unsigned short* k; const unsigned short* v; const unsigned short* wq; const unsigned short* wo;
  const unsigned short* w1; const unsigned short* b1; const unsigned short* w2; const unsigned short* b2;
  unsigned short* out; unsigned char* ws;
};

constexpr int D = P_D, RA = P_RA, RB = P_RB;
constexpr int NQ = 15 * RA + D;
constexpr int N_W1 = NQ + 4;
constexpr int N_WO = NQ + 6;
constexpr int N_W2 = 92;
constexpr int N_G = 100;
static_assert(N_WO + 15 * RB < 124, "wo loads land before the end");

__global__ void __launch_bounds__(512, 2) k_fused(Params p_arg) {
  __shared__ __attribute__((aligned(16))) float lds[8 * 128 + 4 * 512 + 8 * 32 + 8 * 128 + 8 * 128 + 16 + 64];
  float* l_q = lds;
  float* l_y = l_q + 8 * 128;
  float* l_h = l_y + 4 * 512;
  float* l_o = l_h + 8 * 32;
  float* l_p = l_o + 8 * 128;
  float* l_ml = l_p + 8 * 128;
  float* l_m = l_ml + 16;
  unsigned long long t0s = 0, tmark = 0;
  const Params p = p_arg;
  const int blk = blockIdx.x, tid = threadIdx.x, lane = tid & 63, w = __builtin_amdgcn_readfirstlane(tid >> 6);
  const int idx = blk >> 3, h = (blk & 7) * 4 + (idx & 3), b = idx >> 3, sp = (idx >> 2) & 1;
  const int prod = 2 * h + sp;
  const int cons = (blk & 7) * 8 + (idx & 7);
  const int r = lane >> 4, c = lane & 15, rg = lane >> 3, cg = lane & 7;
  const bool has_mlp = idx < 28, row3 = w < 4;
  const int u = (blk & 7) * 28 + idx;

  const __amdgpu_buffer_rsrc_t rws = mk_rsrc(p.ws, WS_BYTES);
  const __amdgpu_buffer_rsrc_t rx = mk_rsrc(p.x, NB * DM * 2);
  const __amdgpu_buffer_rsrc_t rq = mk_rsrc(p.wq + (size_t)w * 64 * (NH * DH) + h * DH, 64 * NH * DH * 2);
  const size_t kvoff = ((size_t)(b * NH + h) * SK + sp * 2048 + w * 4) * DH;
  const __amdgpu_buffer_rsrc_t rk = mk_rsrc(p.k + kvoff, 524288 - w * 1024), rv = mk_rsrc(p.v + kvoff, 524288 - w * 1024);
  const int l16 = lane * 16;
  const int vq = r * 16 * 8192 + c * 16;
  const int vo = rg * 16 * 1024 + cg * 16;
  const unsigned tb_off = (lane & 24) ? 0x80000000u : WS_TB + (unsigned)(((b * 64 + 8 * w + cg) * 64 + prod) * 32 + (lane >> 5) * 16);

  v4u S[128], Wq[16], Wo[16], W2[4];
  const v2u ep = __builtin_amdgcn_raw_buffer_load_b64(rws, 0, WS_E, AUX_SC1);
  const v4u xa = __builtin_amdgcn_raw_buffer_load_b128(rx, l16, b * DM * 2, 0);
  const v4u xr0 = __builtin_amdgcn_raw_buffer_load_b128(rx, r * 32, b * DM * 2 + w * 128, 0);
  const v4u xr1 = __builtin_amdgcn_raw_buffer_load_b128(rx, r * 32 + 16, b * DM * 2 + w * 128, 0);
  const v4u xm = __builtin_amdgcn_raw_buffer_load_b128(rx, l16, (w & 3) * DM * 2, 0);
  FENCE();

  unsigned xn[8], q[4], tagA = 0, tagB = 0;
  float qa[8], sc[4], ka[16], acc[8], coef = 0.f;
  v4u w1v; v2u hg[4]; v4u msg; unsigned b1v = 0, b2v = 0, xv = 0;
  constexpr float C2 = 0.088388347648318440f * 1.4426950408889634f;

  auto kdot = [&](auto I) { constexpr int i = I; const v4u t = S[i];
    float d = dot2(t[0], q[0], 0.f); d = dot2(t[1], q[1], d); d = dot2(t[2], q[2], d); d = dot2(t[3], q[3], d); PIN(d); ka[i & 15] = d;
    if constexpr ((i & 15) == 15) {
#pragma unroll
      for (int j = 0; j < 8; ++j) ka[j] = trd<XOR8>(ka[j], ka[j + 8], lane & 8);
#pragma unroll
      for (int j = 0; j < 4; ++j) ka[j] = trd<XOR7>(ka[j], ka[j + 4], lane & 4);
#pragma unroll
      for (int j = 0; j < 2; ++j) ka[j] = trd<XOR2>(ka[j], ka[j + 2], lane & 2);
      sc[i >> 4] = trd<XOR1>(ka[0], ka[1], lane & 1); PIN(sc[i >> 4]);
    } };
  auto pv = [&](auto I) { constexpr int i = I; const v4u t = S[64 + i]; const float pb = dpp<BCAST + (i & 15)>(sc[i >> 4]);
#pragma unroll
    for (int j = 0; j < 4; ++j) { acc[2 * j] = fmaf(pb, bflo(t[j]), acc[2 * j]); acc[2 * j + 1] = fmaf(pb, bfhi(t[j]), acc[2 * j + 1]); }
    asm volatile("" : "+v"(acc[0]), "+v"(acc[1]), "+v"(acc[2]), "+v"(acc[3]), "+v"(acc[4]), "+v"(acc[5]), "+v"(acc[6]), "+v"(acc[7])); };

  static_for<128 + D>([&](auto N) {
    constexpr int n = N;
    if constexpr (n >= D) TOUCH(S[n - D]);
    FENCE();
    if constexpr (n == D) MARK(1);
    if constexpr (n == NQ) MARK(2);
    if constexpr (n == 63 + D) MARK(3);
    if constexpr (n == 127 + D) MARK(4);
    if constexpr (n == N_G + D) MARK(7);
    if constexpr (n % RA == 0 && n / RA < 16) Wq[n / RA] = __builtin_amdgcn_raw_buffer_load_b128(rq, vq, (n / RA) * 8192, AUX_WQ);
    if constexpr (n >= N_WO && (n - N_WO) % RB == 0 && (n - N_WO) / RB < 16)
      Wo[(n - N_WO) / RB] = __builtin_amdgcn_raw_buffer_load_b128(mk_rsrc(p.wo + (size_t)h * DH * DM + w * 64, DH * DM * 2), vo, ((n - N_WO) / RB) * 1024, AUX_WO);
    if constexpr (n == 1) __builtin_amdgcn_raw_buffer_store_b128(v4u{SENT, SENT, SENT, SENT}, rws, tb_off, 0, AUX_SC1);
    if constexpr (n == N_W1 - D - 1) {
      w1v = __builtin_amdgcn_raw_buffer_load_b128(mk_rsrc(p.w1, has_mlp ? DM * FF * 2 : 0), tid * (FF * 2), u * 16, AUX_W12);
      b1v = __builtin_amdgcn_raw_buffer_load_b16(mk_rsrc(p.b1, has_mlp ? FF * 2 : 0), (lane & 7) * 2, u * 16, 0);
    }
    if constexpr (n >= N_W2 && n < N_W2 + 4) W2[n - N_W2] = __builtin_amdgcn_raw_buffer_load_b128(mk_rsrc(p.w2, FF * DM * 2), (n == N_W2 + 3 && !row3) ? 0x80000000u : (unsigned)(tid * (DM * 2)), (n - N_W2) * 512 * (DM * 2) + cons * 16, AUX_W12);
    if constexpr (n == N_G) {
#pragma unroll
      for (int i = 0; i < 4; ++i) hg[i] = __builtin_amdgcn_raw_buffer_load_b64(rws, tid * 8, WS_HG + (b * FF + i * 512) * 8, AUX_SC1);
      msg = __builtin_amdgcn_raw_buffer_load_b128(rws, l16, WS_MSG + ((b + w) & 3) * 1024, AUX_SC1);
      b2v = __builtin_amdgcn_raw_buffer_load_b16(mk_rsrc(p.b2, DM * 2), rg * 2, cons * 16, 0);
      xv = __builtin_amdgcn_raw_buffer_load_b16(rx, rg * 2, b * DM * 2 + cons * 16, 0);
    }
    if constexpr (n < 64) S[n] = __builtin_amdgcn_raw_buffer_load_b128(rk, l16, n * 8192, AUX_KV);
    else if constexpr (n < 128) S[n] = __builtin_amdgcn_raw_buffer_load_b128(rv, l16, (n - 64) * 8192, AUX_KV);
    FENCE();
    if constexpr (n == D - 1) {
      float v[8], mean, rs;
#pragma unroll
      for (int j = 0; j < 4; ++j) { v[2 * j] = bflo(xa[j]); v[2 * j + 1] = bfhi(xa[j]); }
      ln_stats(v, mean, rs);
#pragma unroll
      for (int j = 0; j < 8; ++j) { const unsigned t = j < 4 ? xr0[j & 3] : xr1[j & 3]; xn[j] = pk_bf16((bflo(t) - mean) * rs, (bfhi(t) - mean) * rs); }
#pragma unroll
      for (int j = 0; j < 8; ++j) { qa[j] = 0.f; acc[j] = 0.f; }
      tagA = __builtin_amdgcn_readfirstlane(ep[0]) + 1u; tagB = __builtin_amdgcn_readfirstlane(ep[1]) + 1u;
    }
    if constexpr (n == D + 1) {
      float v[8], mean, rs;
#pragma unroll
      for (int j = 0; j < 4; ++j) { v[2 * j] = bflo(xm[j]); v[2 * j + 1] = bfhi(xm[j]); }
      ln_stats(v, mean, rs);
#pragma unroll
      for (int j = 0; j < 8; ++j) v[j] = rbf((v[j] - mean) * rs);
      ln_stats(v, mean, rs);
#pragma unroll
      for (int j = 0; j < 8; ++j) v[j] = rbf((v[j] - mean) * rs);
      if (w < 4) {
        *(f32x4*)(l_y + w * 512 + lane * 8) = f32x4{v[0], v[1], v[2], v[3]};
        *(f32x4*)(l_y + w * 512 + lane * 8 + 4) = f32x4{v[4], v[5], v[6], v[7]};
      }
    }
    if constexpr (n >= D && (n - D) % RA == 0 && ((n - D) / RA) % 2 == 1 && (n - D) / RA < 16) {
      constexpr int j = (n - D) / RA;
#pragma unroll
      for (int k = 0; k < 4; ++k) {
        const unsigned e = Wq[j - 1][k], o = Wq[j][k];
        qa[2 * k] = dot2(__builtin_amdgcn_perm(o, e, 0x05040100u), xn[j >> 1], qa[2 * k]);
        qa[2 * k + 1] = dot2(__builtin_amdgcn_perm(o, e, 0x07060302u), xn[j >> 1], qa[2 * k + 1]);
      }
    }
    if constexpr (n == NQ) {
      float t4[4];
#pragma unroll
      for (int j = 0; j < 4; ++j) t4[j] = tr32(qa[j], qa[j + 4]);
      *(f32x2*)(l_q + w * 128 + 8 * c + 2 * r) = f32x2{tr16(t4[0], t4[2]), tr16(t4[1], t4[3])};
      LDS_BARRIER();
      f32x4 s0 = *(const f32x4*)(l_q + (2 * r) * 128 + 8 * c) + *(const f32x4*)(l_q + (2 * r + 1) * 128 + 8 * c);
      f32x4 s1 = *(const f32x4*)(l_q + (2 * r) * 128 + 8 * c + 4) + *(const f32x4*)(l_q + (2 * r + 1) * 128 + 8 * c + 4);
#pragma unroll
      for (int j = 0; j < 4; ++j) { s0[j] = tr16(s0[j], s0[j]); s0[j] = tr32(s0[j], s0[j]); s1[j] = tr16(s1[j], s1[j]); s1[j] = tr32(s1[j], s1[j]); }
      q[0] = pk_bf16(s0[0], s0[1]); q[1] = pk_bf16(s0[2], s0[3]); q[2] = pk_bf16(s1[0], s1[1]); q[3] = pk_bf16(s1[2], s1[3]);
    }
    if constexpr (n > NQ && n - D < 64) {
      constexpr int hi = (n - NQ) * 16 < n - D + 1 ? (n - NQ) * 16 : n - D + 1;
      constexpr int lo = n - 1 == NQ ? 0 : ((n - 1 - NQ) * 16 < n - D ? (n - 1 - NQ) * 16 : n - D);
      static_for<hi - lo>([&](auto J) { kdot(std::integral_constant<int, lo + J>{}); });
    }
    if constexpr (n == N_W1) {
      float a[32];
#pragma unroll
      for (int bb = 0; bb < 4; ++bb) {
        const float y = l_y[bb * 512 + tid];
#pragma unroll
        for (int j = 0; j < 4; ++j) { a[bb * 8 + 2 * j] = y * bflo(w1v[j]); a[bb * 8 + 2 * j + 1] = y * bfhi(w1v[j]); }
      }
#pragma unroll
      for (int j = 0; j < 16; ++j) a[j] = tr32(a[j], a[j + 16]);
#pragma unroll
      for (int j = 0; j < 8; ++j) a[j] = tr16(a[j], a[j + 8]);
#pragma unroll
      for (int j = 0; j < 4; ++j) a[j] = trd<XOR8>(a[j], a[j + 4], lane & 8);
#pragma unroll
      for (int j = 0; j < 2; ++j) a[j] = trd<XOR7>(a[j], a[j + 2], lane & 4);
      float z = trd<XOR2>(a[0], a[1], lane & 2);
      z += dpp<XOR1>(z);
      if ((lane & 1) == 0) l_h[w * 32 + (lane >> 1)] = z;
    }
    if constexpr (n == 63 + D) {
      const float mw = wave_max(fmaxf(fmaxf(sc[0], sc[1]), fmaxf(sc[2], sc[3]))) * C2;
#pragma unroll
      for (int g = 0; g < 4; ++g) sc[g] = __builtin_amdgcn_exp2f(fmaf(sc[g], C2, -mw));
      const float lw = wave_sum((sc[0] + sc[1]) + (sc[2] + sc[3]));
      if (lane == 0) *(f32x2*)(l_ml + 2 * w) = f32x2{mw, lw};
      LDS_BARRIER();
      const f32x2 ml = *(const f32x2*)(l_ml + 2 * (lane & 7));
      float M = ml[0];
      M = fmaxf(M, dpp<XOR1>(M)); M = fmaxf(M, dpp<XOR2>(M)); M = fmaxf(M, dpp<XOR7>(M));
      float L = ml[1] * __builtin_amdgcn_exp2f(ml[0] - M);
      L += dpp<XOR1>(L); L += dpp<XOR2>(L); L += dpp<XOR7>(L);
      const float f = __builtin_amdgcn_exp2f(mw - M) * __builtin_amdgcn_rcpf(L);
#pragma unroll
      for (int g = 0; g < 4; ++g) sc[g] *= f;
      const float lse = M + __builtin_amdgcn_logf(L);
      __builtin_amdgcn_raw_buffer_store_b128(v4u{__float_as_uint(lse), tagA, 0u, tagB}, rws, tid == 0 ? 0u : 0x80000000u, WS_MSG + (b * 64 + prod) * 16, AUX_SC1);
      float z = 0.f;
#pragma unroll
      for (int ww = 0; ww < 8; ++ww) z += l_h[ww * 32 + (lane & 31)];
      z = rbf(rbf(z) + bflo(b1v));
      const float e2 = __builtin_amdgcn_exp2f((-2.0f * 0.7978845608028654f * 1.4426950408889634f) * (z + 0.044715f * z * z * z));
      const float g = z * __builtin_amdgcn_rcpf(1.0f + e2);
      __builtin_amdgcn_raw_buffer_store_b64(v2u{bf_bits(g) | (tagB << 16), tagA}, rws,
          (has_mlp && w == 1 && lane < 32) ? (unsigned)(((lane >> 3) * FF + (lane & 7)) * 8) : 0x80000000u, WS_HG + u * 64, AUX_SC1);
    }
    if constexpr (n - D >= 64) pv(std::integral_constant<int, n - D - 64>{});
    if constexpr (n == N_G + D) {
      auto stale = [&]() {
        unsigned bad = (msg[1] ^ tagA) | (msg[3] ^ tagB);
#pragma unroll
        for (int i = 0; i < 4; ++i) { const unsigned d = (hg[i][1] ^ tagA) | ((hg[i][0] ^ (tagB << 16)) >> 16); bad |= (i == 3 && !row3) ? 0u : d; }
        return __any(bad != 0u); };
      TOUCH(msg);
      if (__builtin_expect(stale(), 0)) {
#pragma unroll 1
        for (unsigned spin = 0; spin < SPIN_CAP; ++spin) {
          __builtin_amdgcn_s_sleep(8);
          asm volatile("" ::: "memory");
#pragma unroll
          for (int i = 0; i < 4; ++i) hg[i] = __builtin_amdgcn_raw_buffer_load_b64(rws, tid * 8, WS_HG + (b * FF + i * 512) * 8, AUX_SC1);
          msg = __builtin_amdgcn_raw_buffer_load_b128(rws, l16, WS_MSG + ((b + w) & 3) * 1024, AUX_SC1);
          TOUCH(hg[0]); TOUCH(hg[1]); TOUCH(hg[2]); TOUCH(hg[3]); TOUCH(msg);
          if (!stale()) break;
        }
      }
      const float lse = __uint_as_float(msg[0]);
      coef = __builtin_amdgcn_rcpf(1.0f + __builtin_amdgcn_exp2f(dpp<XOR1>(lse) - lse));
      PIN(coef);
      float a[8];
#pragma unroll
      for (int j = 0; j < 8; ++j) a[j] = 0.f;
#pragma unroll
      for (int i = 0; i < 4; ++i) {
        const float hv = (i == 3 && !row3) ? 0.f : bflo(hg[i][0]);
#pragma unroll
        for (int j = 0; j < 4; ++j) { a[2 * j] = fmaf(hv, bflo(W2[i][j]), a[2 * j]); a[2 * j + 1] = fmaf(hv, bfhi(W2[i][j]), a[2 * j + 1]); }
      }
#pragma unroll
      for (int j = 0; j < 4; ++j) a[j] = tr32(a[j], a[j + 4]);
#pragma unroll
      for (int j = 0; j < 2; ++j) a[j] = tr16(a[j], a[j + 2]);
      float z = trd<XOR8>(a[0], a[1], lane & 8);
      z += dpp<XOR7>(z); z += dpp<XOR2>(z); z += dpp<XOR1>(z);
      if (cg == 0) l_m[w * 8 + rg] = z;
    }
    FENCE();
  });

  {
    float t4[4];
#pragma unroll
    for (int j = 0; j < 4; ++j) t4[j] = tr32(acc[j], acc[j + 4]);
    *(f32x2*)(l_o + w * 128 + 8 * c + 2 * r) = f32x2{tr16(t4[0], t4[2]), tr16(t4[1], t4[3])};
  }
  LDS_BARRIER();
  {
    f32x2 o2 = *(const f32x2*)(l_o + 2 * lane);
#pragma unroll
    for (int ww = 1; ww < 8; ++ww) o2 += *(const f32x2*)(l_o + ww * 128 + 2 * lane);
    *(f32x2*)(l_p + w * 128 + 2 * lane) = o2;
    asm volatile("s_waitcnt lgkmcnt(0)" ::: "memory");
    float a[8];
#pragma unroll
    for (int j = 0; j < 8; ++j) a[j] = 0.f;
#pragma unroll
    for (int i4 = 0; i4 < 4; ++i4) {
      const f32x4 ov = *(const f32x4*)(l_p + w * 128 + 16 * rg + 4 * i4);
#pragma unroll
      for (int ii = 0; ii < 4; ++ii) {
        const v4u t = Wo[4 * i4 + ii];
#pragma unroll
        for (int j = 0; j < 4; ++j) { a[2 * j] = fmaf(ov[ii], bflo(t[j]), a[2 * j]); a[2 * j + 1] = fmaf(ov[ii], bfhi(t[j]), a[2 * j + 1]); }
      }
    }
    v4u tv;
#pragma unroll
    for (int j = 0; j < 4; ++j) { float t = tr32(a[j], a[j + 4]); t = tr16(t, t); t += dpp<XOR8>(t); tv[j] = min(__float_as_uint(t), SENT - 1u); }
    __builtin_amdgcn_raw_buffer_store_b128(tv, rws, tb_off, 0, AUX_SC1);
  }
  if (w != 0) return;
  MARK(5);
  if (blk == 0) __builtin_amdgcn_raw_buffer_store_b64(v2u{tagA, tagB}, rws, lane == 0 ? 0u : 0x80000000u, WS_E, AUX_SC1);
  float gate;
  {
    float m = l_m[cg * 8 + rg];
    m += dpp<XOR1>(m); m += dpp<XOR2>(m); m += dpp<XOR7>(m);
    m = rbf(rbf(m) + bflo(b2v));
    gate = rbf(m * __builtin_amdgcn_rcpf(1.0f + __builtin_amdgcn_exp2f(-1.4426950408889634f * m)));
    PIN(gate);
  }
  v4u t0, t1;
#pragma unroll 1
  for (unsigned spin = 0; spin < SPIN_CAP; ++spin) {
    t0 = __builtin_amdgcn_raw_buffer_load_b128(rws, lane * 32, WS_TB + (b * 64 + cons) * 2048, AUX_SC1);
    t1 = __builtin_amdgcn_raw_buffer_load_b128(rws, lane * 32 + 16, WS_TB + (b * 64 + cons) * 2048, AUX_SC1);
    const unsigned mx = max(max(max(t0[0], t0[1]), max(t0[2], t0[3])), max(max(t1[0], t1[1]), max(t1[2], t1[3])));
    if (__all(mx != SENT)) break;
    __builtin_amdgcn_s_sleep(1);
    asm volatile("" ::: "memory");
  }
  float a[4];
#pragma unroll
  for (int j = 0; j < 4; ++j) a[j] = tr32(__uint_as_float(t0[j]) * coef, __uint_as_float(t1[j]) * coef);
  a[0] = tr16(a[0], a[2]); a[1] = tr16(a[1], a[3]);
  float s = trd<XOR8>(a[0], a[1], lane & 8);
  s += dpp<XOR7>(s); s += dpp<XOR2>(s); s += dpp<XOR1>(s);
  const float o = bflo(xv) + rbf(rbf(s) * gate);
  __builtin_amdgcn_raw_buffer_store_b16((unsigned short)bf_bits(o), mk_rsrc(p.out, NB * DM * 2), cg == 0 ? (unsigned)(rg * 2) : 0x80000000u, b * DM * 2 + cons * 16, AUX_OUT);
  MARK(6);
  if (DIAG && blk == DIAG_BLK) { const unsigned long long te = __builtin_amdgcn_s_memrealtime(); while (__builtin_amdgcn_s_memrealtime() - te < tmark - t0s + 2000) __builtin_amdgcn_s_sleep(1); }
}

extern "C" void kernel_launch(void* const* d_in, const int* in_sizes, int n_in, void* d_out, int out_size, void* d_ws, size_t ws_size,
                              hipStream_t stream) {
  Params p;
  p.x = (const unsigned short*)d_in[0]; p.k = (const unsigned short*)d_in[1]; p.v = (const unsigned short*)d_in[2];
  p.wq = (const unsigned short*)d_in[3]; p.wo = (const unsigned short*)d_in[4];
  p.w1 = (const unsigned short*)d_in[6]; p.b1 = (const unsigned short*)d_in[7]; p.w2 = (const unsigned short*)d_in[8]; p.b2 = (const unsigned short*)d_in[9];
  p.out = (unsigned short*)d_out;
  p.ws = (unsigned char*)d_ws + (WS_AT_END ? ((ws_size - (1u << 20)) & ~(size_t)4095) : 0);
  k_fused<<<256, 512, 0, stream>>>(p);
}
```

```cpp
#include <hip/hip_runtime.h>
#include <hip/hip_bf16.h>
#include <stdint.h>
#include <utility>

#define P_D 4
#define P_RA 2
#define P_RB 3
#define AUX_KV 2
#define AUX_WQ 2
#define AUX_WO 2
#define AUX_W12 0
#define DIAG 8
#define DIAG_BLK 0
#define AUX_OUT 0
#define WS_AT_END 0

typedef unsigned v4u __attribute__((ext_vector_type(4)));
typedef unsigned v2u __attribute__((ext_vector_type(2)));
typedef float f32x4 __attribute__((ext_vector_type(4)));
typedef float f32x2 __attribute__((ext_vector_type(2)));
typedef __bf16 bf16x2 __attribute__((ext_vector_type(2)));

constexpr int NB = 4, DM = 512, NH = 32, SK = 4096, DH = 128, FF = 1792;
constexpr int AUX_SC1 = 16;
constexpr unsigned SENT = 0xFFFFFFFFu;
constexpr unsigned SPIN_CAP = 1u << 18;

template <class F, int... I> __device__ __forceinline__ void static_for_impl(F&& f, std::integer_sequence<int, I...>) { (f(std::integral_constant<int, I>{}), ...); }
template <int N, class F> __device__ __forceinline__ void static_for(F&& f) { static_for_impl(f, std::make_integer_sequence<int, N>{}); }

__device__ __forceinline__ float dot2(unsigned a, unsigned b, float acc) {
  return __builtin_amdgcn_fdot2_f32_bf16(__builtin_bit_cast(bf16x2, a), __builtin_bit_cast(bf16x2, b), acc, false);
}
__device__ __forceinline__ float bflo(unsigned u) { return __uint_as_float(u << 16); }
__device__ __forceinline__ float bfhi(unsigned u) { return __uint_as_float(u & 0xffff0000u); }
__device__ __forceinline__ unsigned pk_bf16(float lo, float hi) { bf16x2 r; r.x = (__bf16)lo; r.y = (__bf16)hi; return __builtin_bit_cast(unsigned, r); }
__device__ __forceinline__ unsigned bf_bits(float x) { return (unsigned)__builtin_bit_cast(unsigned short, (__bf16)x); }
__device__ __forceinline__ float rbf(float x) { return bflo(bf_bits(x)); }
template <int CTRL> __device__ __forceinline__ float dpp(float x) { return __int_as_float(__builtin_amdgcn_mov_dpp(__float_as_int(x), CTRL, 0xf, 0xf, true)); }
constexpr int XOR1 = 0xB1, XOR2 = 0x4E, XOR7 = 0x141, XOR8 = 0x128, BCAST = 0x150;
__device__ __forceinline__ float tr32(float a, float b) {
  auto s = __builtin_amdgcn_permlane32_swap(__float_as_uint(a), __float_as_uint(b), false, false);
  return __uint_as_float(s[0]) + __uint_as_float(s[1]);
}
__device__ __forceinline__ float tr16(float a, float b) {
  auto s = __builtin_amdgcn_permlane16_swap(__float_as_uint(a), __float_as_uint(b), false, false);
  return __uint_as_float(s[0]) + __uint_as_float(s[1]);
}
template <int CTRL> __device__ __forceinline__ float trd(float lo, float hi, bool up) {
  const float t1 = lo + dpp<CTRL>(lo), t2 = hi + dpp<CTRL>(hi);
  return up ? t2 : t1;
}
__device__ __forceinline__ float wave_sum(float x) {
  x += dpp<XOR1>(x); x += dpp<XOR2>(x); x += dpp<XOR7>(x); x += dpp<XOR8>(x);
  x = tr16(x, x); x = tr32(x, x);
  return x;
}
__device__ __forceinline__ float wave_max(float x) {
  x = fmaxf(x, dpp<XOR1>(x)); x = fmaxf(x, dpp<XOR2>(x)); x = fmaxf(x, dpp<XOR7>(x)); x = fmaxf(x, dpp<XOR8>(x));
  auto s = __builtin_amdgcn_permlane16_swap(__float_as_uint(x), __float_as_uint(x), false, false);
  x = fmaxf(__uint_as_float(s[0]), __uint_as_float(s[1]));
  auto t = __builtin_amdgcn_permlane32_swap(__float_as_uint(x), __float_as_uint(x), false, false);
  return fmaxf(__uint_as_float(t[0]), __uint_as_float(t[1]));
}
__device__ __forceinline__ __amdgpu_buffer_rsrc_t mk_rsrc(const void* p, unsigned bytes) {
  return __builtin_amdgcn_make_buffer_rsrc(const_cast<void*>(p), 0, bytes, 0x00020000);
}
__device__ __forceinline__ void ln_stats(const float (&v)[8], float& mean, float& rs) {
  float s = 0.f;
#pragma unroll
  for (int j = 0; j < 8; ++j) s += v[j];
  mean = wave_sum(s) * (1.0f / DM);
  float q = 0.f;
#pragma unroll
  for (int j = 0; j < 8; ++j) { const float d = v[j] - mean; q = fmaf(d, d, q); }
  rs = __builtin_amdgcn_rsqf(wave_sum(q) * (1.0f / DM) + 1e-5f);
}
#define TOUCH(x) asm volatile("" :: "v"(x))
#define PIN(x) asm volatile("" : "+v"(x))
#define MARK(k) do { if (DIAG == (k)) { FENCE(); tmark = __builtin_amdgcn_s_memrealtime(); FENCE(); } } while (0)
#define FENCE() __builtin_amdgcn_sched_barrier(0)
#define LDS_BARRIER() do { asm volatile("s_waitcnt lgkmcnt(0)\n\ts_barrier" ::: "memory"); } while (0)

constexpr unsigned WS_E = 0;
constexpr unsigned WS_MSG = 4096;
constexpr unsigned WS_HG = 16384;
constexpr unsigned WS_TB = 131072;
constexpr unsigned WS_BYTES = WS_TB + 4 * 64 * 64 * 8 * 4;

struct Params {
  const unsigned short* x; const unsigned short* k; const unsigned short* v; const unsigned short* wq; const unsigned short* wo;
  const unsigned short* w1; const unsigned short* b1; const unsigned short* w2; const unsigned short* b2;
  unsigned short* out; unsigned char* ws;
};

constexpr int D = P_D, RA = P_RA, RB = P_RB;
constexpr int NQ = 15 * RA + D;
constexpr int N_W1 = NQ + 4;
constexpr int N_WO = NQ + 6;
constexpr int N_W2 = 92;
constexpr int N_G = 100;
static_assert(N_WO + 15 * RB < 124, "wo loads land before the end");

__global__ void __launch_bounds__(512, 2) k_fused(Params p_arg) {
  __shared__ __attribute__((aligned(16))) float lds[8 * 128 + 4 * 512 + 8 * 32 + 8 * 128 + 8 * 128 + 16 + 64];
  float* l_q = lds;
  float* l_y = l_q + 8 * 128;
  float* l_h = l_y + 4 * 512;
  float* l_o = l_h + 8 * 32;
  float* l_p = l_o + 8 * 128;
  float* l_ml = l_p + 8 * 128;
  float* l_m = l_ml + 16;
  unsigned long long t0s = 0, tmark = 0;
  const __attribute__((address_space(4))) Params* kp = (const __attribute__((address_space(4))) Params*)__builtin_amdgcn_kernarg_segment_ptr();
  asm volatile("s_memrealtime %0\n\ts_waitcnt lgkmcnt(0)" : "=s"(t0s), "+s"(kp));
  tmark = t0s;
  Params p; p.x = kp->x; p.k = kp->k; p.v = kp->v; p.wq = kp->wq; p.wo = kp->wo; p.w1 = kp->w1; p.b1 = kp->b1; p.w2 = kp->w2; p.b2 = kp->b2; p.out = kp->out; p.ws = kp->ws;
  if (DIAG == 9) { asm volatile("s_waitcnt lgkmcnt(0)\n\ts_memrealtime %0\n\ts_waitcnt lgkmcnt(0)" : "=s"(tmark), "+s"(p.x), "+s"(p.ws), "+s"(p.b2)); }
  const int blk = blockIdx.x, tid = threadIdx.x, lane = tid & 63, w = __builtin_amdgcn_readfirstlane(tid >> 6);
  const int idx = blk >> 3, h = (blk & 7) * 4 + (idx & 3), b = idx >> 3, sp = (idx >> 2) & 1;
  const int prod = 2 * h + sp;
  const int cons = (blk & 7) * 8 + (idx & 7);
  const int r = lane >> 4, c = lane & 15, rg = lane >> 3, cg = lane & 7;
  const bool has_mlp = idx < 28, row3 = w < 4;
  const int u = (blk & 7) * 28 + idx;

  const __amdgpu_buffer_rsrc_t rws = mk_rsrc(p.ws, WS_BYTES);
  const __amdgpu_buffer_rsrc_t rx = mk_rsrc(p.x, NB * DM * 2);
  const __amdgpu_buffer_rsrc_t rq = mk_rsrc(p.wq + (size_t)w * 64 * (NH * DH) + h * DH, 64 * NH * DH * 2);
  const size_t kvoff = ((size_t)(b * NH + h) * SK + sp * 2048 + w * 4) * DH;
  const __amdgpu_buffer_rsrc_t rk = mk_rsrc(p.k + kvoff, 524288 - w * 1024), rv = mk_rsrc(p.v + kvoff, 524288 - w * 1024);
  const int l16 = lane * 16;
  const int vq = r * 16 * 8192 + c * 16;
  const int vo = rg * 16 * 1024 + cg * 16;
  const unsigned tb_off = (lane & 24) ? 0x80000000u : WS_TB + (unsigned)(((b * 64 + 8 * w + cg) * 64 + prod) * 32 + (lane >> 5) * 16);

  v4u S[128], Wq[16], Wo[16], W2[4];
  const v2u ep = __builtin_amdgcn_raw_buffer_load_b64(rws, 0, WS_E, AUX_SC1);
  const v4u xa = __builtin_amdgcn_raw_buffer_load_b128(rx, l16, b * DM * 2, 0);
  const v4u xr0 = __builtin_amdgcn_raw_buffer_load_b128(rx, r * 32, b * DM * 2 + w * 128, 0);
  const v4u xr1 = __builtin_amdgcn_raw_buffer_load_b128(rx, r * 32 + 16, b * DM * 2 + w * 128, 0);
  const v4u xm = __builtin_amdgcn_raw_buffer_load_b128(rx, l16, (w & 3) * DM * 2, 0);
  FENCE();

  unsigned xn[8], q[4], tagA = 0, tagB = 0;
  float qa[8], sc[4], ka[16], acc[8], coef = 0.f;
  v4u w1v; v2u hg[4]; v4u msg; unsigned b1v = 0, b2v = 0, xv = 0;
  constexpr float C2 = 0.088388347648318440f * 1.4426950408889634f;

  auto kdot = [&](auto I) { constexpr int i = I; const v4u t = S[i];
    float d = dot2(t[0], q[0], 0.f); d = dot2(t[1], q[1], d); d = dot2(t[2], q[2], d); d = dot2(t[3], q[3], d); PIN(d); ka[i & 15] = d;
    if constexpr ((i & 15) == 15) {
#pragma unroll
      for (int j = 0; j < 8; ++j) ka[j] = trd<XOR8>(ka[j], ka[j + 8], lane & 8);
#pragma unroll
      for (int j = 0; j < 4; ++j) ka[j] = trd<XOR7>(ka[j], ka[j + 4], lane & 4);
#pragma unroll
      for (int j = 0; j < 2; ++j) ka[j] = trd<XOR2>(ka[j], ka[j + 2], lane & 2);
      sc[i >> 4] = trd<XOR1>(ka[0], ka[1], lane & 1); PIN(sc[i >> 4]);
    } };
  auto pv = [&](auto I) { constexpr int i = I; const v4u t = S[64 + i]; const float pb = dpp<BCAST + (i & 15)>(sc[i >> 4]);
#pragma unroll
    for (int j = 0; j < 4; ++j) { acc[2 * j] = fmaf(pb, bflo(t[j]), acc[2 * j]); acc[2 * j + 1] = fmaf(pb, bfhi(t[j]), acc[2 * j + 1]); }
    asm volatile("" : "+v"(acc[0]), "+v"(acc[1]), "+v"(acc[2]), "+v"(acc[3]), "+v"(acc[4]), "+v"(acc[5]), "+v"(acc[6]), "+v"(acc[7])); };

  static_for<128 + D>([&](auto N) {
    constexpr int n = N;
    if constexpr (n >= D) TOUCH(S[n - D]);
    FENCE();
    if constexpr (n == D) MARK(1);
    if constexpr (n == NQ) MARK(2);
    if constexpr (n == 63 + D) MARK(3);
    if constexpr (n == 127 + D) MARK(4);
    if constexpr (n == N_G + D) MARK(7);
    if constexpr (n % RA == 0 && n / RA < 16) Wq[n / RA] = __builtin_amdgcn_raw_buffer_load_b128(rq, vq, (n / RA) * 8192, AUX_WQ);
    if constexpr (n >= N_WO && (n - N_WO) % RB == 0 && (n - N_WO) / RB < 16)
      Wo[(n - N_WO) / RB] = __builtin_amdgcn_raw_buffer_load_b128(mk_rsrc(p.wo + (size_t)h * DH * DM + w * 64, DH * DM * 2), vo, ((n - N_WO) / RB) * 1024, AUX_WO);
    if constexpr (n == 1) __builtin_amdgcn_raw_buffer_store_b128(v4u{SENT, SENT, SENT, SENT}, rws, tb_off, 0, AUX_SC1);
    if constexpr (n == N_W1 - D - 1) {
      w1v = __builtin_amdgcn_raw_buffer_load_b128(mk_rsrc(p.w1, has_mlp ? DM * FF * 2 : 0), tid * (FF * 2), u * 16, AUX_W12);
      b1v = __builtin_amdgcn_raw_buffer_load_b16(mk_rsrc(p.b1, has_mlp ? FF * 2 : 0), (lane & 7) * 2, u * 16, 0);
    }
    if constexpr (n >= N_W2 && n < N_W2 + 4) W2[n - N_W2] = __builtin_amdgcn_raw_buffer_load_b128(mk_rsrc(p.w2, FF * DM * 2), (n == N_W2 + 3 && !row3) ? 0x80000000u : (unsigned)(tid * (DM * 2)), (n - N_W2) * 512 * (DM * 2) + cons * 16, AUX_W12);
    if constexpr (n == N_G) {
#pragma unroll
      for (int i = 0; i < 4; ++i) hg[i] = __builtin_amdgcn_raw_buffer_load_b64(rws, tid * 8, WS_HG + (b * FF + i * 512) * 8, AUX_SC1);
      msg = __builtin_amdgcn_raw_buffer_load_b128(rws, l16, WS_MSG + ((b + w) & 3) * 1024, AUX_SC1);
      b2v = __builtin_amdgcn_raw_buffer_load_b16(mk_rsrc(p.b2, DM * 2), rg * 2, cons * 16, 0);
      xv = __builtin_amdgcn_raw_buffer_load_b16(rx, rg * 2, b * DM * 2 + cons * 16, 0);
    }
    if constexpr (n < 64) S[n] = __builtin_amdgcn_raw_buffer_load_b128(rk, l16, n * 8192, AUX_KV);
    else if constexpr (n < 128) S[n] = __builtin_amdgcn_raw_buffer_load_b128(rv, l16, (n - 64) * 8192, AUX_KV);
    FENCE();
    if constexpr (n == D - 1) {
      float v[8], mean, rs;
#pragma unroll
      for (int j = 0; j < 4; ++j) { v[2 * j] = bflo(xa[j]); v[2 * j + 1] = bfhi(xa[j]); }
      ln_stats(v, mean, rs);
#pragma unroll
      for (int j = 0; j < 8; ++j) { const unsigned t = j < 4 ? xr0[j & 3] : xr1[j & 3]; xn[j] = pk_bf16((bflo(t) - mean) * rs, (bfhi(t) - mean) * rs); }
#pragma unroll
      for (int j = 0; j < 8; ++j) { qa[j] = 0.f; acc[j] = 0.f; }
      tagA = __builtin_amdgcn_readfirstlane(ep[0]) + 1u; tagB = __builtin_amdgcn_readfirstlane(ep[1]) + 1u;
    }
    if constexpr (n == D + 1) {
      float v[8], mean, rs;
#pragma unroll
      for (int j = 0; j < 4; ++j) { v[2 * j] = bflo(xm[j]); v[2 * j + 1] = bfhi(xm[j]); }
      ln_stats(v, mean, rs);
#pragma unroll
      for (int j = 0; j < 8; ++j) v[j] = rbf((v[j] - mean) * rs);
      ln_stats(v, mean, rs);
#pragma unroll
      for (int j = 0; j < 8; ++j) v[j] = rbf((v[j] - mean) * rs);
      if (w < 4) {
        *(f32x4*)(l_y + w * 512 + lane * 8) = f32x4{v[0], v[1], v[2], v[3]};
        *(f32x4*)(l_y + w * 512 + lane * 8 + 4) = f32x4{v[4], v[5], v[6], v[7]};
      }
    }
    if constexpr (n >= D && (n - D) % RA == 0 && ((n - D) / RA) % 2 == 1 && (n - D) / RA < 16) {
      constexpr int j = (n - D) / RA;
#pragma unroll
      for (int k = 0; k < 4; ++k) {
        const unsigned e = Wq[j - 1][k], o = Wq[j][k];
        qa[2 * k] = dot2(__builtin_amdgcn_perm(o, e, 0x05040100u), xn[j >> 1], qa[2 * k]);
        qa[2 * k + 1] = dot2(__builtin_amdgcn_perm(o, e, 0x07060302u), xn[j >> 1], qa[2 * k + 1]);
      }
    }
    if constexpr (n == NQ) {
      float t4[4];
#pragma unroll
      for (int j = 0; j < 4; ++j) t4[j] = tr32(qa[j], qa[j + 4]);
      *(f32x2*)(l_q + w * 128 + 8 * c + 2 * r) = f32x2{tr16(t4[0], t4[2]), tr16(t4[1], t4[3])};
      LDS_BARRIER();
      f32x4 s0 = *(const f32x4*)(l_q + (2 * r) * 128 + 8 * c) + *(const f32x4*)(l_q + (2 * r + 1) * 128 + 8 * c);
      f32x4 s1 = *(const f32x4*)(l_q + (2 * r) * 128 + 8 * c + 4) + *(const f32x4*)(l_q + (2 * r + 1) * 128 + 8 * c + 4);
#pragma unroll
      for (int j = 0; j < 4; ++j) { s0[j] = tr16(s0[j], s0[j]); s0[j] = tr32(s0[j], s0[j]); s1[j] = tr16(s1[j], s1[j]); s1[j] = tr32(s1[j], s1[j]); }
      q[0] = pk_bf16(s0[0], s0[1]); q[1] = pk_bf16(s0[2], s0[3]); q[2] = pk_bf16(s1[0], s1[1]); q[3] = pk_bf16(s1[2], s1[3]);
    }
    if constexpr (n > NQ && n - D < 64) {
      constexpr int hi = (n - NQ) * 16 < n - D + 1 ? (n - NQ) * 16 : n - D + 1;
      constexpr int lo = n - 1 == NQ ? 0 : ((n - 1 - NQ) * 16 < n - D ? (n - 1 - NQ) * 16 : n - D);
      static_for<hi - lo>([&](auto J) { kdot(std::integral_constant<int, lo + J>{}); });
    }
    if constexpr (n == N_W1) {
      float a[32];
#pragma unroll
      for (int bb = 0; bb < 4; ++bb) {
        const float y = l_y[bb * 512 + tid];
#pragma unroll
        for (int j = 0; j < 4; ++j) { a[bb * 8 + 2 * j] = y * bflo(w1v[j]); a[bb * 8 + 2 * j + 1] = y * bfhi(w1v[j]); }
      }
#pragma unroll
      for (int j = 0; j < 16; ++j) a[j] = tr32(a[j], a[j + 16]);
#pragma unroll
      for (int j = 0; j < 8; ++j) a[j] = tr16(a[j], a[j + 8]);
#pragma unroll
      for (int j = 0; j < 4; ++j) a[j] = trd<XOR8>(a[j], a[j + 4], lane & 8);
#pragma unroll
      for (int j = 0; j < 2; ++j) a[j] = trd<XOR7>(a[j], a[j + 2], lane & 4);
      float z = trd<XOR2>(a[0], a[1], lane & 2);
      z += dpp<XOR1>(z);
      if ((lane & 1) == 0) l_h[w * 32 + (lane >> 1)] = z;
    }
    if constexpr (n == 63 + D) {
      const float mw = wave_max(fmaxf(fmaxf(sc[0], sc[1]), fmaxf(sc[2], sc[3]))) * C2;
#pragma unroll
      for (int g = 0; g < 4; ++g) sc[g] = __builtin_amdgcn_exp2f(fmaf(sc[g], C2, -mw));
      const float lw = wave_sum((sc[0] + sc[1]) + (sc[2] + sc[3]));
      if (lane == 0) *(f32x2*)(l_ml + 2 * w) = f32x2{mw, lw};
      LDS_BARRIER();
      const f32x2 ml = *(const f32x2*)(l_ml + 2 * (lane & 7));
      float M = ml[0];
      M = fmaxf(M, dpp<XOR1>(M)); M = fmaxf(M, dpp<XOR2>(M)); M = fmaxf(M, dpp<XOR7>(M));
      float L = ml[1] * __builtin_amdgcn_exp2f(ml[0] - M);
      L += dpp<XOR1>(L); L += dpp<XOR2>(L); L += dpp<XOR7>(L);
      const float f = __builtin_amdgcn_exp2f(mw - M) * __builtin_amdgcn_rcpf(L);
#pragma unroll
      for (int g = 0; g < 4; ++g) sc[g] *= f;
      const float lse = M + __builtin_amdgcn_logf(L);
      __builtin_amdgcn_raw_buffer_store_b128(v4u{__float_as_uint(lse), tagA, 0u, tagB}, rws, tid == 0 ? 0u : 0x80000000u, WS_MSG + (b * 64 + prod) * 16, AUX_SC1);
      float z = 0.f;
#pragma unroll
      for (int ww = 0; ww < 8; ++ww) z += l_h[ww * 32 + (lane & 31)];
      z = rbf(rbf(z) + bflo(b1v));
      const float e2 = __builtin_amdgcn_exp2f((-2.0f * 0.7978845608028654f * 1.4426950408889634f) * (z + 0.044715f * z * z * z));
      const float g = z * __builtin_amdgcn_rcpf(1.0f + e2);
      __builtin_amdgcn_raw_buffer_store_b64(v2u{bf_bits(g) | (tagB << 16), tagA}, rws,
          (has_mlp && w == 1 && lane < 32) ? (unsigned)(((lane >> 3) * FF + (lane & 7)) * 8) : 0x80000000u, WS_HG + u * 64, AUX_SC1);
    }
    if constexpr (n - D >= 64) pv(std::integral_constant<int, n - D - 64>{});
    if constexpr (n == N_G + D) {
      auto stale = [&]() {
        unsigned bad = (msg[1] ^ tagA) | (msg[3] ^ tagB);
#pragma unroll
        for (int i = 0; i < 4; ++i) { const unsigned d = (hg[i][1] ^ tagA) | ((hg[i][0] ^ (tagB << 16)) >> 16); bad |= (i == 3 && !row3) ? 0u : d; }
        return __any(bad != 0u); };
      TOUCH(msg);
      if (__builtin_expect(stale(), 0)) {
#pragma unroll 1
        for (unsigned spin = 0; spin < SPIN_CAP; ++spin) {
          __builtin_amdgcn_s_sleep(8);
          asm volatile("" ::: "memory");
#pragma unroll
          for (int i = 0; i < 4; ++i) hg[i] = __builtin_amdgcn_raw_buffer_load_b64(rws, tid * 8, WS_HG + (b * FF + i * 512) * 8, AUX_SC1);
          msg = __builtin_amdgcn_raw_buffer_load_b128(rws, l16, WS_MSG + ((b + w) & 3) * 1024, AUX_SC1);
          TOUCH(hg[0]); TOUCH(hg[1]); TOUCH(hg[2]); TOUCH(hg[3]); TOUCH(msg);
          if (!stale()) break;
        }
      }
      const float lse = __uint_as_float(msg[0]);
      coef = __builtin_amdgcn_rcpf(1.0f + __builtin_amdgcn_exp2f(dpp<XOR1>(lse) - lse));
      PIN(coef);
      float a[8];
#pragma unroll
      for (int j = 0; j < 8; ++j) a[j] = 0.f;
#pragma unroll
      for (int i = 0; i < 4; ++i) {
        const float hv = (i == 3 && !row3) ? 0.f : bflo(hg[i][0]);
#pragma unroll
        for (int j = 0; j < 4; ++j) { a[2 * j] = fmaf(hv, bflo(W2[i][j]), a[2 * j]); a[2 * j + 1] = fmaf(hv, bfhi(W2[i][j]), a[2 * j + 1]); }
      }
#pragma unroll
      for (int j = 0; j < 4; ++j) a[j] = tr32(a[j], a[j + 4]);
#pragma unroll
      for (int j = 0; j < 2; ++j) a[j] = tr16(a[j], a[j + 2]);
      float z = trd<XOR8>(a[0], a[1], lane & 8);
      z += dpp<XOR7>(z); z += dpp<XOR2>(z); z += dpp<XOR1>(z);
      if (cg == 0) l_m[w * 8 + rg] = z;
    }
    FENCE();
  });

  {
    float t4[4];
#pragma unroll
    for (int j = 0; j < 4; ++j) t4[j] = tr32(acc[j], acc[j + 4]);
    *(f32x2*)(l_o + w * 128 + 8 * c + 2 * r) = f32x2{tr16(t4[0], t4[2]), tr16(t4[1], t4[3])};
  }
  LDS_BARRIER();
  {
    f32x2 o2 = *(const f32x2*)(l_o + 2 * lane);
#pragma unroll
    for (int ww = 1; ww < 8; ++ww) o2 += *(const f32x2*)(l_o + ww * 128 + 2 * lane);
    *(f32x2*)(l_p + w * 128 + 2 * lane) = o2;
    asm volatile("s_waitcnt lgkmcnt(0)" ::: "memory");
    float a[8];
#pragma unroll
    for (int j = 0; j < 8; ++j) a[j] = 0.f;
#pragma unroll
    for (int i4 = 0; i4 < 4; ++i4) {
      const f32x4 ov = *(const f32x4*)(l_p + w * 128 + 16 * rg + 4 * i4);
#pragma unroll
      for (int ii = 0; ii < 4; ++ii) {
        const v4u t = Wo[4 * i4 + ii];
#pragma unroll
        for (int j = 0; j < 4; ++j) { a[2 * j] = fmaf(ov[ii], bflo(t[j]), a[2 * j]); a[2 * j + 1] = fmaf(ov[ii], bfhi(t[j]), a[2 * j + 1]); }
      }
    }
    v4u tv;
#pragma unroll
    for (int j = 0; j < 4; ++j) { float t = tr32(a[j], a[j + 4]); t = tr16(t, t); t += dpp<XOR8>(t); tv[j] = min(__float_as_uint(t), SENT - 1u); }
    __builtin_amdgcn_raw_buffer_store_b128(tv, rws, tb_off, 0, AUX_SC1);
  }
  if (w != 0) return;
  MARK(5);
  if (blk == 0) __builtin_amdgcn_raw_buffer_store_b64(v2u{tagA, tagB}, rws, lane == 0 ? 0u : 0x80000000u, WS_E, AUX_SC1);
  float gate;
  {
    float m = l_m[cg * 8 + rg];
    m += dpp<XOR1>(m); m += dpp<XOR2>(m); m += dpp<XOR7>(m);
    m = rbf(rbf(m) + bflo(b2v));
    gate = rbf(m * __builtin_amdgcn_rcpf(1.0f + __builtin_amdgcn_exp2f(-1.4426950408889634f * m)));
    PIN(gate);
  }
  v4u t0, t1;
#pragma unroll 1
  for (unsigned spin = 0; spin < SPIN_CAP; ++spin) {
    t0 = __builtin_amdgcn_raw_buffer_load_b128(rws, lane * 32, WS_TB + (b * 64 + cons) * 2048, AUX_SC1);
    t1 = __builtin_amdgcn_raw_buffer_load_b128(rws, lane * 32 + 16, WS_TB + (b * 64 + cons) * 2048, AUX_SC1);
    const unsigned mx = max(max(max(t0[0], t0[1]), max(t0[2], t0[3])), max(max(t1[0], t1[1]), max(t1[2], t1[3])));
    if (__all(mx != SENT)) break;
    __builtin_amdgcn_s_sleep(1);
    asm volatile("" ::: "memory");
  }
  float a[4];
#pragma unroll
  for (int j = 0; j < 4; ++j) a[j] = tr32(__uint_as_float(t0[j]) * coef, __uint_as_float(t1[j]) * coef);
  a[0] = tr16(a[0], a[2]); a[1] = tr16(a[1], a[3]);
  float s = trd<XOR8>(a[0], a[1], lane & 8);
  s += dpp<XOR7>(s); s += dpp<XOR2>(s); s += dpp<XOR1>(s);
  const float o = bflo(xv) + rbf(rbf(s) * gate);
  __builtin_amdgcn_raw_buffer_store_b16((unsigned short)bf_bits(o), mk_rsrc(p.out, NB * DM * 2), cg == 0 ? (unsigned)(rg * 2) : 0x80000000u, b * DM * 2 + cons * 16, AUX_OUT);
  MARK(6);
  if (DIAG && blk == DIAG_BLK) { while (__builtin_amdgcn_s_memrealtime() - t0s < tmark - t0s + 10000) __builtin_amdgcn_s_sleep(1); }
}

extern "C" void kernel_launch(void* const* d_in, const int* in_sizes, int n_in, void* d_out, int out_size, void* d_ws, size_t ws_size,
                              hipStream_t stream) {
  Params p;
  p.x = (const unsigned short*)d_in[0]; p.k = (const unsigned short*)d_in[1]; p.v = (const unsigned short*)d_in[2];
  p.wq = (const unsigned short*)d_in[3]; p.wo = (const unsigned short*)d_in[4];
  p.w1 = (const unsigned short*)d_in[6]; p.b1 = (const unsigned short*)d_in[7]; p.w2 = (const unsigned short*)d_in[8]; p.b2 = (const unsigned short*)d_in[9];
  p.out = (unsigned short*)d_out;
  p.ws = (unsigned char*)d_ws + (WS_AT_END ? ((ws_size - (1u << 20)) & ~(size_t)4095) : 0);
  k_fused<<<256, 512, 0, stream>>>(p);
}
```

```cpp
#include <hip/hip_runtime.h>
#include <hip/hip_bf16.h>
#include <stdint.h>
#include <utility>

#define P_D 6
#define P_NA 40
#define P_DO 3
#define AUX_KV 2
#define AUX_WQ 2
#define AUX_WO 0
#define AUX_W12 2
#define P_TA 200
#define P_TAU 225
#define P_BAND 6
#define P_LO 4
#define P_FIX -1
#define ABL_WO 0
#define ROT 1
#define DIAG 0
#define DIAG_BLK 0

typedef unsigned v4u __attribute__((ext_vector_type(4)));
typedef unsigned v2u __attribute__((ext_vector_type(2)));
typedef float f32x4 __attribute__((ext_vector_type(4)));
typedef float f32x2 __attribute__((ext_vector_type(2)));
typedef __bf16 bf16x2 __attribute__((ext_vector_type(2)));
typedef __bf16 bf16x8 __attribute__((ext_vector_type(8)));
typedef __amdgpu_buffer_rsrc_t rsrc_t;
typedef __attribute__((address_space(3))) unsigned lds_u32;

constexpr int NB = 4, DM = 512, NH = 32, SK = 4096, DH = 128, FF = 1792;
constexpr int AUX_SC1 = 16;
constexpr unsigned SENT = 0xFFFFFFFFu, OOB = 0x80000000u;
constexpr unsigned SPIN_CAP = 1u << 14;

template <class F, int... I> __device__ __forceinline__ void static_for_impl(F&& f, std::integer_sequence<int, I...>) { (f(std::integral_constant<int, I>{}), ...); }
template <int N, class F> __device__ __forceinline__ void static_for(F&& f) { static_for_impl(f, std::make_integer_sequence<int, N>{}); }

__device__ __forceinline__ float dot2(unsigned a, unsigned b, float acc) {
  return __builtin_amdgcn_fdot2_f32_bf16(__builtin_bit_cast(bf16x2, a), __builtin_bit_cast(bf16x2, b), acc, false);
}
__device__ __forceinline__ f32x4 mfma(v4u a, v4u b, f32x4 c) {
  return __builtin_amdgcn_mfma_f32_16x16x32_bf16(__builtin_bit_cast(bf16x8, a), __builtin_bit_cast(bf16x8, b), c, 0, 0, 0);
}
__device__ __forceinline__ float bflo(unsigned u) { return __uint_as_float(u << 16); }
__device__ __forceinline__ float bfhi(unsigned u) { return __uint_as_float(u & 0xffff0000u); }
__device__ __forceinline__ unsigned pk_bf16(float lo, float hi) { bf16x2 r; r.x = (__bf16)lo; r.y = (__bf16)hi; return __builtin_bit_cast(unsigned, r); }
__device__ __forceinline__ unsigned bf_bits(float x) { return (unsigned)__builtin_bit_cast(unsigned short, (__bf16)x); }
__device__ __forceinline__ float rbf(float x) { return bflo(bf_bits(x)); }
__device__ __forceinline__ unsigned pair_lo(unsigned e, unsigned o) { return __builtin_amdgcn_perm(o, e, 0x05040100u); }
__device__ __forceinline__ unsigned pair_hi(unsigned e, unsigned o) { return __builtin_amdgcn_perm(o, e, 0x07060302u); }
template <int CTRL> __device__ __forceinline__ float dpp(float x) { return __int_as_float(__builtin_amdgcn_mov_dpp(__float_as_int(x), CTRL, 0xf, 0xf, true)); }
constexpr int XOR1 = 0xB1, XOR2 = 0x4E, XOR7 = 0x141, XOR8 = 0x128, ROR4 = 0x124, BCAST = 0x150;
__device__ __forceinline__ float tr32(float a, float b) {
  auto s = __builtin_amdgcn_permlane32_swap(__float_as_uint(a), __float_as_uint(b), false, false);
  return __uint_as_float(s[0]) + __uint_as_float(s[1]);
}
__device__ __forceinline__ float tr16(float a, float b) {
  auto s = __builtin_amdgcn_permlane16_swap(__float_as_uint(a), __float_as_uint(b), false, false);
  return __uint_as_float(s[0]) + __uint_as_float(s[1]);
}
template <int CTRL> __device__ __forceinline__ float trd(float lo, float hi, bool up) {
  const float t1 = lo + dpp<CTRL>(lo), t2 = hi + dpp<CTRL>(hi);
  return up ? t2 : t1;
}
__device__ __forceinline__ float wave_sum(float x) {
  x += dpp<XOR1>(x); x += dpp<XOR2>(x); x += dpp<XOR7>(x); x += dpp<XOR8>(x);
  x = tr16(x, x); x = tr32(x, x);
  return x;
}
__device__ __forceinline__ float wave_max(float x) {
  x = fmaxf(x, dpp<XOR1>(x)); x = fmaxf(x, dpp<XOR2>(x)); x = fmaxf(x, dpp<XOR7>(x)); x = fmaxf(x, dpp<XOR8>(x));
  auto s = __builtin_amdgcn_permlane16_swap(__float_as_uint(x), __float_as_uint(x), false, false);
  x = fmaxf(__uint_as_float(s[0]), __uint_as_float(s[1]));
  auto t = __builtin_amdgcn_permlane32_swap(__float_as_uint(x), __float_as_uint(x), false, false);
  return fmaxf(__uint_as_float(t[0]), __uint_as_float(t[1]));
}
__device__ __forceinline__ float sum2_quadlane(float v0, float v1) {
  float s = tr32(v0, v1);
  s = tr16(s, s); s += dpp<XOR8>(s); s += dpp<ROR4>(s);
  return s;
}
__device__ __forceinline__ v4u gran2(float v0, float v1, unsigned tagA, unsigned tagB) {
  const unsigned tb = tagB & 0xfffu;
  return v4u{((__float_as_uint(v0) + 0x800u) & 0xfffff000u) | tb, tagA, ((__float_as_uint(v1) + 0x800u) & 0xfffff000u) | tb, tagA};
}
__device__ __forceinline__ unsigned take2(v4u g, unsigned tagA, unsigned tagB, float& v0, float& v1) {
  v0 += __uint_as_float(g[0] & 0xfffff000u); v1 += __uint_as_float(g[2] & 0xfffff000u);
  return (g[1] ^ tagA) | (g[3] ^ tagA) | (((g[0] ^ tagB) | (g[2] ^ tagB)) & 0xfffu);
}
__device__ __forceinline__ rsrc_t mk_rsrc(const void* p, unsigned bytes) { return __builtin_amdgcn_make_buffer_rsrc(const_cast<void*>(p), 0, bytes, 0x00020000); }
template <int AUX> __device__ __forceinline__ v4u ld128(rsrc_t r, unsigned v, unsigned s) { return __builtin_amdgcn_raw_buffer_load_b128(r, v, s, AUX); }
template <int AUX> __device__ __forceinline__ v2u ld64(rsrc_t r, unsigned v, unsigned s) { return __builtin_amdgcn_raw_buffer_load_b64(r, v, s, AUX); }
__device__ __forceinline__ void st128(v4u d, rsrc_t r, unsigned v, unsigned s) { __builtin_amdgcn_raw_buffer_store_b128(d, r, v + s, 0, AUX_SC1); }
__device__ __forceinline__ void st64(v2u d, rsrc_t r, unsigned v, unsigned s) { __builtin_amdgcn_raw_buffer_store_b64(d, r, v + s, 0, AUX_SC1); }
__device__ __forceinline__ float rows4_sum(float x) { x += dpp<XOR1>(x); x += dpp<XOR2>(x); x = tr16(x, x); x = tr32(x, x); return x; }
__device__ __forceinline__ void ln_stats(const v4u (&x)[4], float& mean, float& inv) {
  float s = 0.f;
#pragma unroll
  for (int t = 0; t < 4; ++t)
#pragma unroll
    for (int j = 0; j < 4; ++j) s += bflo(x[t][j]) + bfhi(x[t][j]);
  const float mu = rows4_sum(s) * (1.0f / DM);
  float q = 0.f;
#pragma unroll
  for (int t = 0; t < 4; ++t)
#pragma unroll
    for (int j = 0; j < 4; ++j) { const float d0 = bflo(x[t][j]) - mu, d1 = bfhi(x[t][j]) - mu; q = fmaf(d0, d0, q); q = fmaf(d1, d1, q); }
  const float var = rows4_sum(q) * (1.0f / DM);
  mean = rbf(mu);
  inv = __builtin_amdgcn_rcpf(rbf(__builtin_amdgcn_sqrtf(rbf(rbf(var) + 1e-5f))));
}
__device__ __forceinline__ v4u ln_apply(v4u x, float mean, float inv) {
  v4u o;
#pragma unroll
  for (int j = 0; j < 4; ++j) { const unsigned d = pk_bf16(bflo(x[j]) - mean, bfhi(x[j]) - mean); o[j] = pk_bf16(bflo(d) * inv, bfhi(d) * inv); }
  return o;
}
template <int J> __device__ __forceinline__ v4u bfrag(const v4u* t) {
  v4u f;
#pragma unroll
  for (int d = 0; d < 4; ++d) f[d] = (J & 1) ? pair_hi(t[2 * d][J >> 1], t[2 * d + 1][J >> 1]) : pair_lo(t[2 * d][J >> 1], t[2 * d + 1][J >> 1]);
  return f;
}
template <int J> __device__ __forceinline__ v4u bfrag2(const v2u* t) {
  v4u f;
#pragma unroll
  for (int d = 0; d < 4; ++d) f[d] = (J & 1) ? pair_hi(t[2 * d][J >> 1], t[2 * d + 1][J >> 1]) : pair_lo(t[2 * d][J >> 1], t[2 * d + 1][J >> 1]);
  return f;
}
__device__ __forceinline__ f32x4 tile64(v4u a0, v4u a1, const v2u* t) {
  f32x4 o;
  static_for<4>([&](auto J) { constexpr int j = J;
    f32x4 d = mfma(a0, bfrag2<j>(t), f32x4{0.f, 0.f, 0.f, 0.f}); d = mfma(a1, bfrag2<j>(t + 8), d); o[j] = d[0]; });
  return o;
}
#define TOUCH(x) asm volatile("" :: "v"(x))
#define PIN(x) asm volatile("" : "+v"(x))
#define STR2(x) #x
#define STR(x) STR2(x)
#define THROTTLE(mask, i) asm volatile("s_bitcmp1_b32 %0, %1\n\ts_cbranch_scc0 1f\n\ts_waitcnt vmcnt(" STR(P_LO) ")\n1:" :: "s"(mask), "n"(i) : "scc")
#define FENCE() __builtin_amdgcn_sched_barrier(0)
#define WG_BARRIER() asm volatile("s_waitcnt lgkmcnt(0)\n\ts_barrier" ::: "memory")
#define DRAIN() asm volatile("s_waitcnt vmcnt(0)" ::: "memory")
#define LDS_WAIT() asm volatile("s_waitcnt lgkmcnt(0)" ::: "memory")
#define MARK(k) do { if (DIAG == (k) + 1) { FENCE(); tmark = __builtin_amdgcn_s_memrealtime(); FENCE(); } } while (0)
#define FLUSH_STAMPS()

constexpr unsigned WS_E = 0;
constexpr unsigned WS_XF = 4096;
constexpr unsigned WS_LS = 16384;
constexpr unsigned WS_X1 = 65536;
constexpr unsigned WS_MP = WS_X1 + 32 * 8192;
constexpr unsigned WS_TB = WS_MP + 4 * 32 * 4096;
constexpr unsigned WS_QX = WS_TB + 4 * 64 * 4096;
constexpr unsigned WS_BYTES = WS_QX + 32 * 4 * 8 * 1024;

struct Params {
  const unsigned short* x; const unsigned short* k; const unsigned short* v; const unsigned short* wq; const unsigned short* wo;
  const unsigned short* w1; const unsigned short* b1; const unsigned short* w2; const unsigned short* b2;
  unsigned short* out; unsigned char* ws;
};

constexpr int D = P_D, NA = P_NA, DO = P_DO;
constexpr int LD = 8;
constexpr int NU = (NA <= 48 + LD - 12) ? 48 : 64;
static_assert((NU + LD - NA) * 4 >= NU && D <= LD, "the backlog is gone before the loop starts");
constexpr float C2 = 0.088388347648318440f * 1.4426950408889634f;

constexpr int L_Q = 0, L_M = 256, L_F = 272, L_TAG = 288, L_GX = 320, L_H = 1024, L_O = 1536, L_S = L_O + 8 * 256, L_END = L_S + 8192;

__global__ void __launch_bounds__(512, 2) k_fused(Params p) {
  __shared__ __attribute__((aligned(16))) unsigned char lds[L_END];
  unsigned long long t0s = 0, tmark = 0;
  if (DIAG) { t0s = __builtin_amdgcn_s_memrealtime(); tmark = t0s; }
  const int blk = blockIdx.x, tid = threadIdx.x, lane = tid & 63, w = __builtin_amdgcn_readfirstlane(tid >> 6);
  MARK(0);
  asm volatile("" :: "s"(p.x), "s"(p.k), "s"(p.v), "s"(p.wq), "s"(p.wo), "s"(p.w1), "s"(p.b1), "s"(p.w2), "s"(p.b2), "s"(p.out), "s"(p.ws));
  const int idx = blk >> 3, h = (blk & 7) * 4 + (idx & 3), bs = idx >> 2, b = bs >> 1, sp = bs & 1;
  const int cons = (blk & 7) * 8 + (idx & 7);
  const int col0 = 128 * (cons >> 4) + 32 * (cons & 3) + (cons & 4) + ((cons >> 2) & 2);
  const int r = lane >> 4, c = lane & 15;
  const unsigned l16 = lane * 16;
  const rsrc_t rws = mk_rsrc(p.ws, WS_BYTES);
  const unsigned rot = ROT ? (bs >> 1) * 32 + (bs & 1) * 4 : 0;

  if (__builtin_expect(w < 4, 1)) {
    __builtin_amdgcn_s_setprio(2);
    const size_t kvoff = ((size_t)(b * NH + h) * SK + sp * 2048 + w * 4) * DH;
    const unsigned short* kp = p.k + kvoff; const unsigned short* vp = p.v + kvoff;
    const unsigned kvlen = 524288 - w * 1024;
    const rsrc_t rk = mk_rsrc(kp, kvlen);
    float* const scs = (float*)(lds + L_S + w * 2048) + lane;
    v4u S[NU + LD], q;
    const unsigned tstart = (unsigned)__builtin_amdgcn_s_memrealtime();
    auto pace = [&](int g, unsigned now) { const int lag = (int)(now - tstart) - (P_TA + g * P_TAU);
      const int lv = P_FIX >= 0 ? P_FIX : lag >> P_BAND;
      unsigned m = 0xffffu; m = lv > 0 ? 0x5555u : m; m = lv > 1 ? 0x9249u : m; m = lv > 2 ? 0x1111u : m; m = lv > 3 ? 0x0101u : m; m = lv > 4 ? 0u : m;
      return (unsigned)__builtin_amdgcn_readfirstlane(m); };
    float ka[16], mw = -INFINITY;
    auto kdot = [&](v4u t, int slot) {
      float d = dot2(t[0], q[0], 0.f); d = dot2(t[1], q[1], d); d = dot2(t[2], q[2], d); d = dot2(t[3], q[3], d); PIN(d); ka[slot] = d; };
    auto kfold = [&](int g) {
#pragma unroll
      for (int j = 0; j < 8; ++j) ka[j] = trd<XOR8>(ka[j], ka[j + 8], lane & 8);
#pragma unroll
      for (int j = 0; j < 4; ++j) ka[j] = trd<XOR7>(ka[j], ka[j + 4], lane & 4);
#pragma unroll
      for (int j = 0; j < 2; ++j) ka[j] = trd<XOR2>(ka[j], ka[j + 2], lane & 2);
      const float s = trd<XOR1>(ka[0], ka[1], lane & 1) * C2;
      scs[g * 64] = s; mw = fmaxf(mw, s); PIN(mw); };
    static_for<NU + LD>([&](auto N) {
      constexpr int n = N;
      if constexpr (n >= D) TOUCH(S[n - D]);
      FENCE();
      if constexpr (n == D) MARK(1);
      S[n] = ld128<AUX_KV>(rk, l16, n * 4096);
      FENCE();
      if constexpr (n == NA - 1) {
        MARK(2);
        WG_BARRIER();
        MARK(3);
        q = *(const v4u*)(lds + L_Q + c * 16);
      }
      if constexpr (n >= NA) {
        constexpr int hi = (n - NA + 1) * 4 < n - LD + 1 ? (n - NA + 1) * 4 : n - LD + 1;
        constexpr int lo = (n - NA) * 4 < n - LD ? (n - NA) * 4 : n - LD;
        static_for<hi - lo>([&](auto J) { constexpr int i = lo + J; kdot(S[i], i & 15); if constexpr ((i & 15) == 15) kfold(i >> 4); });
      }
      FENCE();
    });
    v4u R[16];
#pragma unroll
    for (int i = 0; i < LD; ++i) R[i] = S[NU + i];
    float snext = scs[0];
    unsigned now = (unsigned)__builtin_amdgcn_s_memrealtime();
#pragma unroll 1
    for (int g = NU / 16; g < 8; ++g) {
      const rsrc_t rn = mk_rsrc(g == 7 ? vp : kp, kvlen);
      const unsigned o0 = g * 65536, o1 = g == 7 ? 0u : o0 + 65536;
      const unsigned mask = pace(g, now);
      static_for<16>([&](auto I) { constexpr int i = I;
        TOUCH(R[i]); THROTTLE(mask, i); FENCE();
        if constexpr (i + LD < 16) R[i + LD] = ld128<AUX_KV>(rk, l16, o0 + (i + LD) * 4096);
        else R[i + LD - 16] = ld128<AUX_KV>(rn, l16, o1 + (i + LD - 16) * 4096);
        if constexpr (i == 15) now = (unsigned)__builtin_amdgcn_s_memrealtime();
        FENCE();
        kdot(R[i], i);
        FENCE(); });
      kfold(g);
    }
    MARK(4);
    mw = wave_max(mw);
    asm volatile("" ::: "memory");
    __hip_atomic_store((lds_u32*)lds + L_M / 4 + w, min(__float_as_uint(mw), SENT - 1u), __ATOMIC_RELAXED, __HIP_MEMORY_SCOPE_WORKGROUP);
    float acc[8];
#pragma unroll
    for (int j = 0; j < 8; ++j) acc[j] = 0.f;
    const rsrc_t rv = mk_rsrc(vp, kvlen);
    MARK(5);
#pragma unroll 1
    for (int g = 0; g < 8; ++g) {
      const rsrc_t rn = mk_rsrc(vp, g == 7 ? 0u : kvlen);
      const unsigned o0 = g * 65536;
      const float pg = __builtin_amdgcn_exp2f(snext - mw);
      snext = scs[((g + 1) & 7) * 64];
      const unsigned mask = pace(8 + g, now) & (g == 7 ? 0xffu : 0xffffu);
      static_for<16>([&](auto I) { constexpr int i = I;
        TOUCH(R[i]); THROTTLE(mask, i); FENCE();
        R[(i + LD) & 15] = ld128<AUX_KV>(i + LD < 16 ? rv : rn, l16, o0 + (i + LD) * 4096);
        if constexpr (i == 15) now = (unsigned)__builtin_amdgcn_s_memrealtime();
        FENCE();
        const v4u t = R[i]; const float pb = dpp<BCAST + i>(pg);
#pragma unroll
        for (int j = 0; j < 4; ++j) { acc[2 * j] = fmaf(pb, bflo(t[j]), acc[2 * j]); acc[2 * j + 1] = fmaf(pb, bfhi(t[j]), acc[2 * j + 1]); }
        asm volatile("" : "+v"(acc[0]), "+v"(acc[1]), "+v"(acc[2]), "+v"(acc[3]), "+v"(acc[4]), "+v"(acc[5]), "+v"(acc[6]), "+v"(acc[7]));
        FENCE(); });
    }
    MARK(6);
    {
      float t4[4];
#pragma unroll
      for (int j = 0; j < 4; ++j) t4[j] = tr32(acc[j], acc[j + 4]);
      const float o0 = tr16(t4[0], t4[2]), o1 = tr16(t4[1], t4[3]);
      const unsigned hi = pk_bf16(o0, o1), lo = pk_bf16(o0 - bflo(hi), o1 - bfhi(hi));
      *(unsigned*)(lds + L_O + w * 256 + ((c * 16 + r * 4) ^ (rot * 2))) = hi;
      *(unsigned*)(lds + L_O + (4 + w) * 256 + ((c * 16 + r * 4) ^ (rot * 2))) = lo;
    }
    WG_BARRIER();
    MARK(7);
    if (w != 0) { FLUSH_STAMPS(); return; }
    const v2u tg = *(const v2u*)(lds + L_TAG);
    const f32x2 gx = *(const f32x2*)(lds + L_GX + ((lane & 3) * 2 + (lane >> 5)) * 8);
    const unsigned go = (lane >> 2) * 4096 + (lane & 3) * 16;
    float s0, s1;
#pragma unroll 1
    for (unsigned spin = 0; spin < SPIN_CAP; ++spin) {
      v4u G[4];
#pragma unroll
      for (int j = 0; j < 4; ++j) G[j] = ld128<AUX_SC1>(rws, go, WS_TB + b * 262144 + j * 65536 + cons * 64);
      unsigned bad = 0; s0 = 0.f; s1 = 0.f;
#pragma unroll
      for (int j = 0; j < 4; ++j) bad |= take2(G[j], tg[0], tg[1], s0, s1);
      if (!__any(bad != 0u)) break;
      __builtin_amdgcn_s_sleep(1);
      asm volatile("" ::: "memory");
    }
    const float o = gx[1] + rbf(rbf(sum2_quadlane(s0, s1)) * gx[0]);
    __builtin_amdgcn_raw_buffer_store_b16((unsigned short)bf_bits(o), mk_rsrc(p.out, NB * DM * 2), (lane & 28) == 0 ? (unsigned)((lane & 3) * 16 + (lane >> 5) * 2) : OOB, (b * DM + col0) * 2, 0);
    if (blk == 0) st64(tg, rws, lane == 0 ? 0u : OOB, WS_E);
    MARK(8);
    FLUSH_STAMPS();
    if (DIAG && blk == DIAG_BLK) { while (__builtin_amdgcn_s_memrealtime() - t0s < tmark - t0s + 10000) __builtin_amdgcn_s_sleep(1); }
    return;
  }

  const int a = w - 4;
  const rsrc_t rx = mk_rsrc(p.x, NB * DM * 2);
  const unsigned xrow = ((lane >> 2) & 3) * (DM * 2);
  const bool mlp = h < 28;
  if (a == 0) {
    v4u xs[4], xa[2], T[16];
#pragma unroll
    for (int t = 0; t < 4; ++t) xs[t] = ld128<0>(rx, xrow + ((lane & 3) + 4 * r) * 16, t * 256);
#pragma unroll
    for (int s = 0; s < 2; ++s) xa[s] = ld128<0>(rx, xrow + r * 16, bs * 128 + s * 64);
    FENCE();
    const rsrc_t rq = mk_rsrc(p.wq + (size_t)bs * 64 * (NH * DH) + h * DH, 64 * NH * DH * 2);
#pragma unroll
    for (int i = 0; i < 16; ++i) T[i] = ld128<AUX_WQ>(rq, r * 8 * 8192 + c * 16, ((i >> 3) * 32 + (i & 7)) * 8192);
    const v2u ep = ld64<AUX_SC1>(rws, 0, WS_E);
    FENCE();
    MARK(10);
    float mean, inv;
    ln_stats(xs, mean, inv);
    const v4u A0 = ln_apply(xa[0], mean, inv), A1 = ln_apply(xa[1], mean, inv);
    float g[8];
    static_for<8>([&](auto J) { constexpr int j = J;
      f32x4 d = mfma(A0, bfrag<j>(T), f32x4{0.f, 0.f, 0.f, 0.f}); d = mfma(A1, bfrag<j>(T + 8), d); g[j] = d[0]; });
    FENCE();
    const unsigned tA = __builtin_amdgcn_readfirstlane(ep[0]) + 1u, tB = __builtin_amdgcn_readfirstlane(ep[1]) + 1u;
#pragma unroll
    for (int j = 0; j < 4; ++j)
      st128(gran2(g[2 * j], g[2 * j + 1], tA, tB), rws, r * 8192 + c * 16 + j * 256, WS_QX + h * 32768 + bs * 1024);
    MARK(11);
    DRAIN();
    MARK(1);
    float q0, q1;
#pragma unroll 1
    for (unsigned spin = 0; spin < SPIN_CAP; ++spin) {
      v4u G[8];
#pragma unroll
      for (int j = 0; j < 8; ++j) G[j] = ld128<AUX_SC1>(rws, l16, WS_QX + h * 32768 + b * 8192 + j * 1024);
      unsigned bad = 0; q0 = 0.f; q1 = 0.f;
#pragma unroll
      for (int j = 0; j < 8; ++j) bad |= take2(G[j], tA, tB, q0, q1);
      if (!__any(bad != 0u)) break;
      asm volatile("" ::: "memory");
    }
    *(unsigned*)(lds + L_Q + c * 16 + r * 4) = pk_bf16(q0, q1);
    if (lane == 0) { *(v4u*)(lds + L_M) = v4u{SENT, SENT, SENT, SENT}; *(v2u*)(lds + L_TAG) = v2u{tA, tB}; }
  }
  MARK(2);
  WG_BARRIER();
  MARK(3);
  const v2u tg = *(const v2u*)(lds + L_TAG);
  const unsigned tagA = __builtin_amdgcn_readfirstlane(tg[0]), tagB = __builtin_amdgcn_readfirstlane(tg[1]);
  if (a == 2 && mlp) {
    v4u xs[4], xa[2]; v2u U[16];
#pragma unroll
    for (int t = 0; t < 4; ++t) xs[t] = ld128<0>(rx, xrow + ((lane & 3) + 4 * r) * 16, t * 256);
#pragma unroll
    for (int s = 0; s < 2; ++s) xa[s] = ld128<0>(rx, xrow + r * 16, bs * 128 + s * 64);
    const rsrc_t r1 = mk_rsrc(p.w1 + (size_t)bs * 64 * FF + h * 64, 64 * FF * 2);
#pragma unroll
    for (int i = 0; i < 16; ++i) U[i] = ld64<AUX_W12>(r1, r * 8 * (FF * 2) + c * 8, ((i >> 3) * 32 + (i & 7)) * (FF * 2));
    FENCE();
#pragma unroll 1
    for (int it = 0; it < 2; ++it) {
      float mean, inv;
      ln_stats(xs, mean, inv);
#pragma unroll
      for (int t = 0; t < 4; ++t) xs[t] = ln_apply(xs[t], mean, inv);
      xa[0] = ln_apply(xa[0], mean, inv); xa[1] = ln_apply(xa[1], mean, inv);
    }
    st128(__builtin_bit_cast(v4u, tile64(xa[0], xa[1], U)), rws, l16, WS_X1 + h * 8192 + bs * 1024);
    DRAIN();
    st64(v2u{tagA, tagB}, rws, lane == 0 ? 0u : OOB, WS_XF + h * 128 + bs * 8);
  }
  MARK(4);
  v4u Bf[4][8];
  {
    const rsrc_t ro = mk_rsrc(p.wo + (size_t)h * DH * DM + a * 128, ABL_WO ? 0 : DH * DM * 2);
    const unsigned vo = r * 8 * 1024 + c * 16;
    v4u T[32];
    static_for<32 + DO>([&](auto N) { constexpr int n = N;
      if constexpr (n >= DO) TOUCH(T[n - DO]);
      FENCE();
      if constexpr (n < 32) T[n] = ld128<AUX_WO>(ro, vo, (((n >> 3) * 32 + (n & 7)) ^ rot) * 1024);
      FENCE();
      if constexpr (n >= DO && ((n - DO) & 7) == 7) { constexpr int s = (n - DO) >> 3;
        static_for<8>([&](auto J) { constexpr int j = J; Bf[s][j] = bfrag<j>(T + 8 * s); PIN(Bf[s][j]); }); }
      FENCE(); });
  }
  MARK(5);
  if (a == 3) {
    unsigned mb;
#pragma unroll 1
    for (unsigned spin = 0; spin < SPIN_CAP; ++spin) {
      mb = __hip_atomic_load((lds_u32*)lds + L_M / 4 + (lane & 3), __ATOMIC_RELAXED, __HIP_MEMORY_SCOPE_WORKGROUP);
      if (__all(mb != SENT)) break;
      __builtin_amdgcn_s_sleep(4);
    }
    asm volatile("" ::: "memory");
    const float m4 = __uint_as_float(mb);
    float M = fmaxf(m4, dpp<XOR1>(m4)); M = fmaxf(M, dpp<XOR2>(M));
    float L = 0.f;
#pragma unroll
    for (int i = 0; i < 8; ++i) { const f32x4 s4 = *(const f32x4*)(lds + L_S + i * 1024 + l16);
#pragma unroll
      for (int j = 0; j < 4; ++j) L += __builtin_amdgcn_exp2f(s4[j] - M); }
    L = wave_sum(L);
    const float lse = M + __builtin_amdgcn_logf(L);
    st128(v4u{__float_as_uint(lse), tagA, 0u, tagB}, rws, lane == 0 ? 0u : OOB, WS_LS + (b * 64 + 2 * h + sp) * 64);
    MARK(6);
    v4u m;
#pragma unroll 1
    for (unsigned spin = 0; spin < SPIN_CAP; ++spin) {
      __builtin_amdgcn_s_sleep(100);
      m = ld128<AUX_SC1>(rws, 0, WS_LS + (b * 64 + 2 * h + (sp ^ 1)) * 64);
      if (!__any(((m[1] ^ tagA) | (m[3] ^ tagB)) != 0u)) break;
      asm volatile("" ::: "memory");
    }
    *(float*)(lds + L_F + 4 * (lane & 3)) = __builtin_amdgcn_exp2f(m4 - M) * __builtin_amdgcn_rcpf(L) * __builtin_amdgcn_rcpf(1.0f + __builtin_amdgcn_exp2f(__uint_as_float(m[0]) - lse));
  } else if (a == 2) {
    const unsigned co = (lane & 3) * 16 + (lane >> 5) * 2;
    const unsigned b2v = __builtin_amdgcn_raw_buffer_load_b16(mk_rsrc(p.b2, DM * 2), co, col0 * 2, 0);
    const unsigned xv = __builtin_amdgcn_raw_buffer_load_b16(rx, co, (b * DM + col0) * 2, 0);
    if (mlp) {
      v2u U[16];
      const rsrc_t r2 = mk_rsrc(p.w2 + (size_t)h * 64 * DM + bs * 64, 64 * DM * 2);
#pragma unroll
      for (int i = 0; i < 16; ++i) U[i] = ld64<AUX_W12>(r2, r * 8 * 1024 + (c & 7) * 16 + (c >> 3) * 8, ((i >> 3) * 32 + (i & 7)) * 1024);
      const v2u b1v = ld64<0>(mk_rsrc(p.b1 + h * 64, 128), c * 8, 0);
      FENCE();
#pragma unroll 1
      for (unsigned spin = 0; spin < SPIN_CAP; ++spin) {
        const v2u f = ld64<AUX_SC1>(rws, (lane & 7) * 8, WS_XF + h * 128);
        if (!__any(((f[0] ^ tagA) | (f[1] ^ tagB)) != 0u)) break;
        __builtin_amdgcn_s_sleep(32);
        asm volatile("" ::: "memory");
      }
      asm volatile("" ::: "memory");
      f32x4 z4 = {0.f, 0.f, 0.f, 0.f};
      v4u G[8];
#pragma unroll
      for (int j = 0; j < 8; ++j) G[j] = ld128<AUX_SC1>(rws, l16, WS_X1 + h * 8192 + j * 1024);
#pragma unroll
      for (int j = 0; j < 8; ++j) z4 += __builtin_bit_cast(f32x4, G[j]);
      float hv[4];
#pragma unroll
      for (int j = 0; j < 4; ++j) {
        const float z = rbf(rbf(z4[j]) + ((j & 1) ? bfhi(b1v[j >> 1]) : bflo(b1v[j >> 1])));
        const float e2 = __builtin_amdgcn_exp2f((-2.0f * 0.7978845608028654f * 1.4426950408889634f) * (z + 0.044715f * z * z * z));
        hv[j] = z * __builtin_amdgcn_rcpf(1.0f + e2);
      }
      *(v2u*)(lds + L_H + r * 128 + c * 8) = v2u{pk_bf16(hv[0], hv[1]), pk_bf16(hv[2], hv[3])};
      LDS_WAIT();
      const v4u A0 = *(const v4u*)(lds + L_H + ((lane >> 2) & 3) * 128 + r * 16), A1 = *(const v4u*)(lds + L_H + ((lane >> 2) & 3) * 128 + r * 16 + 64);
      const f32x4 o = tile64(A0, A1, U);
#pragma unroll
      for (int j = 0; j < 2; ++j)
        st128(gran2(o[2 * j], o[2 * j + 1], tagA, tagB), rws, r * (28 * 4096) + (c >> 3) * 256 + (c & 7) * 16 + j * 512,
                                               WS_MP + h * 4096 + (bs >> 1) * 1024 + (bs & 1) * 128);
    } else __builtin_amdgcn_s_sleep(127);
    MARK(6);
    const unsigned go = (lane >> 2) * 4096 + (lane & 3) * 16;
    float m0, m1;
#pragma unroll 1
    for (unsigned spin = 0; spin < SPIN_CAP; ++spin) {
      __builtin_amdgcn_s_sleep(127);
      const v4u G0 = ld128<AUX_SC1>(rws, go, WS_MP + b * (28 * 4096) + cons * 64);
      const v4u G1 = ld128<AUX_SC1>(rws, lane < 48 ? go : OOB, WS_MP + b * (28 * 4096) + 65536 + cons * 64);
      m0 = 0.f; m1 = 0.f;
      const unsigned bad0 = take2(G0, tagA, tagB, m0, m1), bad1 = take2(G1, tagA, tagB, m0, m1);
      if (!__any((bad0 | (lane < 48 ? bad1 : 0u)) != 0u)) break;
      asm volatile("" ::: "memory");
    }
    const float m = rbf(rbf(sum2_quadlane(m0, m1)) + bflo(b2v));
    const float gate = rbf(m * __builtin_amdgcn_rcpf(1.0f + __builtin_amdgcn_exp2f(-1.4426950408889634f * m)));
    *(f32x2*)(lds + L_GX + ((lane & 3) * 2 + (lane >> 5)) * 8) = f32x2{gate, bflo(xv)};
  }
  MARK(9);
  WG_BARRIER();
  MARK(7);
  {
    v4u A[4];
#pragma unroll
    for (int s = 0; s < 4; ++s) A[s] = *(const v4u*)(lds + L_O + (lane & 7) * 256 + s * 64 + r * 16);
    const f32x4 f = *(const f32x4*)(lds + L_F);
    float t[8];
#pragma unroll
    for (int j = 0; j < 8; ++j) {
      f32x4 d = mfma(A[0], Bf[0][j], f32x4{0.f, 0.f, 0.f, 0.f});
#pragma unroll
      for (int s = 1; s < 4; ++s) d = mfma(A[s], Bf[s][j], d);
      t[j] = fmaf(d[3], f[3], d[2] * f[2]) + fmaf(d[1], f[1], d[0] * f[0]);
    }
    const bool up = lane >= 32;
    const float u0 = tr16(up ? t[2] : t[0], up ? t[6] : t[4]), u1 = tr16(up ? t[3] : t[1], up ? t[7] : t[5]);
    st128(gran2(u0, u1, tagA, tagB), rws, l16, WS_TB + (b * 64 + 2 * h + sp) * 4096 + a * 1024);
  }
  MARK(8);
  FLUSH_STAMPS();
}

extern "C" void kernel_launch(void* const* d_in, const int* in_sizes, int n_in, void* d_out, int out_size, void* d_ws, size_t ws_size,
                              hipStream_t stream) {
  Params p;
  p.x = (const unsigned short*)d_in[0]; p.k = (const unsigned short*)d_in[1]; p.v = (const unsigned short*)d_in[2];
  p.wq = (const unsigned short*)d_in[3]; p.wo = (const unsigned short*)d_in[4];
  p.w1 = (const unsigned short*)d_in[6]; p.b1 = (const unsigned short*)d_in[7]; p.w2 = (const unsigned short*)d_in[8]; p.b2 = (const unsigned short*)d_in[9];
  p.out = (unsigned short*)d_out;
  p.ws = (unsigned char*)d_ws;
  k_fused<<<256, 512, 0, stream>>>(p);
}
```

```cpp
#include <hip/hip_runtime.h>
#include <hip/hip_bf16.h>
#include <stdint.h>
#include <utility>

#define P_D 6
#define P_D0 6
#define P_N0 0
#define P_NA 40
#define P_DO 3
#define AUX_KV 2
#define AUX_WQ 2
#define AUX_WO 0
#define AUX_W12 2
#define P_BAND 5
#define P_KFIX 2
#define P_TAUV 215
#define P_OFF 110
#define P_WAIT 100
#define P_PD 20
#define P_PS 1
#define P_DRY 0
#define P_QD 0
#define P_QS 0
#define P_WD 0
#define P_WD2 0
#define P_MD 0
#define P_TLB 0
#define ABL 0
#define DIAG 0
#define DIAG_BLK 0

typedef unsigned v4u __attribute__((ext_vector_type(4)));
typedef unsigned v2u __attribute__((ext_vector_type(2)));
typedef float f32x4 __attribute__((ext_vector_type(4)));
typedef float f32x2 __attribute__((ext_vector_type(2)));
typedef __bf16 bf16x2 __attribute__((ext_vector_type(2)));
typedef __bf16 bf16x8 __attribute__((ext_vector_type(8)));
typedef __amdgpu_buffer_rsrc_t rsrc_t;
typedef __attribute__((address_space(3))) unsigned lds_u32;

constexpr int NB = 4, DM = 512, NH = 32, SK = 4096, DH = 128, FF = 1792;
constexpr int AUX_SC1 = 16;
constexpr unsigned SENT = 0xFFFFFFFFu, OOB = 0x80000000u;
constexpr unsigned SPIN_CAP = 1u << 14;

template <class F, int... I> __device__ __forceinline__ void static_for_impl(F&& f, std::integer_sequence<int, I...>) { (f(std::integral_constant<int, I>{}), ...); }
template <int N, class F> __device__ __forceinline__ void static_for(F&& f) { static_for_impl(f, std::make_integer_sequence<int, N>{}); }

__device__ __forceinline__ float dot2(unsigned a, unsigned b, float acc) {
  return __builtin_amdgcn_fdot2_f32_bf16(__builtin_bit_cast(bf16x2, a), __builtin_bit_cast(bf16x2, b), acc, false);
}
__device__ __forceinline__ f32x4 mfma(v4u a, v4u b, f32x4 c) {
  return __builtin_amdgcn_mfma_f32_16x16x32_bf16(__builtin_bit_cast(bf16x8, a), __builtin_bit_cast(bf16x8, b), c, 0, 0, 0);
}
__device__ __forceinline__ float bflo(unsigned u) { return __uint_as_float(u << 16); }
__device__ __forceinline__ float bfhi(unsigned u) { return __uint_as_float(u & 0xffff0000u); }
__device__ __forceinline__ unsigned pk_bf16(float lo, float hi) { bf16x2 r; r.x = (__bf16)lo; r.y = (__bf16)hi; return __builtin_bit_cast(unsigned, r); }
__device__ __forceinline__ unsigned bf_bits(float x) { return (unsigned)__builtin_bit_cast(unsigned short, (__bf16)x); }
__device__ __forceinline__ float rbf(float x) { return bflo(bf_bits(x)); }
__device__ __forceinline__ unsigned pair_lo(unsigned e, unsigned o) { return __builtin_amdgcn_perm(o, e, 0x05040100u); }
__device__ __forceinline__ unsigned pair_hi(unsigned e, unsigned o) { return __builtin_amdgcn_perm(o, e, 0x07060302u); }
template <int CTRL> __device__ __forceinline__ float dpp(float x) { return __int_as_float(__builtin_amdgcn_mov_dpp(__float_as_int(x), CTRL, 0xf, 0xf, true)); }
constexpr int XOR1 = 0xB1, XOR2 = 0x4E, XOR7 = 0x141, XOR8 = 0x128, ROR4 = 0x124, BCAST = 0x150;
__device__ __forceinline__ float tr32(float a, float b) {
  auto s = __builtin_amdgcn_permlane32_swap(__float_as_uint(a), __float_as_uint(b), false, false);
  return __uint_as_float(s[0]) + __uint_as_float(s[1]);
}
__device__ __forceinline__ float tr16(float a, float b) {
  auto s = __builtin_amdgcn_permlane16_swap(__float_as_uint(a), __float_as_uint(b), false, false);
  return __uint_as_float(s[0]) + __uint_as_float(s[1]);
}
template <int CTRL> __device__ __forceinline__ float trd(float lo, float hi, bool up) {
  const float t1 = lo + dpp<CTRL>(lo), t2 = hi + dpp<CTRL>(hi);
  return up ? t2 : t1;
}
__device__ __forceinline__ float wave_sum(float x) {
  x += dpp<XOR1>(x); x += dpp<XOR2>(x); x += dpp<XOR7>(x); x += dpp<XOR8>(x);
  x = tr16(x, x); x = tr32(x, x);
  return x;
}
__device__ __forceinline__ float wave_max(float x) {
  x = fmaxf(x, dpp<XOR1>(x)); x = fmaxf(x, dpp<XOR2>(x)); x = fmaxf(x, dpp<XOR7>(x)); x = fmaxf(x, dpp<XOR8>(x));
  auto s = __builtin_amdgcn_permlane16_swap(__float_as_uint(x), __float_as_uint(x), false, false);
  x = fmaxf(__uint_as_float(s[0]), __uint_as_float(s[1]));
  auto t = __builtin_amdgcn_permlane32_swap(__float_as_uint(x), __float_as_uint(x), false, false);
  return fmaxf(__uint_as_float(t[0]), __uint_as_float(t[1]));
}
__device__ __forceinline__ float sum4_parity(f32x4 v) {
  float s = tr16(tr32(v[0], v[2]), tr32(v[1], v[3]));
  s += dpp<XOR8>(s); s += dpp<ROR4>(s); s += dpp<XOR2>(s);
  return s;
}
__device__ __forceinline__ v4u tag4(f32x4 v, unsigned tag) {
  return ((__builtin_bit_cast(v4u, v) + 0x800u) & 0xfffff000u) | (tag & 0xfffu);
}
__device__ __forceinline__ unsigned take4(v4u g, unsigned tag, f32x4& acc) {
  acc += __builtin_bit_cast(f32x4, g & 0xfffff000u);
  return (g[0] ^ tag) | (g[1] ^ tag) | (g[2] ^ tag) | (g[3] ^ tag);
}
__device__ __forceinline__ rsrc_t mk_rsrc(const void* p, unsigned bytes) { return __builtin_amdgcn_make_buffer_rsrc(const_cast<void*>(p), 0, bytes, 0x00020000); }
template <int AUX> __device__ __forceinline__ v4u ld128(rsrc_t r, unsigned v, unsigned s) { return __builtin_amdgcn_raw_buffer_load_b128(r, v, s, AUX); }
template <int AUX> __device__ __forceinline__ v2u ld64(rsrc_t r, unsigned v, unsigned s) { return __builtin_amdgcn_raw_buffer_load_b64(r, v, s, AUX); }
__device__ __forceinline__ void st128(v4u d, rsrc_t r, unsigned v, unsigned s) { __builtin_amdgcn_raw_buffer_store_b128(d, r, v + s, 0, AUX_SC1); }
__device__ __forceinline__ void st64(v2u d, rsrc_t r, unsigned v, unsigned s) { __builtin_amdgcn_raw_buffer_store_b64(d, r, v + s, 0, AUX_SC1); }
__device__ __forceinline__ float rows4_sum(float x) { x += dpp<XOR1>(x); x += dpp<XOR2>(x); x = tr16(x, x); x = tr32(x, x); return x; }
__device__ __forceinline__ void ln_stats(const v4u (&x)[4], float& mean, float& inv) {
  float s = 0.f;
#pragma unroll
  for (int t = 0; t < 4; ++t)
#pragma unroll
    for (int j = 0; j < 4; ++j) s += bflo(x[t][j]) + bfhi(x[t][j]);
  const float mu = rows4_sum(s) * (1.0f / DM);
  float q = 0.f;
#pragma unroll
  for (int t = 0; t < 4; ++t)
#pragma unroll
    for (int j = 0; j < 4; ++j) { const float d0 = bflo(x[t][j]) - mu, d1 = bfhi(x[t][j]) - mu; q = fmaf(d0, d0, q); q = fmaf(d1, d1, q); }
  const float var = rows4_sum(q) * (1.0f / DM);
  mean = rbf(mu);
  inv = __builtin_amdgcn_rcpf(rbf(__builtin_amdgcn_sqrtf(rbf(rbf(var) + 1e-5f))));
}
__device__ __forceinline__ v4u ln_apply(v4u x, float mean, float inv) {
  v4u o;
#pragma unroll
  for (int j = 0; j < 4; ++j) { const unsigned d = pk_bf16(bflo(x[j]) - mean, bfhi(x[j]) - mean); o[j] = pk_bf16(bflo(d) * inv, bfhi(d) * inv); }
  return o;
}
template <int J> __device__ __forceinline__ v4u bfrag(const v4u* t) {
  v4u f;
#pragma unroll
  for (int d = 0; d < 4; ++d) f[d] = (J & 1) ? pair_hi(t[2 * d][J >> 1], t[2 * d + 1][J >> 1]) : pair_lo(t[2 * d][J >> 1], t[2 * d + 1][J >> 1]);
  return f;
}
template <int J> __device__ __forceinline__ v4u bfrag2(const v2u* t) {
  v4u f;
#pragma unroll
  for (int d = 0; d < 4; ++d) f[d] = (J & 1) ? pair_hi(t[2 * d][J >> 1], t[2 * d + 1][J >> 1]) : pair_lo(t[2 * d][J >> 1], t[2 * d + 1][J >> 1]);
  return f;
}
__device__ __forceinline__ f32x4 tile64(v4u a0, v4u a1, const v2u* t) {
  f32x4 o;
  static_for<4>([&](auto J) { constexpr int j = J;
    f32x4 d = mfma(a0, bfrag2<j>(t), f32x4{0.f, 0.f, 0.f, 0.f}); d = mfma(a1, bfrag2<j>(t + 8), d); o[j] = d[0]; });
  return o;
}
#define TOUCH(x) asm volatile("" :: "v"(x))
#define PIN(x) asm volatile("" : "+v"(x))
#define THROTTLE(mask, i) asm volatile("s_bitcmp1_b32 %0, %1\n\ts_cbranch_scc0 1f\n\ts_waitcnt vmcnt(" "3" ")\n1:" :: "s"(mask), "n"(i) : "scc")
#define FENCE() __builtin_amdgcn_sched_barrier(0)
#define WG_BARRIER() asm volatile("s_waitcnt lgkmcnt(0)\n\ts_barrier" ::: "memory")
#define DRAIN() asm volatile("s_waitcnt vmcnt(0)" ::: "memory")
#define LDS_WAIT() asm volatile("s_waitcnt lgkmcnt(0)" ::: "memory")
#define MARK(k) do { if (DIAG == (k) + 1) { FENCE(); tmark = __builtin_amdgcn_s_memrealtime(); FENCE(); } } while (0)
#define FLUSH_STAMPS()

constexpr unsigned WS_E = 0;
constexpr unsigned WS_LS = 4096;
constexpr unsigned WS_X1 = 65536;
constexpr unsigned WS_MP = WS_X1 + 32 * 8192;
constexpr unsigned WS_TB = WS_MP + 4 * 32 * 2048;
constexpr unsigned WS_QX = WS_TB + 4 * 64 * 2048;
constexpr unsigned WS_BYTES = WS_QX + 32 * 4 * 8 * 512;

struct Params {
  const unsigned short* x; const unsigned short* k; const unsigned short* v; const unsigned short* wq; const unsigned short* wo;
  const unsigned short* w1; const unsigned short* b1; const unsigned short* w2; const unsigned short* b2;
  unsigned short* out; unsigned char* ws;
};

constexpr int D = P_D, NA = P_NA, DO = P_DO;
constexpr int LD = 8;
constexpr int NU = (NA <= 48 + LD - 12) ? 48 : 64;
static_assert((NU + LD - NA) * 4 >= NU && D <= LD, "the backlog is gone before the loop starts");
constexpr float C2 = 0.088388347648318440f * 1.4426950408889634f;

constexpr int L_Q = 0, L_M = 256, L_F = 272, L_TAG = 288, L_REF = 292, L_T = 304, L_GX = 320, L_H = 1024, L_O = 1536, O_ROW = 272, L_S = L_O + 8 * O_ROW, L_END = L_S + 8192;

__global__ void __launch_bounds__(512, 2) k_fused(Params p) {
  __shared__ __attribute__((aligned(16))) unsigned char lds[L_END];
  unsigned long long t0s = 0, tmark = 0;
  if (DIAG) { t0s = __builtin_amdgcn_s_memrealtime(); tmark = t0s; }
  const int blk = blockIdx.x, tid = threadIdx.x, lane = tid & 63, w = __builtin_amdgcn_readfirstlane(tid >> 6);
  MARK(0);
  asm volatile("" :: "s"(p.x), "s"(p.k), "s"(p.v), "s"(p.wq), "s"(p.wo), "s"(p.w1), "s"(p.b1), "s"(p.w2), "s"(p.b2), "s"(p.out), "s"(p.ws));
  const int idx = blk >> 3, h = (blk & 7) * 4 + (idx & 3), bs = idx >> 2, b = bs >> 1, sp = bs & 1;
  const int cons = (blk & 7) * 8 + (idx & 7);
  const int col0 = 128 * (cons >> 4) + 16 * (cons & 7) + 4 * ((cons >> 3) & 1);
  const int r = lane >> 4, c = lane & 15;
  const unsigned l16 = lane * 16;
  const rsrc_t rws = mk_rsrc(p.ws, WS_BYTES);
  const unsigned rot = (bs >> 1) * 32 + (bs & 1) * 4;

  if (__builtin_expect(w < 4, 1)) {
    __builtin_amdgcn_s_setprio(2);
    const size_t kvoff = ((size_t)(b * NH + h) * SK + sp * 2048 + w * 4) * DH;
    const unsigned short* kp = p.k + kvoff; const unsigned short* vp = p.v + kvoff;
    const unsigned kvlen = 524288 - w * 1024;
    const rsrc_t rk = mk_rsrc(kp, kvlen);
    float* const scs = (float*)(lds + L_S + w * 2048) + lane;
    v4u S[NU + LD], q;
    auto pace = [&](int lv) {
      unsigned m = 0xffffu; m = lv > 0 ? 0x5555u : m; m = lv > 1 ? 0x9249u : m; m = lv > 2 ? 0x1111u : m;
      m = lv > 3 ? 0x0421u : m; m = lv > 4 ? 0x0101u : m; m = lv > 5 ? 0u : m;
      return (unsigned)__builtin_amdgcn_readfirstlane(m); };
    float ka[16], mw = -INFINITY;
    auto kdot = [&](v4u t, int slot) {
      float d = dot2(t[0], q[0], 0.f); d = dot2(t[1], q[1], d); d = dot2(t[2], q[2], d); d = dot2(t[3], q[3], d); PIN(d); ka[slot] = d; };
    auto kfold = [&](int g) {
#pragma unroll
      for (int j = 0; j < 8; ++j) ka[j] = trd<XOR8>(ka[j], ka[j + 8], lane & 8);
#pragma unroll
      for (int j = 0; j < 4; ++j) ka[j] = trd<XOR7>(ka[j], ka[j + 4], lane & 4);
#pragma unroll
      for (int j = 0; j < 2; ++j) ka[j] = trd<XOR2>(ka[j], ka[j + 2], lane & 2);
      const float s = trd<XOR1>(ka[0], ka[1], lane & 1) * C2;
      scs[g * 64] = s; mw = fmaxf(mw, s); PIN(mw); };
    static_for<NU + LD>([&](auto N) {
      constexpr int n = N;
      constexpr int dn = n < P_N0 ? P_D0 : D;
      if constexpr (n >= dn) TOUCH(S[n - dn]);
      FENCE();
      if constexpr (n == (P_N0 ? P_D0 : D)) MARK(1);
      S[n] = ld128<AUX_KV>(rk, l16, n * 4096);
      FENCE();
      if constexpr (n == NA - 1) {
        MARK(2);
        WG_BARRIER();
        MARK(3);
        q = *(const v4u*)(lds + L_Q + c * 16);
      }
      if constexpr (n >= NA) {
        constexpr int hi = (n - NA + 1) * 4 < n - LD + 1 ? (n - NA + 1) * 4 : n - LD + 1;
        constexpr int lo = (n - NA) * 4 < n - LD ? (n - NA) * 4 : n - LD;
        static_for<hi - lo>([&](auto J) { constexpr int i = lo + J; kdot(S[i], i & 15); if constexpr ((i & 15) == 15) kfold(i >> 4); });
      }
      FENCE();
    });
    v4u R[16];
#pragma unroll
    for (int i = 0; i < LD; ++i) R[i] = S[NU + i];
    float snext = scs[0];
#pragma unroll 1
    for (int g = NU / 16; g < 8; ++g) {
      const rsrc_t rn = mk_rsrc(g == 7 ? vp : kp, kvlen);
      const unsigned o0 = g * 65536, o1 = g == 7 ? 0u : o0 + 65536;
      const unsigned mask = pace(P_KFIX);
      static_for<16>([&](auto I) { constexpr int i = I;
        TOUCH(R[i]); THROTTLE(mask, i); FENCE();
        if constexpr (i + LD < 16) R[i + LD] = ld128<AUX_KV>(rk, l16, o0 + (i + LD) * 4096);
        else R[i + LD - 16] = ld128<AUX_KV>(rn, l16, o1 + (i + LD - 16) * 4096);
        FENCE();
        kdot(R[i], i);
        FENCE(); });
      kfold(g);
    }
    MARK(4);
    unsigned now = (unsigned)__builtin_amdgcn_s_memrealtime();
    mw = wave_max(mw);
    *(unsigned*)(lds + L_T + 4 * w) = now;
    unsigned refv = 0;
    asm volatile("" ::: "memory");
    __hip_atomic_store((lds_u32*)lds + L_M / 4 + w, min(__float_as_uint(mw), SENT - 1u), __ATOMIC_RELAXED, __HIP_MEMORY_SCOPE_WORKGROUP);
    float acc[8];
#pragma unroll
    for (int j = 0; j < 8; ++j) acc[j] = 0.f;
    const rsrc_t rv = mk_rsrc(vp, kvlen);
    MARK(5);
#pragma unroll 1
    for (int g = 0; g < 8; ++g) {
      const rsrc_t rn = mk_rsrc(vp, g == 7 ? 0u : kvlen);
      const unsigned o0 = g * 65536;
      const float pg = __builtin_amdgcn_exp2f(snext - mw);
      snext = scs[((g + 1) & 7) * 64];
      const unsigned rf = __builtin_amdgcn_readfirstlane(refv);
      const unsigned mask = pace(rf ? ((int)(now - rf) - g * P_TAUV) >> P_BAND : P_KFIX) & (g == 7 ? 0xffu : 0xffffu);
      static_for<16>([&](auto I) { constexpr int i = I;
        TOUCH(R[i]); THROTTLE(mask, i); FENCE();
        R[(i + LD) & 15] = ld128<AUX_KV>(i + LD < 16 ? rv : rn, l16, o0 + (i + LD) * 4096);
        if constexpr (i == 15) { now = (unsigned)__builtin_amdgcn_s_memrealtime();
          refv = __hip_atomic_load((lds_u32*)lds + L_REF / 4, __ATOMIC_RELAXED, __HIP_MEMORY_SCOPE_WORKGROUP); }
        FENCE();
        const v4u t = R[i]; const float pb = dpp<BCAST + i>(pg);
#pragma unroll
        for (int j = 0; j < 4; ++j) { acc[2 * j] = fmaf(pb, bflo(t[j]), acc[2 * j]); acc[2 * j + 1] = fmaf(pb, bfhi(t[j]), acc[2 * j + 1]); }
        asm volatile("" : "+v"(acc[0]), "+v"(acc[1]), "+v"(acc[2]), "+v"(acc[3]), "+v"(acc[4]), "+v"(acc[5]), "+v"(acc[6]), "+v"(acc[7]));
        FENCE(); });
    }
    MARK(6);
    {
      float t4[4];
#pragma unroll
      for (int j = 0; j < 4; ++j) t4[j] = tr32(acc[j], acc[j + 4]);
      const float o0 = tr16(t4[0], t4[2]), o1 = tr16(t4[1], t4[3]);
      const unsigned hi = pk_bf16(o0, o1), lo = pk_bf16(o0 - bflo(hi), o1 - bfhi(hi));
      *(unsigned*)(lds + L_O + w * O_ROW + ((c * 16 + r * 4) ^ (rot * 2))) = hi;
      *(unsigned*)(lds + L_O + (4 + w) * O_ROW + ((c * 16 + r * 4) ^ (rot * 2))) = lo;
    }
    WG_BARRIER();
    MARK(7);
    FLUSH_STAMPS();
    return;
  }

  const int a = w - 4;
  const rsrc_t rx = mk_rsrc(p.x, NB * DM * 2);
  const unsigned xrow = ((lane >> 2) & 3) * (DM * 2);
  const bool mlp = h < 28 && !(ABL & 2);
  if (P_TLB && a == 1) {
    const unsigned short* tp[8] = {p.v + ((size_t)(b * NH + h) * SK + sp * 2048) * DH, p.wo + (size_t)h * DH * DM, p.w1 + (size_t)bs * 64 * FF, p.w1 + (size_t)(bs * 64 + 63) * FF,
                                   p.w2 + (size_t)(h < 28 ? h : 0) * 64 * DM, p.b1, p.b2, p.out};
    if (P_TLB > 1) __builtin_amdgcn_s_sleep(P_TLB);
    unsigned t0, t1, t2, t3, t4, t5, t6, t7;
    asm volatile("s_load_dword %0, %8, 0x0\n\ts_load_dword %1, %9, 0x0\n\ts_load_dword %2, %10, 0x0\n\ts_load_dword %3, %11, 0x0\n\t"
                 "s_load_dword %4, %12, 0x0\n\ts_load_dword %5, %13, 0x0\n\ts_load_dword %6, %14, 0x0\n\ts_load_dword %7, %15, 0x0\n\ts_waitcnt lgkmcnt(0)"
                 : "=&s"(t0), "=&s"(t1), "=&s"(t2), "=&s"(t3), "=&s"(t4), "=&s"(t5), "=&s"(t6), "=&s"(t7)
                 : "s"(tp[0]), "s"(tp[1]), "s"(tp[2]), "s"(tp[3]), "s"(tp[4]), "s"(tp[5]), "s"(tp[6]), "s"(tp[7]));
  }
  if (a == 0) {
    v4u xs[4], xa[2], T[16];
#pragma unroll
    for (int t = 0; t < 4; ++t) xs[t] = ld128<0>(rx, xrow + ((lane & 3) + 4 * r) * 16, t * 256);
#pragma unroll
    for (int s = 0; s < 2; ++s) xa[s] = ld128<0>(rx, xrow + r * 16, bs * 128 + s * 64);
    FENCE();
    const rsrc_t rq = mk_rsrc(p.wq + (size_t)bs * 64 * (NH * DH) + h * DH, (ABL & 8) ? 0 : 64 * NH * DH * 2);
#pragma unroll
    for (int i = 0; i < 16; ++i) T[i] = ld128<AUX_WQ>(rq, r * 8 * 8192 + c * 16, ((i >> 3) * 32 + (i & 7)) * 8192);
    const unsigned ep = __builtin_amdgcn_raw_buffer_load_b32(rws, 0, WS_E, AUX_SC1);
    FENCE();
    MARK(10);
    float mean, inv;
    ln_stats(xs, mean, inv);
    const v4u A0 = ln_apply(xa[0], mean, inv), A1 = ln_apply(xa[1], mean, inv);
    f32x4 g[2];
    static_for<8>([&](auto J) { constexpr int j = J;
      f32x4 d = mfma(A0, bfrag<j>(T), f32x4{0.f, 0.f, 0.f, 0.f}); d = mfma(A1, bfrag<j>(T + 8), d); g[j >> 2][j & 3] = d[0]; });
    FENCE();
    const unsigned tg = __builtin_amdgcn_readfirstlane(ep) + 1u;
#pragma unroll
    for (int j = 0; j < 2; ++j) st128(tag4(g[j], tg), rws, (ABL & 4) ? OOB : r * 4096 + c * 16 + j * 256, WS_QX + h * 16384 + bs * 512);
    MARK(11);
    DRAIN();
    MARK(1);
    if (P_QD) __builtin_amdgcn_s_sleep(P_QD);
    f32x4 q4;
#pragma unroll 1
    for (unsigned spin = 0; spin < SPIN_CAP; ++spin) {
      v4u G[4];
#pragma unroll
      for (int j = 0; j < 4; ++j) G[j] = ld128<AUX_SC1>(rws, (ABL & 4) ? OOB : l16, WS_QX + h * 16384 + b * 4096 + j * 1024);
      unsigned bad = 0; q4 = f32x4{0.f, 0.f, 0.f, 0.f};
#pragma unroll
      for (int j = 0; j < 4; ++j) bad |= take4(G[j], tg, q4);
      if (!__any((bad & 0xfffu) != 0u) || (ABL & 4)) break;
      if (P_QS) __builtin_amdgcn_s_sleep(P_QS);
      asm volatile("" ::: "memory");
    }
    *(unsigned*)(lds + L_Q + c * 16 + (r & 1) * 8 + (r >> 1) * 4) = pk_bf16(tr32(q4[0], q4[2]), tr32(q4[1], q4[3]));
    if (lane == 0) { *(v4u*)(lds + L_M) = v4u{SENT, SENT, SENT, SENT}; *(v2u*)(lds + L_TAG) = v2u{tg, 0u}; }
  }
  MARK(2);
  WG_BARRIER();
  MARK(3);
  const unsigned tag = __builtin_amdgcn_readfirstlane(*(const unsigned*)(lds + L_TAG));
  if (a == 2 && mlp) {
    v4u xs[4], xa[2]; v2u U[16];
#pragma unroll
    for (int t = 0; t < 4; ++t) xs[t] = ld128<0>(rx, xrow + ((lane & 3) + 4 * r) * 16, t * 256);
#pragma unroll
    for (int s = 0; s < 2; ++s) xa[s] = ld128<0>(rx, xrow + r * 16, bs * 128 + s * 64);
    const rsrc_t r1 = mk_rsrc(p.w1 + (size_t)bs * 64 * FF + h * 64, 64 * FF * 2);
#pragma unroll
    for (int i = 0; i < 16; ++i) U[i] = ld64<AUX_W12>(r1, r * 8 * (FF * 2) + c * 8, ((i >> 3) * 32 + (i & 7)) * (FF * 2));
    FENCE();
#pragma unroll 1
    for (int it = 0; it < 2; ++it) {
      float mean, inv;
      ln_stats(xs, mean, inv);
#pragma unroll
      for (int t = 0; t < 4; ++t) xs[t] = ln_apply(xs[t], mean, inv);
      xa[0] = ln_apply(xa[0], mean, inv); xa[1] = ln_apply(xa[1], mean, inv);
    }
    st128(tag4(tile64(xa[0], xa[1], U), tag), rws, l16, WS_X1 + h * 8192 + bs * 1024);
  }
  for (int i = 0; i < (a == 2 && mlp ? P_WD2 : P_WD); ++i) __builtin_amdgcn_s_sleep(127);
  MARK(4);
  v4u Bf[4][8];
  {
    const rsrc_t ro = mk_rsrc(p.wo + (size_t)h * DH * DM + a * 128, (ABL & 1) ? 0 : DH * DM * 2);
    const unsigned vo = r * 8 * 1024 + c * 16;
    v4u T[32];
    static_for<32 + DO>([&](auto N) { constexpr int n = N;
      if constexpr (n >= DO) TOUCH(T[n - DO]);
      FENCE();
      if constexpr (n < 32) T[n] = ld128<AUX_WO>(ro, vo, (((n >> 3) * 32 + (n & 7)) ^ rot) * 1024);
      FENCE();
      if constexpr (n >= DO && ((n - DO) & 7) == 7) { constexpr int s = (n - DO) >> 3;
        static_for<8>([&](auto J) { constexpr int j = J; Bf[s][j] = bfrag<j>(T + 8 * s); PIN(Bf[s][j]); }); }
      FENCE(); });
  }
  MARK(5);
  const unsigned co = r * 2 + (lane & 1) * 16;
  if (a == 3) {
    unsigned mb;
#pragma unroll 1
    for (unsigned spin = 0; spin < SPIN_CAP; ++spin) {
      mb = __hip_atomic_load((lds_u32*)lds + L_M / 4 + (lane & 3), __ATOMIC_RELAXED, __HIP_MEMORY_SCOPE_WORKGROUP);
      if (__all(mb != SENT)) break;
      __builtin_amdgcn_s_sleep(4);
    }
    asm volatile("" ::: "memory");
    const float m4 = __uint_as_float(mb);
    float M = fmaxf(m4, dpp<XOR1>(m4)); M = fmaxf(M, dpp<XOR2>(M));
    float L = 0.f;
#pragma unroll
    for (int i = 0; i < 8; ++i) { const f32x4 s4 = *(const f32x4*)(lds + L_S + i * 1024 + l16);
#pragma unroll
      for (int j = 0; j < 4; ++j) L += __builtin_amdgcn_exp2f(s4[j] - M); }
    L = wave_sum(L);
    const float lse = M + __builtin_amdgcn_logf(L);
    const unsigned tk = *(const unsigned*)(lds + L_T);
    st128(v4u{__float_as_uint(lse), tag, tk, tag}, rws, lane == 0 ? 0u : OOB, WS_LS + (b * 64 + 2 * h + sp) * 16);
    MARK(6);
    float lo;
#pragma unroll 1
    for (unsigned spin = 0; spin < SPIN_CAP; ++spin) {
      __builtin_amdgcn_s_sleep(P_WAIT);
      const v4u m = ld128<AUX_SC1>(rws, l16, WS_LS + b * 1024);
      const bool ok = ((m[1] ^ tag) | (m[3] ^ tag)) == 0u;
      const unsigned tn = (unsigned)__builtin_amdgcn_s_memrealtime();
      const float dt = wave_sum((float)(int)((ok ? m[2] : tn) - tk)) * (1.0f / 64);
      __hip_atomic_store((lds_u32*)lds + L_REF / 4, (tk + (int)dt - P_OFF) | 1u, __ATOMIC_RELAXED, __HIP_MEMORY_SCOPE_WORKGROUP);
      lo = __uint_as_float(__builtin_amdgcn_readlane(m[0], 2 * h + (sp ^ 1)));
      if ((__ballot(ok) >> (2 * h + (sp ^ 1))) & 1) break;
      asm volatile("" ::: "memory");
    }
    *(float*)(lds + L_F + 4 * (lane & 3)) = __builtin_amdgcn_exp2f(m4 - M) * __builtin_amdgcn_rcpf(L) * __builtin_amdgcn_rcpf(1.0f + __builtin_amdgcn_exp2f(lo - lse));
  } else if (a == 2) {
    const unsigned b2v = __builtin_amdgcn_raw_buffer_load_b16(mk_rsrc(p.b2, DM * 2), co, col0 * 2, 0);
    const unsigned xv = __builtin_amdgcn_raw_buffer_load_b16(rx, co, (b * DM + col0) * 2, 0);
    if (mlp) {
      v2u U[16];
      const rsrc_t r2 = mk_rsrc(p.w2 + (size_t)h * 64 * DM + bs * 64, 64 * DM * 2);
#pragma unroll
      for (int i = 0; i < 16; ++i) U[i] = ld64<AUX_W12>(r2, r * 8 * 1024 + c * 8, ((i >> 3) * 32 + (i & 7)) * 1024);
      const v2u b1v = ld64<0>(mk_rsrc(p.b1 + h * 64, 128), c * 8, 0);
      DRAIN();
      f32x4 z4;
#pragma unroll 1
      for (unsigned spin = 0; spin < SPIN_CAP; ++spin) {
        v4u G[8];
#pragma unroll
        for (int j = 0; j < 8; ++j) G[j] = ld128<AUX_SC1>(rws, l16, WS_X1 + h * 8192 + j * 1024);
        unsigned bad = 0; z4 = f32x4{0.f, 0.f, 0.f, 0.f};
#pragma unroll
        for (int j = 0; j < 8; ++j) bad |= take4(G[j], tag, z4);
        if (!__any((bad & 0xfffu) != 0u)) break;
        __builtin_amdgcn_s_sleep(32);
        asm volatile("" ::: "memory");
      }
      float hv[4];
#pragma unroll
      for (int j = 0; j < 4; ++j) {
        const float z = rbf(rbf(z4[j]) + ((j & 1) ? bfhi(b1v[j >> 1]) : bflo(b1v[j >> 1])));
        const float e2 = __builtin_amdgcn_exp2f((-2.0f * 0.7978845608028654f * 1.4426950408889634f) * (z + 0.044715f * z * z * z));
        hv[j] = z * __builtin_amdgcn_rcpf(1.0f + e2);
      }
      *(v2u*)(lds + L_H + r * 128 + c * 8) = v2u{pk_bf16(hv[0], hv[1]), pk_bf16(hv[2], hv[3])};
      LDS_WAIT();
      const v4u A0 = *(const v4u*)(lds + L_H + ((lane >> 2) & 3) * 128 + r * 16), A1 = *(const v4u*)(lds + L_H + ((lane >> 2) & 3) * 128 + r * 16 + 64);
      st128(tag4(tile64(A0, A1, U), tag), rws, r * (28 * 2048) + c * 16, WS_MP + h * 2048 + bs * 256);
    } else { __builtin_amdgcn_s_sleep(127); __builtin_amdgcn_s_sleep(64); }
    MARK(6);
    for (int i = 0; i < P_MD; ++i) __builtin_amdgcn_s_sleep(127);
    f32x4 m4;
#pragma unroll 1
    for (unsigned spin = 0; spin < SPIN_CAP; ++spin) {
      __builtin_amdgcn_s_sleep(127);
      const v4u G = ld128<AUX_SC1>(rws, lane < 56 ? (unsigned)((lane >> 1) * 2048 + (lane & 1) * 32) : OOB, WS_MP + b * (28 * 2048) + col0 * 4);
      m4 = f32x4{0.f, 0.f, 0.f, 0.f};
      const unsigned bad = take4(G, tag, m4);
      if (!__any(lane < 56 && (bad & 0xfffu) != 0u) || (ABL & 2)) break;
      asm volatile("" ::: "memory");
    }
    const float m = rbf(rbf(sum4_parity(m4)) + bflo(b2v));
    const float gate = rbf(m * __builtin_amdgcn_rcpf(1.0f + __builtin_amdgcn_exp2f(-1.4426950408889634f * m)));
    *(f32x2*)(lds + L_GX + co * 4) = f32x2{gate, bflo(xv)};
  }
  MARK(9);
#pragma unroll 1
  for (int pass = P_DRY ? 0 : 1; pass < 2; ++pass) {
    const unsigned dry = pass ? 0u : OOB;
    if (pass) { WG_BARRIER(); MARK(7); }
    v4u A[4];
#pragma unroll
    for (int s = 0; s < 4; ++s) A[s] = *(const v4u*)(lds + L_O + (lane & 7) * O_ROW + s * 64 + r * 16);
    const f32x4 f = *(const f32x4*)(lds + L_F);
    const f32x2 gx = *(const f32x2*)(lds + L_GX + co * 4);
    float t[8];
#pragma unroll
    for (int j = 0; j < 8; ++j) {
      f32x4 d = mfma(A[0], Bf[0][j], f32x4{0.f, 0.f, 0.f, 0.f});
#pragma unroll
      for (int s = 1; s < 4; ++s) d = mfma(A[s], Bf[s][j], d);
      t[j] = fmaf(d[3], f[3], d[2] * f[2]) + fmaf(d[1], f[1], d[0] * f[0]);
    }
    const f32x4 tv = {tr16(t[0], t[4]), tr16(t[1], t[5]), tr16(t[2], t[6]), tr16(t[3], t[7])};
    st128(tag4(tv, tag), rws, lane < 32 ? l16 | dry : OOB, WS_TB + (b * 64 + 2 * h + sp) * 2048 + a * 512);
    if (pass) MARK(10);
    if (a == 0) {
      const unsigned go = ((lane >> 1) * 2048 + (lane & 1) * 16) | dry;
      f32x4 s4;
      __builtin_amdgcn_s_sleep(P_PD);
      if (pass) MARK(11);
#pragma unroll 1
      for (unsigned spin = 0; spin < SPIN_CAP; ++spin) {
        const v4u G0 = ld128<AUX_SC1>(rws, go, WS_TB + b * 131072 + cons * 32), G1 = ld128<AUX_SC1>(rws, go, WS_TB + b * 131072 + 65536 + cons * 32);
        s4 = f32x4{0.f, 0.f, 0.f, 0.f};
        unsigned bad = take4(G0, tag, s4) | take4(G1, tag, s4);
        if (!__any((bad & 0xfffu) != 0u) || !pass) break;
        __builtin_amdgcn_s_sleep(P_PS);
        asm volatile("" ::: "memory");
      }
      const float o = gx[1] + rbf(rbf(sum4_parity(s4)) * gx[0]);
      __builtin_amdgcn_raw_buffer_store_b16((unsigned short)bf_bits(o), mk_rsrc(p.out, NB * DM * 2), (lane & 14) == 0 ? co | dry : OOB, (b * DM + col0) * 2, 0);
      __builtin_amdgcn_raw_buffer_store_b32(tag, rws, (blk == 0 && lane == 0) ? WS_E | dry : OOB, 0, AUX_SC1);
    }
  }
  MARK(8);
  FLUSH_STAMPS();
  if (DIAG && blk == DIAG_BLK && a == 0) { while (__builtin_amdgcn_s_memrealtime() - t0s < tmark - t0s + 10000) __builtin_amdgcn_s_sleep(1); }
}

extern "C" void kernel_launch(void* const* d_in, const int* in_sizes, int n_in, void* d_out, int out_size, void* d_ws, size_t ws_size,
                              hipStream_t stream) {
  Params p;
  p.x = (const unsigned short*)d_in[0]; p.k = (const unsigned short*)d_in[1]; p.v = (const unsigned short*)d_in[2];
  p.wq = (const unsigned short*)d_in[3]; p.wo = (const unsigned short*)d_in[4];
  p.w1 = (const unsigned short*)d_in[6]; p.b1 = (const unsigned short*)d_in[7]; p.w2 = (const unsigned short*)d_in[8]; p.b2 = (const unsigned short*)d_in[9];
  p.out = (unsigned short*)d_out;
  p.ws = (unsigned char*)d_ws;
  k_fused<<<256, 512, 0, stream>>>(p);
}
```

```cpp
#include <hip/hip_runtime.h>
#include <hip/hip_bf16.h>
#include <stdint.h>
#include <utility>

#define P_D 6
#define P_D0 6
#define P_N0 0
#define P_NA 40
#define P_DO 3
#define AUX_KV 2
#define AUX_WQ 2
#define AUX_WO 0
#define AUX_W12 2
#define P_BAND 5
#define P_KFIX 2
#define P_TAUV 217
#define P_TAUK 236
#define P_KAP 7
#define P_CK 0x104
#define P_OFF 50
#define P_WAIT 100
#define P_PD 20
#define P_PS 1
#define P_DRY 0
#define ABL 0
#define DIAG 0
#define DIAG_BLK 0

typedef unsigned v4u __attribute__((ext_vector_type(4)));
typedef unsigned v2u __attribute__((ext_vector_type(2)));
typedef float f32x4 __attribute__((ext_vector_type(4)));
typedef float f32x2 __attribute__((ext_vector_type(2)));
typedef __bf16 bf16x2 __attribute__((ext_vector_type(2)));
typedef __bf16 bf16x8 __attribute__((ext_vector_type(8)));
typedef __amdgpu_buffer_rsrc_t rsrc_t;
typedef __attribute__((address_space(3))) unsigned lds_u32;

constexpr int NB = 4, DM = 512, NH = 32, SK = 4096, DH = 128, FF = 1792;
constexpr int AUX_SC1 = 16;
constexpr unsigned SENT = 0xFFFFFFFFu, OOB = 0x80000000u;
constexpr unsigned SPIN_CAP = 1u << 14;

template <class F, int... I> __device__ __forceinline__ void static_for_impl(F&& f, std::integer_sequence<int, I...>) { (f(std::integral_constant<int, I>{}), ...); }
template <int N, class F> __device__ __forceinline__ void static_for(F&& f) { static_for_impl(f, std::make_integer_sequence<int, N>{}); }

__device__ __forceinline__ float dot2(unsigned a, unsigned b, float acc) {
  return __builtin_amdgcn_fdot2_f32_bf16(__builtin_bit_cast(bf16x2, a), __builtin_bit_cast(bf16x2, b), acc, false);
}
__device__ __forceinline__ f32x4 mfma(v4u a, v4u b, f32x4 c) {
  return __builtin_amdgcn_mfma_f32_16x16x32_bf16(__builtin_bit_cast(bf16x8, a), __builtin_bit_cast(bf16x8, b), c, 0, 0, 0);
}
__device__ __forceinline__ float bflo(unsigned u) { return __uint_as_float(u << 16); }
__device__ __forceinline__ float bfhi(unsigned u) { return __uint_as_float(u & 0xffff0000u); }
__device__ __forceinline__ unsigned pk_bf16(float lo, float hi) { bf16x2 r; r.x = (__bf16)lo; r.y = (__bf16)hi; return __builtin_bit_cast(unsigned, r); }
__device__ __forceinline__ unsigned bf_bits(float x) { return (unsigned)__builtin_bit_cast(unsigned short, (__bf16)x); }
__device__ __forceinline__ float rbf(float x) { return bflo(bf_bits(x)); }
__device__ __forceinline__ unsigned pair_lo(unsigned e, unsigned o) { return __builtin_amdgcn_perm(o, e, 0x05040100u); }
__device__ __forceinline__ unsigned pair_hi(unsigned e, unsigned o) { return __builtin_amdgcn_perm(o, e, 0x07060302u); }
template <int CTRL> __device__ __forceinline__ float dpp(float x) { return __int_as_float(__builtin_amdgcn_mov_dpp(__float_as_int(x), CTRL, 0xf, 0xf, true)); }
constexpr int XOR1 = 0xB1, XOR2 = 0x4E, XOR7 = 0x141, XOR8 = 0x128, ROR4 = 0x124, BCAST = 0x150;
__device__ __forceinline__ float tr32(float a, float b) {
  auto s = __builtin_amdgcn_permlane32_swap(__float_as_uint(a), __float_as_uint(b), false, false);
  return __uint_as_float(s[0]) + __uint_as_float(s[1]);
}
__device__ __forceinline__ float tr16(float a, float b) {
  auto s = __builtin_amdgcn_permlane16_swap(__float_as_uint(a), __float_as_uint(b), false, false);
  return __uint_as_float(s[0]) + __uint_as_float(s[1]);
}
template <int CTRL> __device__ __forceinline__ float trd(float lo, float hi, bool up) {
  const float t1 = lo + dpp<CTRL>(lo), t2 = hi + dpp<CTRL>(hi);
  return up ? t2 : t1;
}
__device__ __forceinline__ float wave_sum(float x) {
  x += dpp<XOR1>(x); x += dpp<XOR2>(x); x += dpp<XOR7>(x); x += dpp<XOR8>(x);
  x = tr16(x, x); x = tr32(x, x);
  return x;
}
__device__ __forceinline__ float wave_max(float x) {
  x = fmaxf(x, dpp<XOR1>(x)); x = fmaxf(x, dpp<XOR2>(x)); x = fmaxf(x, dpp<XOR7>(x)); x = fmaxf(x, dpp<XOR8>(x));
  auto s = __builtin_amdgcn_permlane16_swap(__float_as_uint(x), __float_as_uint(x), false, false);
  x = fmaxf(__uint_as_float(s[0]), __uint_as_float(s[1]));
  auto t = __builtin_amdgcn_permlane32_swap(__float_as_uint(x), __float_as_uint(x), false, false);
  return fmaxf(__uint_as_float(t[0]), __uint_as_float(t[1]));
}
__device__ __forceinline__ float sum4_parity(f32x4 v) {
  float s = tr16(tr32(v[0], v[2]), tr32(v[1], v[3]));
  s += dpp<XOR8>(s); s += dpp<ROR4>(s); s += dpp<XOR2>(s);
  return s;
}
__device__ __forceinline__ v4u tag4(f32x4 v, unsigned tag) {
  return ((__builtin_bit_cast(v4u, v) + 0x800u) & 0xfffff000u) | (tag & 0xfffu);
}
__device__ __forceinline__ unsigned take4(v4u g, unsigned tag, f32x4& acc) {
  acc += __builtin_bit_cast(f32x4, g & 0xfffff000u);
  return (g[0] ^ tag) | (g[1] ^ tag) | (g[2] ^ tag) | (g[3] ^ tag);
}
__device__ __forceinline__ rsrc_t mk_rsrc(const void* p, unsigned bytes) { return __builtin_amdgcn_make_buffer_rsrc(const_cast<void*>(p), 0, bytes, 0x00020000); }
template <int AUX> __device__ __forceinline__ v4u ld128(rsrc_t r, unsigned v, unsigned s) { return __builtin_amdgcn_raw_buffer_load_b128(r, v, s, AUX); }
template <int AUX> __device__ __forceinline__ v2u ld64(rsrc_t r, unsigned v, unsigned s) { return __builtin_amdgcn_raw_buffer_load_b64(r, v, s, AUX); }
__device__ __forceinline__ void st128(v4u d, rsrc_t r, unsigned v, unsigned s) { __builtin_amdgcn_raw_buffer_store_b128(d, r, v + s, 0, AUX_SC1); }
__device__ __forceinline__ void st64(v2u d, rsrc_t r, unsigned v, unsigned s) { __builtin_amdgcn_raw_buffer_store_b64(d, r, v + s, 0, AUX_SC1); }
__device__ __forceinline__ float rows4_sum(float x) { x += dpp<XOR1>(x); x += dpp<XOR2>(x); x = tr16(x, x); x = tr32(x, x); return x; }
__device__ __forceinline__ void ln_stats(const v4u (&x)[4], float& mean, float& inv) {
  float s = 0.f;
#pragma unroll
  for (int t = 0; t < 4; ++t)
#pragma unroll
    for (int j = 0; j < 4; ++j) s += bflo(x[t][j]) + bfhi(x[t][j]);
  const float mu = rows4_sum(s) * (1.0f / DM);
  float q = 0.f;
#pragma unroll
  for (int t = 0; t < 4; ++t)
#pragma unroll
    for (int j = 0; j < 4; ++j) { const float d0 = bflo(x[t][j]) - mu, d1 = bfhi(x[t][j]) - mu; q = fmaf(d0, d0, q); q = fmaf(d1, d1, q); }
  const float var = rows4_sum(q) * (1.0f / DM);
  mean = rbf(mu);
  inv = __builtin_amdgcn_rcpf(rbf(__builtin_amdgcn_sqrtf(rbf(rbf(var) + 1e-5f))));
}
__device__ __forceinline__ v4u ln_apply(v4u x, float mean, float inv) {
  v4u o;
#pragma unroll
  for (int j = 0; j < 4; ++j) { const unsigned d = pk_bf16(bflo(x[j]) - mean, bfhi(x[j]) - mean); o[j] = pk_bf16(bflo(d) * inv, bfhi(d) * inv); }
  return o;
}
template <int J> __device__ __forceinline__ v4u bfrag(const v4u* t) {
  v4u f;
#pragma unroll
  for (int d = 0; d < 4; ++d) f[d] = (J & 1) ? pair_hi(t[2 * d][J >> 1], t[2 * d + 1][J >> 1]) : pair_lo(t[2 * d][J >> 1], t[2 * d + 1][J >> 1]);
  return f;
}
template <int J> __device__ __forceinline__ v4u bfrag2(const v2u* t) {
  v4u f;
#pragma unroll
  for (int d = 0; d < 4; ++d) f[d] = (J & 1) ? pair_hi(t[2 * d][J >> 1], t[2 * d + 1][J >> 1]) : pair_lo(t[2 * d][J >> 1], t[2 * d + 1][J >> 1]);
  return f;
}
__device__ __forceinline__ f32x4 tile64(v4u a0, v4u a1, const v2u* t) {
  f32x4 o;
  static_for<4>([&](auto J) { constexpr int j = J;
    f32x4 d = mfma(a0, bfrag2<j>(t), f32x4{0.f, 0.f, 0.f, 0.f}); d = mfma(a1, bfrag2<j>(t + 8), d); o[j] = d[0]; });
  return o;
}
#define TOUCH(x) asm volatile("" :: "v"(x))
#define PIN(x) asm volatile("" : "+v"(x))
#define THROTTLE(ma, mb, i) asm volatile("s_bitcmp1_b32 %0, %2\n\ts_cbranch_scc0 1f\n\ts_waitcnt vmcnt(%4)\n\ts_bitcmp1_b32 %0, %3\n\ts_cbranch_scc0 1f\n\t" \
    "s_waitcnt vmcnt(%5)\n\ts_bitcmp1_b32 %1, %2\n\ts_cbranch_scc0 1f\n\ts_waitcnt vmcnt(%6)\n1:" :: "s"(ma), "s"(mb), "n"(i), "n"(16 + (i)), \
    "n"(5 + ((i) >= 1 && (i) <= 5 ? 2 : 0)), "n"(4 + ((i) >= 1 && (i) <= 4 ? 2 : 0)), "n"(3 + ((i) >= 1 && (i) <= 3 ? 2 : 0)) : "scc")
#define FENCE() __builtin_amdgcn_sched_barrier(0)
#define WG_BARRIER() asm volatile("s_waitcnt lgkmcnt(0)\n\ts_barrier" ::: "memory")
#define DRAIN() asm volatile("s_waitcnt vmcnt(0)" ::: "memory")
#define LDS_WAIT() asm volatile("s_waitcnt lgkmcnt(0)" ::: "memory")
#define MARK(k) do { if (DIAG == (k) + 1) { FENCE(); tmark = __builtin_amdgcn_s_memrealtime(); FENCE(); } } while (0)
#define FLUSH_STAMPS()

constexpr unsigned WS_E = 0;
constexpr unsigned WS_LS = 4096;
constexpr unsigned WS_CK = 32768;
constexpr unsigned WS_X1 = 65536;
constexpr unsigned WS_MP = WS_X1 + 32 * 8192;
constexpr unsigned WS_TB = WS_MP + 4 * 32 * 2048;
constexpr unsigned WS_QX = WS_TB + 4 * 64 * 2048;
constexpr unsigned WS_BYTES = WS_QX + 32 * 4 * 8 * 512;

struct Params {
  const unsigned short* x; const unsigned short* k; const unsigned short* v; const unsigned short* wq; const unsigned short* wo;
  const unsigned short* w1; const unsigned short* b1; const unsigned short* w2; const unsigned short* b2;
  unsigned short* out; unsigned char* ws;
};

constexpr int D = P_D, NA = P_NA, DO = P_DO;
constexpr int LD = 8;
constexpr int NU = (NA <= 48 + LD - 12) ? 48 : 64;
static_assert((NU + LD - NA) * 4 >= NU && D <= LD, "the backlog is gone before the loop starts");
constexpr float C2 = 0.088388347648318440f * 1.4426950408889634f;

constexpr int L_Q = 0, L_M = 256, L_F = 272, L_TAG = 288, L_REF = 292, L_T = 304, L_GX = 320, L_H = 1024, L_O = 1536, O_ROW = 272, L_S = L_O + 8 * O_ROW, L_END = L_S + 8192;

__global__ void __launch_bounds__(512, 2) k_fused(Params p) {
  __shared__ __attribute__((aligned(16))) unsigned char lds[L_END];
  unsigned long long t0s = 0, tmark = 0;
  if (DIAG) { t0s = __builtin_amdgcn_s_memrealtime(); tmark = t0s; }
  const int blk = blockIdx.x, tid = threadIdx.x, lane = tid & 63, w = __builtin_amdgcn_readfirstlane(tid >> 6);
  MARK(0);
  asm volatile("" :: "s"(p.x), "s"(p.k), "s"(p.v), "s"(p.wq), "s"(p.wo), "s"(p.w1), "s"(p.b1), "s"(p.w2), "s"(p.b2), "s"(p.out), "s"(p.ws));
  const int idx = blk >> 3, h = (blk & 7) * 4 + (idx & 3), bs = idx >> 2, b = bs >> 1, sp = bs & 1;
  const int cons = (blk & 7) * 8 + (idx & 7);
  const int col0 = 128 * (cons >> 4) + 16 * (cons & 7) + 4 * ((cons >> 3) & 1);
  const int r = lane >> 4, c = lane & 15;
  const unsigned l16 = lane * 16;
  const rsrc_t rws = mk_rsrc(p.ws, WS_BYTES);
  const unsigned rot = (bs >> 1) * 32 + (bs & 1) * 4;

  if (__builtin_expect(w < 4, 1)) {
    __builtin_amdgcn_s_setprio(2);
    const size_t kvoff = ((size_t)(b * NH + h) * SK + sp * 2048 + w * 4) * DH;
    const unsigned short* kp = p.k + kvoff; const unsigned short* vp = p.v + kvoff;
    const unsigned kvlen = 524288 - w * 1024;
    const rsrc_t rk = mk_rsrc(kp, kvlen);
    float* const scs = (float*)(lds + L_S + w * 2048) + lane;
    v4u S[NU + LD], q;
    unsigned now, refv = 0, tag = 0, sched = 0, ma, mb, org; int lv = P_KFIX; v2u ck;
    const unsigned ck_st = (lane == 0 && w == 0) ? WS_CK + (b * 64 + 2 * h + sp) * 8 : OOB, ck_ld = w == 0 ? lane * 8 : OOB;
    auto pace = [&](unsigned keep) {
      org = now - sched + lv * P_KAP;
      const unsigned rf = __builtin_amdgcn_readfirstlane(refv);
      lv = rf ? (int)(org - rf) >> P_BAND : P_KFIX; lv = lv < 0 ? 0 : lv; asm volatile("" : "+s"(lv)); lv = lv > 7 ? 7 : lv;
      unsigned m1 = 0xffffu, m2 = 0xffffu, m3 = 0xffffu;
      m3 = lv > 0 ? 0x5555u : m3; m3 = lv > 1 ? 0u : m3; m2 = lv > 2 ? 0x5555u : m2; m2 = lv > 3 ? 0u : m2;
      m1 = lv > 4 ? 0x5555u : m1; m1 = lv > 5 ? 0x9249u : m1; m1 = lv > 6 ? 0u : m1;
      ma = __builtin_amdgcn_readfirstlane((m1 & keep) | (m2 << 16)); mb = __builtin_amdgcn_readfirstlane(m3); };
    auto ck_io = [&](int g) { st64(v2u{org, tag}, mk_rsrc(p.ws, (P_CK >> g) & 1 ? WS_BYTES : 0), ck_st, g * 2048);
      ck = ld64<AUX_SC1>(mk_rsrc(p.ws, (P_CK >> (g - 1)) & 1 ? WS_BYTES : 0), ck_ld, WS_CK + (g - 1) * 2048 + b * 512); };
    auto ck_use = [&](int g) { TOUCH(ck);
      if (w == 0 && ((P_CK >> (g - 1)) & 1)) { const float d = wave_sum(ck[1] == tag ? (float)(int)(ck[0] - org) : (float)P_TAUV) * (1.0f / 64);
        __hip_atomic_store((lds_u32*)lds + L_REF / 4, (org + (int)d - P_OFF) | 1u, __ATOMIC_RELAXED, __HIP_MEMORY_SCOPE_WORKGROUP); } };
    auto sample = [&]() { now = (unsigned)__builtin_amdgcn_s_memrealtime();
      refv = __hip_atomic_load((lds_u32*)lds + L_REF / 4, __ATOMIC_RELAXED, __HIP_MEMORY_SCOPE_WORKGROUP); };
    float ka[16], mw = -INFINITY;
    auto kdot = [&](v4u t, int slot) {
      float d = dot2(t[0], q[0], 0.f); d = dot2(t[1], q[1], d); d = dot2(t[2], q[2], d); d = dot2(t[3], q[3], d); PIN(d); ka[slot] = d; };
    auto kfold = [&](int g) {
#pragma unroll
      for (int j = 0; j < 8; ++j) ka[j] = trd<XOR8>(ka[j], ka[j + 8], lane & 8);
#pragma unroll
      for (int j = 0; j < 4; ++j) ka[j] = trd<XOR7>(ka[j], ka[j + 4], lane & 4);
#pragma unroll
      for (int j = 0; j < 2; ++j) ka[j] = trd<XOR2>(ka[j], ka[j + 2], lane & 2);
      const float s = trd<XOR1>(ka[0], ka[1], lane & 1) * C2;
      scs[g * 64] = s; mw = fmaxf(mw, s); PIN(mw); };
    static_for<NU + LD>([&](auto N) {
      constexpr int n = N;
      constexpr int dn = n < P_N0 ? P_D0 : D;
      if constexpr (n >= dn) TOUCH(S[n - dn]);
      FENCE();
      if constexpr (n == (P_N0 ? P_D0 : D)) MARK(1);
      S[n] = ld128<AUX_KV>(rk, l16, n * 4096);
      FENCE();
      if constexpr (n == NA - 1) {
        MARK(2);
        WG_BARRIER();
        MARK(3);
        q = *(const v4u*)(lds + L_Q + c * 16);
        tag = *(const unsigned*)(lds + L_TAG);
        now = (unsigned)__builtin_amdgcn_s_memrealtime();
      }
      if constexpr (n == NA && ((P_CK >> (NU / 16 - 1)) & 1)) st64(v2u{now + (NU + LD - NA) * P_TAUK / 16 + P_KFIX * P_KAP, tag}, rws, ck_st, (NU / 16 - 1) * 2048);
      if constexpr (n == NU + LD - 1) now = (unsigned)__builtin_amdgcn_s_memrealtime();
      if constexpr (n >= NA) {
        constexpr int hi = (n - NA + 1) * 4 < n - LD + 1 ? (n - NA + 1) * 4 : n - LD + 1;
        constexpr int lo = (n - NA) * 4 < n - LD ? (n - NA) * 4 : n - LD;
        static_for<hi - lo>([&](auto J) { constexpr int i = lo + J; kdot(S[i], i & 15); if constexpr ((i & 15) == 15) kfold(i >> 4); });
      }
      FENCE();
    });
    v4u R[16];
#pragma unroll
    for (int i = 0; i < LD; ++i) R[i] = S[NU + i];
    float snext = scs[0];
#pragma unroll 1
    for (int g = NU / 16; g < 8; ++g) {
      const rsrc_t rn = mk_rsrc(g == 7 ? vp : kp, kvlen);
      const unsigned o0 = g * 65536, o1 = g == 7 ? 0u : o0 + 65536;
      pace(0xffffu);
      sched += P_TAUK;
      static_for<16>([&](auto I) { constexpr int i = I;
        TOUCH(R[i]); THROTTLE(ma, mb, i); FENCE();
        if constexpr (i == 0) { ck_io(g); FENCE(); }
        if constexpr (i + LD < 16) R[i + LD] = ld128<AUX_KV>(rk, l16, o0 + (i + LD) * 4096);
        else R[i + LD - 16] = ld128<AUX_KV>(rn, l16, o1 + (i + LD - 16) * 4096);
        if constexpr (i == 15) sample();
        FENCE();
        if constexpr (i == 10) ck_use(g);
        kdot(R[i], i);
        FENCE(); });
      kfold(g);
    }
    MARK(4);
    mw = wave_max(mw);
    asm volatile("" ::: "memory");
    __hip_atomic_store((lds_u32*)lds + L_M / 4 + w, min(__float_as_uint(mw), SENT - 1u), __ATOMIC_RELAXED, __HIP_MEMORY_SCOPE_WORKGROUP);
    float acc[8];
#pragma unroll
    for (int j = 0; j < 8; ++j) acc[j] = 0.f;
    const rsrc_t rv = mk_rsrc(vp, kvlen);
    MARK(5);
#pragma unroll 1
    for (int g = 0; g < 8; ++g) {
      const rsrc_t rn = mk_rsrc(vp, g == 7 ? 0u : kvlen);
      const unsigned o0 = g * 65536;
      const float pg = __builtin_amdgcn_exp2f(snext - mw);
      snext = scs[((g + 1) & 7) * 64];
      pace(g == 7 ? 0xffu : 0xffffu);
      sched += P_TAUV;
      static_for<16>([&](auto I) { constexpr int i = I;
        TOUCH(R[i]); THROTTLE(ma, mb, i); FENCE();
        if constexpr (i == 0) { ck_io(8 + g); FENCE(); }
        R[(i + LD) & 15] = ld128<AUX_KV>(i + LD < 16 ? rv : rn, l16, o0 + (i + LD) * 4096);
        if constexpr (i == 15) sample();
        FENCE();
        if constexpr (i == 10) ck_use(8 + g);
        const v4u t = R[i]; const float pb = dpp<BCAST + i>(pg);
#pragma unroll
        for (int j = 0; j < 4; ++j) { acc[2 * j] = fmaf(pb, bflo(t[j]), acc[2 * j]); acc[2 * j + 1] = fmaf(pb, bfhi(t[j]), acc[2 * j + 1]); }
        asm volatile("" : "+v"(acc[0]), "+v"(acc[1]), "+v"(acc[2]), "+v"(acc[3]), "+v"(acc[4]), "+v"(acc[5]), "+v"(acc[6]), "+v"(acc[7]));
        FENCE(); });
    }
    MARK(6);
    {
      float t4[4];
#pragma unroll
      for (int j = 0; j < 4; ++j) t4[j] = tr32(acc[j], acc[j + 4]);
      const float o0 = tr16(t4[0], t4[2]), o1 = tr16(t4[1], t4[3]);
      const unsigned hi = pk_bf16(o0, o1), lo = pk_bf16(o0 - bflo(hi), o1 - bfhi(hi));
      *(unsigned*)(lds + L_O + w * O_ROW + ((c * 16 + r * 4) ^ (rot * 2))) = hi;
      *(unsigned*)(lds + L_O + (4 + w) * O_ROW + ((c * 16 + r * 4) ^ (rot * 2))) = lo;
    }
    WG_BARRIER();
    MARK(7);
    FLUSH_STAMPS();
    return;
  }

  const int a = w - 4;
  const rsrc_t rx = mk_rsrc(p.x, NB * DM * 2);
  const unsigned xrow = ((lane >> 2) & 3) * (DM * 2);
  const bool mlp = h < 28 && !(ABL & 2);
  if (a == 0) {
    v4u xs[4], xa[2], T[16];
#pragma unroll
    for (int t = 0; t < 4; ++t) xs[t] = ld128<0>(rx, xrow + ((lane & 3) + 4 * r) * 16, t * 256);
#pragma unroll
    for (int s = 0; s < 2; ++s) xa[s] = ld128<0>(rx, xrow + r * 16, bs * 128 + s * 64);
    FENCE();
    const rsrc_t rq = mk_rsrc(p.wq + (size_t)bs * 64 * (NH * DH) + h * DH, (ABL & 8) ? 0 : 64 * NH * DH * 2);
#pragma unroll
    for (int i = 0; i < 16; ++i) T[i] = ld128<AUX_WQ>(rq, r * 8 * 8192 + c * 16, ((i >> 3) * 32 + (i & 7)) * 8192);
    const unsigned ep = __builtin_amdgcn_raw_buffer_load_b32(rws, 0, WS_E, AUX_SC1);
    FENCE();
    MARK(10);
    float mean, inv;
    ln_stats(xs, mean, inv);
    const v4u A0 = ln_apply(xa[0], mean, inv), A1 = ln_apply(xa[1], mean, inv);
    f32x4 g[2];
    static_for<8>([&](auto J) { constexpr int j = J;
      f32x4 d = mfma(A0, bfrag<j>(T), f32x4{0.f, 0.f, 0.f, 0.f}); d = mfma(A1, bfrag<j>(T + 8), d); g[j >> 2][j & 3] = d[0]; });
    FENCE();
    const unsigned tg = __builtin_amdgcn_readfirstlane(ep) + 1u;
#pragma unroll
    for (int j = 0; j < 2; ++j) st128(tag4(g[j], tg), rws, (ABL & 4) ? OOB : r * 4096 + c * 16 + j * 256, WS_QX + h * 16384 + bs * 512);
    MARK(11);
    DRAIN();
    MARK(1);
    f32x4 q4;
#pragma unroll 1
    for (unsigned spin = 0; spin < SPIN_CAP; ++spin) {
      v4u G[4];
#pragma unroll
      for (int j = 0; j < 4; ++j) G[j] = ld128<AUX_SC1>(rws, (ABL & 4) ? OOB : l16, WS_QX + h * 16384 + b * 4096 + j * 1024);
      unsigned bad = 0; q4 = f32x4{0.f, 0.f, 0.f, 0.f};
#pragma unroll
      for (int j = 0; j < 4; ++j) bad |= take4(G[j], tg, q4);
      if (!__any((bad & 0xfffu) != 0u) || (ABL & 4)) break;
      asm volatile("" ::: "memory");
    }
    *(unsigned*)(lds + L_Q + c * 16 + (r & 1) * 8 + (r >> 1) * 4) = pk_bf16(tr32(q4[0], q4[2]), tr32(q4[1], q4[3]));
    if (lane == 0) { *(v4u*)(lds + L_M) = v4u{SENT, SENT, SENT, SENT}; *(v2u*)(lds + L_TAG) = v2u{tg, 0u}; }
  }
  MARK(2);
  WG_BARRIER();
  MARK(3);
  const unsigned tag = __builtin_amdgcn_readfirstlane(*(const unsigned*)(lds + L_TAG));
  if (a == 2 && mlp) {
    v4u xs[4], xa[2]; v2u U[16];
#pragma unroll
    for (int t = 0; t < 4; ++t) xs[t] = ld128<0>(rx, xrow + ((lane & 3) + 4 * r) * 16, t * 256);
#pragma unroll
    for (int s = 0; s < 2; ++s) xa[s] = ld128<0>(rx, xrow + r * 16, bs * 128 + s * 64);
    const rsrc_t r1 = mk_rsrc(p.w1 + (size_t)bs * 64 * FF + h * 64, 64 * FF * 2);
#pragma unroll
    for (int i = 0; i < 16; ++i) U[i] = ld64<AUX_W12>(r1, r * 8 * (FF * 2) + c * 8, ((i >> 3) * 32 + (i & 7)) * (FF * 2));
    FENCE();
#pragma unroll 1
    for (int it = 0; it < 2; ++it) {
      float mean, inv;
      ln_stats(xs, mean, inv);
#pragma unroll
      for (int t = 0; t < 4; ++t) xs[t] = ln_apply(xs[t], mean, inv);
      xa[0] = ln_apply(xa[0], mean, inv); xa[1] = ln_apply(xa[1], mean, inv);
    }
    st128(tag4(tile64(xa[0], xa[1], U), tag), rws, l16, WS_X1 + h * 8192 + bs * 1024);
  }
  MARK(4);
  v4u Bf[4][8];
  {
    const rsrc_t ro = mk_rsrc(p.wo + (size_t)h * DH * DM + a * 128, (ABL & 1) ? 0 : DH * DM * 2);
    const unsigned vo = r * 8 * 1024 + c * 16;
    v4u T[32];
    static_for<32 + DO>([&](auto N) { constexpr int n = N;
      if constexpr (n >= DO) TOUCH(T[n - DO]);
      FENCE();
      if constexpr (n < 32) T[n] = ld128<AUX_WO>(ro, vo, (((n >> 3) * 32 + (n & 7)) ^ rot) * 1024);
      FENCE();
      if constexpr (n >= DO && ((n - DO) & 7) == 7) { constexpr int s = (n - DO) >> 3;
        static_for<8>([&](auto J) { constexpr int j = J; Bf[s][j] = bfrag<j>(T + 8 * s); PIN(Bf[s][j]); }); }
      FENCE(); });
  }
  MARK(5);
  const unsigned co = r * 2 + (lane & 1) * 16;
  if (a == 3) {
    unsigned mb;
#pragma unroll 1
    for (unsigned spin = 0; spin < SPIN_CAP; ++spin) {
      mb = __hip_atomic_load((lds_u32*)lds + L_M / 4 + (lane & 3), __ATOMIC_RELAXED, __HIP_MEMORY_SCOPE_WORKGROUP);
      if (__all(mb != SENT)) break;
      __builtin_amdgcn_s_sleep(4);
    }
    asm volatile("" ::: "memory");
    const float m4 = __uint_as_float(mb);
    float M = fmaxf(m4, dpp<XOR1>(m4)); M = fmaxf(M, dpp<XOR2>(M));
    float L = 0.f;
#pragma unroll
    for (int i = 0; i < 8; ++i) { const f32x4 s4 = *(const f32x4*)(lds + L_S + i * 1024 + l16);
#pragma unroll
      for (int j = 0; j < 4; ++j) L += __builtin_amdgcn_exp2f(s4[j] - M); }
    L = wave_sum(L);
    const float lse = M + __builtin_amdgcn_logf(L);
    st128(v4u{__float_as_uint(lse), tag, 0u, tag}, rws, lane == 0 ? 0u : OOB, WS_LS + (b * 64 + 2 * h + sp) * 64);
    MARK(6);
    v4u m;
#pragma unroll 1
    for (unsigned spin = 0; spin < SPIN_CAP; ++spin) {
      __builtin_amdgcn_s_sleep(P_WAIT);
      m = ld128<AUX_SC1>(rws, 0, WS_LS + (b * 64 + 2 * h + (sp ^ 1)) * 64);
      if (!__any(((m[1] ^ tag) | (m[3] ^ tag)) != 0u)) break;
      asm volatile("" ::: "memory");
    }
    *(float*)(lds + L_F + 4 * (lane & 3)) = __builtin_amdgcn_exp2f(m4 - M) * __builtin_amdgcn_rcpf(L) * __builtin_amdgcn_rcpf(1.0f + __builtin_amdgcn_exp2f(__uint_as_float(m[0]) - lse));
  } else if (a == 2) {
    const unsigned b2v = __builtin_amdgcn_raw_buffer_load_b16(mk_rsrc(p.b2, DM * 2), co, col0 * 2, 0);
    const unsigned xv = __builtin_amdgcn_raw_buffer_load_b16(rx, co, (b * DM + col0) * 2, 0);
    if (mlp) {
      v2u U[16];
      const rsrc_t r2 = mk_rsrc(p.w2 + (size_t)h * 64 * DM + bs * 64, 64 * DM * 2);
#pragma unroll
      for (int i = 0; i < 16; ++i) U[i] = ld64<AUX_W12>(r2, r * 8 * 1024 + c * 8, ((i >> 3) * 32 + (i & 7)) * 1024);
      const v2u b1v = ld64<0>(mk_rsrc(p.b1 + h * 64, 128), c * 8, 0);
      DRAIN();
      f32x4 z4;
#pragma unroll 1
      for (unsigned spin = 0; spin < SPIN_CAP; ++spin) {
        v4u G[8];
#pragma unroll
        for (int j = 0; j < 8; ++j) G[j] = ld128<AUX_SC1>(rws, l16, WS_X1 + h * 8192 + j * 1024);
        unsigned bad = 0; z4 = f32x4{0.f, 0.f, 0.f, 0.f};
#pragma unroll
        for (int j = 0; j < 8; ++j) bad |= take4(G[j], tag, z4);
        if (!__any((bad & 0xfffu) != 0u)) break;
        __builtin_amdgcn_s_sleep(32);
        asm volatile("" ::: "memory");
      }
      float hv[4];
#pragma unroll
      for (int j = 0; j < 4; ++j) {
        const float z = rbf(rbf(z4[j]) + ((j & 1) ? bfhi(b1v[j >> 1]) : bflo(b1v[j >> 1])));
        const float e2 = __builtin_amdgcn_exp2f((-2.0f * 0.7978845608028654f * 1.4426950408889634f) * (z + 0.044715f * z * z * z));
        hv[j] = z * __builtin_amdgcn_rcpf(1.0f + e2);
      }
      *(v2u*)(lds + L_H + r * 128 + c * 8) = v2u{pk_bf16(hv[0], hv[1]), pk_bf16(hv[2], hv[3])};
      LDS_WAIT();
      const v4u A0 = *(const v4u*)(lds + L_H + ((lane >> 2) & 3) * 128 + r * 16), A1 = *(const v4u*)(lds + L_H + ((lane >> 2) & 3) * 128 + r * 16 + 64);
      st128(tag4(tile64(A0, A1, U), tag), rws, r * (28 * 2048) + c * 16, WS_MP + h * 2048 + bs * 256);
    } else { __builtin_amdgcn_s_sleep(127); __builtin_amdgcn_s_sleep(64); }
    MARK(6);
    f32x4 m4;
#pragma unroll 1
    for (unsigned spin = 0; spin < SPIN_CAP; ++spin) {
      __builtin_amdgcn_s_sleep(127);
      const v4u G = ld128<AUX_SC1>(rws, lane < 56 ? (unsigned)((lane >> 1) * 2048 + (lane & 1) * 32) : OOB, WS_MP + b * (28 * 2048) + col0 * 4);
      m4 = f32x4{0.f, 0.f, 0.f, 0.f};
      const unsigned bad = take4(G, tag, m4);
      if (!__any(lane < 56 && (bad & 0xfffu) != 0u) || (ABL & 2)) break;
      asm volatile("" ::: "memory");
    }
    const float m = rbf(rbf(sum4_parity(m4)) + bflo(b2v));
    const float gate = rbf(m * __builtin_amdgcn_rcpf(1.0f + __builtin_amdgcn_exp2f(-1.4426950408889634f * m)));
    *(f32x2*)(lds + L_GX + co * 4) = f32x2{gate, bflo(xv)};
  }
  MARK(9);
#pragma unroll 1
  for (int pass = P_DRY ? 0 : 1; pass < 2; ++pass) {
    const unsigned dry = pass ? 0u : OOB;
    if (pass) { WG_BARRIER(); MARK(7); }
    v4u A[4];
#pragma unroll
    for (int s = 0; s < 4; ++s) A[s] = *(const v4u*)(lds + L_O + (lane & 7) * O_ROW + s * 64 + r * 16);
    const f32x4 f = *(const f32x4*)(lds + L_F);
    const f32x2 gx = *(const f32x2*)(lds + L_GX + co * 4);
    float t[8];
#pragma unroll
    for (int j = 0; j < 8; ++j) {
      f32x4 d = mfma(A[0], Bf[0][j], f32x4{0.f, 0.f, 0.f, 0.f});
#pragma unroll
      for (int s = 1; s < 4; ++s) d = mfma(A[s], Bf[s][j], d);
      t[j] = fmaf(d[3], f[3], d[2] * f[2]) + fmaf(d[1], f[1], d[0] * f[0]);
    }
    const f32x4 tv = {tr16(t[0], t[4]), tr16(t[1], t[5]), tr16(t[2], t[6]), tr16(t[3], t[7])};
    st128(tag4(tv, tag), rws, lane < 32 ? l16 | dry : OOB, WS_TB + (b * 64 + 2 * h + sp) * 2048 + a * 512);
    if (pass) MARK(10);
    if (a == 0) {
      const unsigned go = ((lane >> 1) * 2048 + (lane & 1) * 16) | dry;
      f32x4 s4;
      __builtin_amdgcn_s_sleep(P_PD);
      if (pass) MARK(11);
#pragma unroll 1
      for (unsigned spin = 0; spin < SPIN_CAP; ++spin) {
        const v4u G0 = ld128<AUX_SC1>(rws, go, WS_TB + b * 131072 + cons * 32), G1 = ld128<AUX_SC1>(rws, go, WS_TB + b * 131072 + 65536 + cons * 32);
        s4 = f32x4{0.f, 0.f, 0.f, 0.f};
        unsigned bad = take4(G0, tag, s4) | take4(G1, tag, s4);
        if (!__any((bad & 0xfffu) != 0u) || !pass) break;
        __builtin_amdgcn_s_sleep(P_PS);
        asm volatile("" ::: "memory");
      }
      const float o = gx[1] + rbf(rbf(sum4_parity(s4)) * gx[0]);
      __builtin_amdgcn_raw_buffer_store_b16((unsigned short)bf_bits(o), mk_rsrc(p.out, NB * DM * 2), (lane & 14) == 0 ? co | dry : OOB, (b * DM + col0) * 2, 0);
      __builtin_amdgcn_raw_buffer_store_b32(tag, rws, (blk == 0 && lane == 0) ? WS_E | dry : OOB, 0, AUX_SC1);
    }
  }
  MARK(8);
  FLUSH_STAMPS();
  if (DIAG && blk == DIAG_BLK && a == 0) { while (__builtin_amdgcn_s_memrealtime() - t0s < tmark - t0s + 10000) __builtin_amdgcn_s_sleep(1); }
}

extern "C" void kernel_launch(void* const* d_in, const int* in_sizes, int n_in, void* d_out, int out_size, void* d_ws, size_t ws_size,
                              hipStream_t stream) {
  Params p;
  p.x = (const unsigned short*)d_in[0]; p.k = (const unsigned short*)d_in[1]; p.v = (const unsigned short*)d_in[2];
  p.wq = (const unsigned short*)d_in[3]; p.wo = (const unsigned short*)d_in[4];
  p.w1 = (const unsigned short*)d_in[6]; p.b1 = (const unsigned short*)d_in[7]; p.w2 = (const unsigned short*)d_in[8]; p.b2 = (const unsigned short*)d_in[9];
  p.out = (unsigned short*)d_out;
  p.ws = (unsigned char*)d_ws;
  k_fused<<<256, 512, 0, stream>>>(p);
}
```

```cpp
#include <hip/hip_runtime.h>
#include <hip/hip_bf16.h>
#include <stdint.h>
#include <utility>

#define P_D 6
#define P_D0 6
#define P_N0 0
#define P_NA 40
#define P_DO 3
#define AUX_KV 2
#define AUX_WQ 2
#define AUX_WO 0
#define AUX_W12 2
#define P_BAND 5
#define P_KFIX 2
#define P_TAUV 215
#define P_OFF 110
#define P_WAIT 100
#define P_PD 20
#define P_PS 1
#define P_DRY 0
#define P_QD 0
#define P_QS 0
#define P_WD 0
#define P_WD2 0
#define P_MD 0
#define P_TLB 0
#define ABL 0
#define DIAG 1
#define DIAG_W 0
#define DIAG_BLK 100

typedef unsigned v4u __attribute__((ext_vector_type(4)));
typedef unsigned v2u __attribute__((ext_vector_type(2)));
typedef float f32x4 __attribute__((ext_vector_type(4)));
typedef float f32x2 __attribute__((ext_vector_type(2)));
typedef __bf16 bf16x2 __attribute__((ext_vector_type(2)));
typedef __bf16 bf16x8 __attribute__((ext_vector_type(8)));
typedef __amdgpu_buffer_rsrc_t rsrc_t;
typedef __attribute__((address_space(3))) unsigned lds_u32;

constexpr int NB = 4, DM = 512, NH = 32, SK = 4096, DH = 128, FF = 1792;
constexpr int AUX_SC1 = 16;
constexpr unsigned SENT = 0xFFFFFFFFu, OOB = 0x80000000u;
constexpr unsigned SPIN_CAP = 1u << 14;

template <class F, int... I> __device__ __forceinline__ void static_for_impl(F&& f, std::integer_sequence<int, I...>) { (f(std::integral_constant<int, I>{}), ...); }
template <int N, class F> __device__ __forceinline__ void static_for(F&& f) { static_for_impl(f, std::make_integer_sequence<int, N>{}); }

__device__ __forceinline__ float dot2(unsigned a, unsigned b, float acc) {
  return __builtin_amdgcn_fdot2_f32_bf16(__builtin_bit_cast(bf16x2, a), __builtin_bit_cast(bf16x2, b), acc, false);
}
__device__ __forceinline__ f32x4 mfma(v4u a, v4u b, f32x4 c) {
  return __builtin_amdgcn_mfma_f32_16x16x32_bf16(__builtin_bit_cast(bf16x8, a), __builtin_bit_cast(bf16x8, b), c, 0, 0, 0);
}
__device__ __forceinline__ float bflo(unsigned u) { return __uint_as_float(u << 16); }
__device__ __forceinline__ float bfhi(unsigned u) { return __uint_as_float(u & 0xffff0000u); }
__device__ __forceinline__ unsigned pk_bf16(float lo, float hi) { bf16x2 r; r.x = (__bf16)lo; r.y = (__bf16)hi; return __builtin_bit_cast(unsigned, r); }
__device__ __forceinline__ unsigned bf_bits(float x) { return (unsigned)__builtin_bit_cast(unsigned short, (__bf16)x); }
__device__ __forceinline__ float rbf(float x) { return bflo(bf_bits(x)); }
__device__ __forceinline__ unsigned pair_lo(unsigned e, unsigned o) { return __builtin_amdgcn_perm(o, e, 0x05040100u); }
__device__ __forceinline__ unsigned pair_hi(unsigned e, unsigned o) { return __builtin_amdgcn_perm(o, e, 0x07060302u); }
template <int CTRL> __device__ __forceinline__ float dpp(float x) { return __int_as_float(__builtin_amdgcn_mov_dpp(__float_as_int(x), CTRL, 0xf, 0xf, true)); }
constexpr int XOR1 = 0xB1, XOR2 = 0x4E, XOR7 = 0x141, XOR8 = 0x128, ROR4 = 0x124, BCAST = 0x150;
__device__ __forceinline__ float tr32(float a, float b) {
  auto s = __builtin_amdgcn_permlane32_swap(__float_as_uint(a), __float_as_uint(b), false, false);
  return __uint_as_float(s[0]) + __uint_as_float(s[1]);
}
__device__ __forceinline__ float tr16(float a, float b) {
  auto s = __builtin_amdgcn_permlane16_swap(__float_as_uint(a), __float_as_uint(b), false, false);
  return __uint_as_float(s[0]) + __uint_as_float(s[1]);
}
template <int CTRL> __device__ __forceinline__ float trd(float lo, float hi, bool up) {
  const float t1 = lo + dpp<CTRL>(lo), t2 = hi + dpp<CTRL>(hi);
  return up ? t2 : t1;
}
__device__ __forceinline__ float wave_sum(float x) {
  x += dpp<XOR1>(x); x += dpp<XOR2>(x); x += dpp<XOR7>(x); x += dpp<XOR8>(x);
  x = tr16(x, x); x = tr32(x, x);
  return x;
}
__device__ __forceinline__ float wave_max(float x) {
  x = fmaxf(x, dpp<XOR1>(x)); x = fmaxf(x, dpp<XOR2>(x)); x = fmaxf(x, dpp<XOR7>(x)); x = fmaxf(x, dpp<XOR8>(x));
  auto s = __builtin_amdgcn_permlane16_swap(__float_as_uint(x), __float_as_uint(x), false, false);
  x = fmaxf(__uint_as_float(s[0]), __uint_as_float(s[1]));
  auto t = __builtin_amdgcn_permlane32_swap(__float_as_uint(x), __float_as_uint(x), false, false);
  return fmaxf(__uint_as_float(t[0]), __uint_as_float(t[1]));
}
__device__ __forceinline__ float sum4_parity(f32x4 v) {
  float s = tr16(tr32(v[0], v[2]), tr32(v[1], v[3]));
  s += dpp<XOR8>(s); s += dpp<ROR4>(s); s += dpp<XOR2>(s);
  return s;
}
__device__ __forceinline__ v4u tag4(f32x4 v, unsigned tag) {
  return ((__builtin_bit_cast(v4u, v) + 0x800u) & 0xfffff000u) | (tag & 0xfffu);
}
__device__ __forceinline__ unsigned take4(v4u g, unsigned tag, f32x4& acc) {
  acc += __builtin_bit_cast(f32x4, g & 0xfffff000u);
  return (g[0] ^ tag) | (g[1] ^ tag) | (g[2] ^ tag) | (g[3] ^ tag);
}
__device__ __forceinline__ rsrc_t mk_rsrc(const void* p, unsigned bytes) { return __builtin_amdgcn_make_buffer_rsrc(const_cast<void*>(p), 0, bytes, 0x00020000); }
template <int AUX> __device__ __forceinline__ v4u ld128(rsrc_t r, unsigned v, unsigned s) { return __builtin_amdgcn_raw_buffer_load_b128(r, v, s, AUX); }
template <int AUX> __device__ __forceinline__ v2u ld64(rsrc_t r, unsigned v, unsigned s) { return __builtin_amdgcn_raw_buffer_load_b64(r, v, s, AUX); }
__device__ __forceinline__ void st128(v4u d, rsrc_t r, unsigned v, unsigned s) { __builtin_amdgcn_raw_buffer_store_b128(d, r, v + s, 0, AUX_SC1); }
__device__ __forceinline__ void st64(v2u d, rsrc_t r, unsigned v, unsigned s) { __builtin_amdgcn_raw_buffer_store_b64(d, r, v + s, 0, AUX_SC1); }
__device__ __forceinline__ float rows4_sum(float x) { x += dpp<XOR1>(x); x += dpp<XOR2>(x); x = tr16(x, x); x = tr32(x, x); return x; }
__device__ __forceinline__ void ln_stats(const v4u (&x)[4], float& mean, float& inv) {
  float s = 0.f;
#pragma unroll
  for (int t = 0; t < 4; ++t)
#pragma unroll
    for (int j = 0; j < 4; ++j) s += bflo(x[t][j]) + bfhi(x[t][j]);
  const float mu = rows4_sum(s) * (1.0f / DM);
  float q = 0.f;
#pragma unroll
  for (int t = 0; t < 4; ++t)
#pragma unroll
    for (int j = 0; j < 4; ++j) { const float d0 = bflo(x[t][j]) - mu, d1 = bfhi(x[t][j]) - mu; q = fmaf(d0, d0, q); q = fmaf(d1, d1, q); }
  const float var = rows4_sum(q) * (1.0f / DM);
  mean = rbf(mu);
  inv = __builtin_amdgcn_rcpf(rbf(__builtin_amdgcn_sqrtf(rbf(rbf(var) + 1e-5f))));
}
__device__ __forceinline__ v4u ln_apply(v4u x, float mean, float inv) {
  v4u o;
#pragma unroll
  for (int j = 0; j < 4; ++j) { const unsigned d = pk_bf16(bflo(x[j]) - mean, bfhi(x[j]) - mean); o[j] = pk_bf16(bflo(d) * inv, bfhi(d) * inv); }
  return o;
}
template <int J> __device__ __forceinline__ v4u bfrag(const v4u* t) {
  v4u f;
#pragma unroll
  for (int d = 0; d < 4; ++d) f[d] = (J & 1) ? pair_hi(t[2 * d][J >> 1], t[2 * d + 1][J >> 1]) : pair_lo(t[2 * d][J >> 1], t[2 * d + 1][J >> 1]);
  return f;
}
template <int J> __device__ __forceinline__ v4u bfrag2(const v2u* t) {
  v4u f;
#pragma unroll
  for (int d = 0; d < 4; ++d) f[d] = (J & 1) ? pair_hi(t[2 * d][J >> 1], t[2 * d + 1][J >> 1]) : pair_lo(t[2 * d][J >> 1], t[2 * d + 1][J >> 1]);
  return f;
}
__device__ __forceinline__ f32x4 tile64(v4u a0, v4u a1, const v2u* t) {
  f32x4 o;
  static_for<4>([&](auto J) { constexpr int j = J;
    f32x4 d = mfma(a0, bfrag2<j>(t), f32x4{0.f, 0.f, 0.f, 0.f}); d = mfma(a1, bfrag2<j>(t + 8), d); o[j] = d[0]; });
  return o;
}
#define TOUCH(x) asm volatile("" :: "v"(x))
#define PIN(x) asm volatile("" : "+v"(x))
#define THROTTLE(mask, i) asm volatile("s_bitcmp1_b32 %0, %1\n\ts_cbranch_scc0 1f\n\ts_waitcnt vmcnt(" "3" ")\n1:" :: "s"(mask), "n"(i) : "scc")
#define FENCE() __builtin_amdgcn_sched_barrier(0)
#define WG_BARRIER() asm volatile("s_waitcnt lgkmcnt(0)\n\ts_barrier" ::: "memory")
#define DRAIN() asm volatile("s_waitcnt vmcnt(0)" ::: "memory")
#define LDS_WAIT() asm volatile("s_waitcnt lgkmcnt(0)" ::: "memory")
#define MARK(k) do { if (DIAG == (k) + 1 && w == DIAG_W) { FENCE(); *(unsigned*)(lds + L_DIAG) = (unsigned)__builtin_amdgcn_s_memrealtime(); FENCE(); } } while (0)
#define FLUSH_STAMPS()

constexpr unsigned WS_E = 0;
constexpr unsigned WS_LS = 4096;
constexpr unsigned WS_X1 = 65536;
constexpr unsigned WS_MP = WS_X1 + 32 * 8192;
constexpr unsigned WS_TB = WS_MP + 4 * 32 * 2048;
constexpr unsigned WS_QX = WS_TB + 4 * 64 * 2048;
constexpr unsigned WS_BYTES = WS_QX + 32 * 4 * 8 * 512;

struct Params {
  const unsigned short* x; const unsigned short* k; const unsigned short* v; const unsigned short* wq; const unsigned short* wo;
  const unsigned short* w1; const unsigned short* b1; const unsigned short* w2; const unsigned short* b2;
  unsigned short* out; unsigned char* ws;
};

constexpr int D = P_D, NA = P_NA, DO = P_DO;
constexpr int LD = 8;
constexpr int NU = (NA <= 48 + LD - 12) ? 48 : 64;
static_assert((NU + LD - NA) * 4 >= NU && D <= LD, "the backlog is gone before the loop starts");
constexpr float C2 = 0.088388347648318440f * 1.4426950408889634f;

constexpr int L_Q = 0, L_M = 256, L_F = 272, L_TAG = 288, L_REF = 292, L_T = 304, L_GX = 320, L_DIAG = 512, L_H = 1024, L_O = 1536, O_ROW = 272, L_S = L_O + 8 * O_ROW, L_END = L_S + 8192;

__global__ void __launch_bounds__(512, 2) k_fused(Params p) {
  __shared__ __attribute__((aligned(16))) unsigned char lds[L_END];
  const int blk = blockIdx.x, tid = threadIdx.x, lane = tid & 63, w = __builtin_amdgcn_readfirstlane(tid >> 6);
  MARK(0);
  asm volatile("" :: "s"(p.x), "s"(p.k), "s"(p.v), "s"(p.wq), "s"(p.wo), "s"(p.w1), "s"(p.b1), "s"(p.w2), "s"(p.b2), "s"(p.out), "s"(p.ws));
  const int idx = blk >> 3, h = (blk & 7) * 4 + (idx & 3), bs = idx >> 2, b = bs >> 1, sp = bs & 1;
  const int cons = (blk & 7) * 8 + (idx & 7);
  const int col0 = 128 * (cons >> 4) + 16 * (cons & 7) + 4 * ((cons >> 3) & 1);
  const int r = lane >> 4, c = lane & 15;
  const unsigned l16 = lane * 16;
  const rsrc_t rws = mk_rsrc(p.ws, WS_BYTES);
  const unsigned rot = (bs >> 1) * 32 + (bs & 1) * 4;

  if (__builtin_expect(w < 4, 1)) {
    __builtin_amdgcn_s_setprio(2);
    const size_t kvoff = ((size_t)(b * NH + h) * SK + sp * 2048 + w * 4) * DH;
    const unsigned short* kp = p.k + kvoff; const unsigned short* vp = p.v + kvoff;
    const unsigned kvlen = 524288 - w * 1024;
    const rsrc_t rk = mk_rsrc(kp, kvlen);
    float* const scs = (float*)(lds + L_S + w * 2048) + lane;
    v4u S[NU + LD], q;
    auto pace = [&](int lv) {
      unsigned m = 0xffffu; m = lv > 0 ? 0x5555u : m; m = lv > 1 ? 0x9249u : m; m = lv > 2 ? 0x1111u : m;
      m = lv > 3 ? 0x0421u : m; m = lv > 4 ? 0x0101u : m; m = lv > 5 ? 0u : m;
      return (unsigned)__builtin_amdgcn_readfirstlane(m); };
    float ka[16], mw = -INFINITY;
    auto kdot = [&](v4u t, int slot) {
      float d = dot2(t[0], q[0], 0.f); d = dot2(t[1], q[1], d); d = dot2(t[2], q[2], d); d = dot2(t[3], q[3], d); PIN(d); ka[slot] = d; };
    auto kfold = [&](int g) {
#pragma unroll
      for (int j = 0; j < 8; ++j) ka[j] = trd<XOR8>(ka[j], ka[j + 8], lane & 8);
#pragma unroll
      for (int j = 0; j < 4; ++j) ka[j] = trd<XOR7>(ka[j], ka[j + 4], lane & 4);
#pragma unroll
      for (int j = 0; j < 2; ++j) ka[j] = trd<XOR2>(ka[j], ka[j + 2], lane & 2);
      const float s = trd<XOR1>(ka[0], ka[1], lane & 1) * C2;
      scs[g * 64] = s; mw = fmaxf(mw, s); PIN(mw); };
    static_for<NU + LD>([&](auto N) {
      constexpr int n = N;
      constexpr int dn = n < P_N0 ? P_D0 : D;
      if constexpr (n >= dn) TOUCH(S[n - dn]);
      FENCE();
      if constexpr (n == (P_N0 ? P_D0 : D)) MARK(1);
      S[n] = ld128<AUX_KV>(rk, l16, n * 4096);
      FENCE();
      if constexpr (n == NA - 1) {
        MARK(2);
        WG_BARRIER();
        MARK(3);
        q = *(const v4u*)(lds + L_Q + c * 16);
      }
      if constexpr (n >= NA) {
        constexpr int hi = (n - NA + 1) * 4 < n - LD + 1 ? (n - NA + 1) * 4 : n - LD + 1;
        constexpr int lo = (n - NA) * 4 < n - LD ? (n - NA) * 4 : n - LD;
        static_for<hi - lo>([&](auto J) { constexpr int i = lo + J; kdot(S[i], i & 15); if constexpr ((i & 15) == 15) kfold(i >> 4); });
      }
      FENCE();
    });
    v4u R[16];
#pragma unroll
    for (int i = 0; i < LD; ++i) R[i] = S[NU + i];
    float snext = scs[0];
#pragma unroll 1
    for (int g = NU / 16; g < 8; ++g) {
      const rsrc_t rn = mk_rsrc(g == 7 ? vp : kp, kvlen);
      const unsigned o0 = g * 65536, o1 = g == 7 ? 0u : o0 + 65536;
      const unsigned mask = pace(P_KFIX);
      static_for<16>([&](auto I) { constexpr int i = I;
        TOUCH(R[i]); THROTTLE(mask, i); FENCE();
        if constexpr (i + LD < 16) R[i + LD] = ld128<AUX_KV>(rk, l16, o0 + (i + LD) * 4096);
        else R[i + LD - 16] = ld128<AUX_KV>(rn, l16, o1 + (i + LD - 16) * 4096);
        FENCE();
        kdot(R[i], i);
        FENCE(); });
      kfold(g);
    }
    MARK(4);
    unsigned now = (unsigned)__builtin_amdgcn_s_memrealtime();
    mw = wave_max(mw);
    *(unsigned*)(lds + L_T + 4 * w) = now;
    unsigned refv = 0;
    asm volatile("" ::: "memory");
    __hip_atomic_store((lds_u32*)lds + L_M / 4 + w, min(__float_as_uint(mw), SENT - 1u), __ATOMIC_RELAXED, __HIP_MEMORY_SCOPE_WORKGROUP);
    float acc[8];
#pragma unroll
    for (int j = 0; j < 8; ++j) acc[j] = 0.f;
    const rsrc_t rv = mk_rsrc(vp, kvlen);
    MARK(5);
#pragma unroll 1
    for (int g = 0; g < 8; ++g) {
      const rsrc_t rn = mk_rsrc(vp, g == 7 ? 0u : kvlen);
      const unsigned o0 = g * 65536;
      const float pg = __builtin_amdgcn_exp2f(snext - mw);
      snext = scs[((g + 1) & 7) * 64];
      const unsigned rf = __builtin_amdgcn_readfirstlane(refv);
      const unsigned mask = pace(rf ? ((int)(now - rf) - g * P_TAUV) >> P_BAND : P_KFIX) & (g == 7 ? 0xffu : 0xffffu);
      static_for<16>([&](auto I) { constexpr int i = I;
        TOUCH(R[i]); THROTTLE(mask, i); FENCE();
        R[(i + LD) & 15] = ld128<AUX_KV>(i + LD < 16 ? rv : rn, l16, o0 + (i + LD) * 4096);
        if constexpr (i == 15) { now = (unsigned)__builtin_amdgcn_s_memrealtime();
          refv = __hip_atomic_load((lds_u32*)lds + L_REF / 4, __ATOMIC_RELAXED, __HIP_MEMORY_SCOPE_WORKGROUP); }
        FENCE();
        const v4u t = R[i]; const float pb = dpp<BCAST + i>(pg);
#pragma unroll
        for (int j = 0; j < 4; ++j) { acc[2 * j] = fmaf(pb, bflo(t[j]), acc[2 * j]); acc[2 * j + 1] = fmaf(pb, bfhi(t[j]), acc[2 * j + 1]); }
        asm volatile("" : "+v"(acc[0]), "+v"(acc[1]), "+v"(acc[2]), "+v"(acc[3]), "+v"(acc[4]), "+v"(acc[5]), "+v"(acc[6]), "+v"(acc[7]));
        FENCE(); });
    }
    MARK(6);
    {
      float t4[4];
#pragma unroll
      for (int j = 0; j < 4; ++j) t4[j] = tr32(acc[j], acc[j + 4]);
      const float o0 = tr16(t4[0], t4[2]), o1 = tr16(t4[1], t4[3]);
      const unsigned hi = pk_bf16(o0, o1), lo = pk_bf16(o0 - bflo(hi), o1 - bfhi(hi));
      *(unsigned*)(lds + L_O + w * O_ROW + ((c * 16 + r * 4) ^ (rot * 2))) = hi;
      *(unsigned*)(lds + L_O + (4 + w) * O_ROW + ((c * 16 + r * 4) ^ (rot * 2))) = lo;
    }
    WG_BARRIER();
    MARK(7);
    FLUSH_STAMPS();
    return;
  }

  const int a = w - 4;
  const rsrc_t rx = mk_rsrc(p.x, NB * DM * 2);
  const unsigned xrow = ((lane >> 2) & 3) * (DM * 2);
  const bool mlp = h < 28 && !(ABL & 2);
  if (P_TLB && a == 1) {
    const unsigned short* tp[8] = {p.v + ((size_t)(b * NH + h) * SK + sp * 2048) * DH, p.wo + (size_t)h * DH * DM, p.w1 + (size_t)bs * 64 * FF, p.w1 + (size_t)(bs * 64 + 63) * FF,
                                   p.w2 + (size_t)(h < 28 ? h : 0) * 64 * DM, p.b1, p.b2, p.out};
    if (P_TLB > 1) __builtin_amdgcn_s_sleep(P_TLB);
    unsigned t0, t1, t2, t3, t4, t5, t6, t7;
    asm volatile("s_load_dword %0, %8, 0x0\n\ts_load_dword %1, %9, 0x0\n\ts_load_dword %2, %10, 0x0\n\ts_load_dword %3, %11, 0x0\n\t"
                 "s_load_dword %4, %12, 0x0\n\ts_load_dword %5, %13, 0x0\n\ts_load_dword %6, %14, 0x0\n\ts_load_dword %7, %15, 0x0\n\ts_waitcnt lgkmcnt(0)"
                 : "=&s"(t0), "=&s"(t1), "=&s"(t2), "=&s"(t3), "=&s"(t4), "=&s"(t5), "=&s"(t6), "=&s"(t7)
                 : "s"(tp[0]), "s"(tp[1]), "s"(tp[2]), "s"(tp[3]), "s"(tp[4]), "s"(tp[5]), "s"(tp[6]), "s"(tp[7]));
  }
  if (a == 0) {
    v4u xs[4], xa[2], T[16];
#pragma unroll
    for (int t = 0; t < 4; ++t) xs[t] = ld128<0>(rx, xrow + ((lane & 3) + 4 * r) * 16, t * 256);
#pragma unroll
    for (int s = 0; s < 2; ++s) xa[s] = ld128<0>(rx, xrow + r * 16, bs * 128 + s * 64);
    FENCE();
    const rsrc_t rq = mk_rsrc(p.wq + (size_t)bs * 64 * (NH * DH) + h * DH, (ABL & 8) ? 0 : 64 * NH * DH * 2);
#pragma unroll
    for (int i = 0; i < 16; ++i) T[i] = ld128<AUX_WQ>(rq, r * 8 * 8192 + c * 16, ((i >> 3) * 32 + (i & 7)) * 8192);
    const unsigned ep = __builtin_amdgcn_raw_buffer_load_b32(rws, 0, WS_E, AUX_SC1);
    FENCE();
    MARK(10);
    float mean, inv;
    ln_stats(xs, mean, inv);
    const v4u A0 = ln_apply(xa[0], mean, inv), A1 = ln_apply(xa[1], mean, inv);
    f32x4 g[2];
    static_for<8>([&](auto J) { constexpr int j = J;
      f32x4 d = mfma(A0, bfrag<j>(T), f32x4{0.f, 0.f, 0.f, 0.f}); d = mfma(A1, bfrag<j>(T + 8), d); g[j >> 2][j & 3] = d[0]; });
    FENCE();
    const unsigned tg = __builtin_amdgcn_readfirstlane(ep) + 1u;
#pragma unroll
    for (int j = 0; j < 2; ++j) st128(tag4(g[j], tg), rws, (ABL & 4) ? OOB : r * 4096 + c * 16 + j * 256, WS_QX + h * 16384 + bs * 512);
    MARK(11);
    DRAIN();
    MARK(1);
    if (P_QD) __builtin_amdgcn_s_sleep(P_QD);
    f32x4 q4;
#pragma unroll 1
    for (unsigned spin = 0; spin < SPIN_CAP; ++spin) {
      v4u G[4];
#pragma unroll
      for (int j = 0; j < 4; ++j) G[j] = ld128<AUX_SC1>(rws, (ABL & 4) ? OOB : l16, WS_QX + h * 16384 + b * 4096 + j * 1024);
      unsigned bad = 0; q4 = f32x4{0.f, 0.f, 0.f, 0.f};
#pragma unroll
      for (int j = 0; j < 4; ++j) bad |= take4(G[j], tg, q4);
      if (!__any((bad & 0xfffu) != 0u) || (ABL & 4)) break;
      if (P_QS) __builtin_amdgcn_s_sleep(P_QS);
      asm volatile("" ::: "memory");
    }
    *(unsigned*)(lds + L_Q + c * 16 + (r & 1) * 8 + (r >> 1) * 4) = pk_bf16(tr32(q4[0], q4[2]), tr32(q4[1], q4[3]));
    if (lane == 0) { *(v4u*)(lds + L_M) = v4u{SENT, SENT, SENT, SENT}; *(v2u*)(lds + L_TAG) = v2u{tg, 0u}; }
  }
  MARK(2);
  WG_BARRIER();
  MARK(3);
  const unsigned tag = __builtin_amdgcn_readfirstlane(*(const unsigned*)(lds + L_TAG));
  if (a == 2 && mlp) {
    v4u xs[4], xa[2]; v2u U[16];
#pragma unroll
    for (int t = 0; t < 4; ++t) xs[t] = ld128<0>(rx, xrow + ((lane & 3) + 4 * r) * 16, t * 256);
#pragma unroll
    for (int s = 0; s < 2; ++s) xa[s] = ld128<0>(rx, xrow + r * 16, bs * 128 + s * 64);
    const rsrc_t r1 = mk_rsrc(p.w1 + (size_t)bs * 64 * FF + h * 64, 64 * FF * 2);
#pragma unroll
    for (int i = 0; i < 16; ++i) U[i] = ld64<AUX_W12>(r1, r * 8 * (FF * 2) + c * 8, ((i >> 3) * 32 + (i & 7)) * (FF * 2));
    FENCE();
#pragma unroll 1
    for (int it = 0; it < 2; ++it) {
      float mean, inv;
      ln_stats(xs, mean, inv);
#pragma unroll
      for (int t = 0; t < 4; ++t) xs[t] = ln_apply(xs[t], mean, inv);
      xa[0] = ln_apply(xa[0], mean, inv); xa[1] = ln_apply(xa[1], mean, inv);
    }
    st128(tag4(tile64(xa[0], xa[1], U), tag), rws, l16, WS_X1 + h * 8192 + bs * 1024);
  }
  for (int i = 0; i < (a == 2 && mlp ? P_WD2 : P_WD); ++i) __builtin_amdgcn_s_sleep(127);
  MARK(4);
  v4u Bf[4][8];
  {
    const rsrc_t ro = mk_rsrc(p.wo + (size_t)h * DH * DM + a * 128, (ABL & 1) ? 0 : DH * DM * 2);
    const unsigned vo = r * 8 * 1024 + c * 16;
    v4u T[32];
    static_for<32 + DO>([&](auto N) { constexpr int n = N;
      if constexpr (n >= DO) TOUCH(T[n - DO]);
      FENCE();
      if constexpr (n < 32) T[n] = ld128<AUX_WO>(ro, vo, (((n >> 3) * 32 + (n & 7)) ^ rot) * 1024);
      FENCE();
      if constexpr (n >= DO && ((n - DO) & 7) == 7) { constexpr int s = (n - DO) >> 3;
        static_for<8>([&](auto J) { constexpr int j = J; Bf[s][j] = bfrag<j>(T + 8 * s); PIN(Bf[s][j]); }); }
      FENCE(); });
  }
  MARK(5);
  const unsigned co = r * 2 + (lane & 1) * 16;
  if (a == 3) {
    unsigned mb;
#pragma unroll 1
    for (unsigned spin = 0; spin < SPIN_CAP; ++spin) {
      mb = __hip_atomic_load((lds_u32*)lds + L_M / 4 + (lane & 3), __ATOMIC_RELAXED, __HIP_MEMORY_SCOPE_WORKGROUP);
      if (__all(mb != SENT)) break;
      __builtin_amdgcn_s_sleep(4);
    }
    asm volatile("" ::: "memory");
    const float m4 = __uint_as_float(mb);
    float M = fmaxf(m4, dpp<XOR1>(m4)); M = fmaxf(M, dpp<XOR2>(M));
    float L = 0.f;
#pragma unroll
    for (int i = 0; i < 8; ++i) { const f32x4 s4 = *(const f32x4*)(lds + L_S + i * 1024 + l16);
#pragma unroll
      for (int j = 0; j < 4; ++j) L += __builtin_amdgcn_exp2f(s4[j] - M); }
    L = wave_sum(L);
    const float lse = M + __builtin_amdgcn_logf(L);
    const unsigned tk = *(const unsigned*)(lds + L_T);
    st128(v4u{__float_as_uint(lse), tag, tk, tag}, rws, lane == 0 ? 0u : OOB, WS_LS + (b * 64 + 2 * h + sp) * 16);
    MARK(6);
    float lo;
#pragma unroll 1
    for (unsigned spin = 0; spin < SPIN_CAP; ++spin) {
      __builtin_amdgcn_s_sleep(P_WAIT);
      const v4u m = ld128<AUX_SC1>(rws, l16, WS_LS + b * 1024);
      const bool ok = ((m[1] ^ tag) | (m[3] ^ tag)) == 0u;
      const unsigned tn = (unsigned)__builtin_amdgcn_s_memrealtime();
      const float dt = wave_sum((float)(int)((ok ? m[2] : tn) - tk)) * (1.0f / 64);
      __hip_atomic_store((lds_u32*)lds + L_REF / 4, (tk + (int)dt - P_OFF) | 1u, __ATOMIC_RELAXED, __HIP_MEMORY_SCOPE_WORKGROUP);
      lo = __uint_as_float(__builtin_amdgcn_readlane(m[0], 2 * h + (sp ^ 1)));
      if ((__ballot(ok) >> (2 * h + (sp ^ 1))) & 1) break;
      asm volatile("" ::: "memory");
    }
    *(float*)(lds + L_F + 4 * (lane & 3)) = __builtin_amdgcn_exp2f(m4 - M) * __builtin_amdgcn_rcpf(L) * __builtin_amdgcn_rcpf(1.0f + __builtin_amdgcn_exp2f(lo - lse));
  } else if (a == 2) {
    const unsigned b2v = __builtin_amdgcn_raw_buffer_load_b16(mk_rsrc(p.b2, DM * 2), co, col0 * 2, 0);
    const unsigned xv = __builtin_amdgcn_raw_buffer_load_b16(rx, co, (b * DM + col0) * 2, 0);
    if (mlp) {
      v2u U[16];
      const rsrc_t r2 = mk_rsrc(p.w2 + (size_t)h * 64 * DM + bs * 64, 64 * DM * 2);
#pragma unroll
      for (int i = 0; i < 16; ++i) U[i] = ld64<AUX_W12>(r2, r * 8 * 1024 + c * 8, ((i >> 3) * 32 + (i & 7)) * 1024);
      const v2u b1v = ld64<0>(mk_rsrc(p.b1 + h * 64, 128), c * 8, 0);
      DRAIN();
      f32x4 z4;
#pragma unroll 1
      for (unsigned spin = 0; spin < SPIN_CAP; ++spin) {
        v4u G[8];
#pragma unroll
        for (int j = 0; j < 8; ++j) G[j] = ld128<AUX_SC1>(rws, l16, WS_X1 + h * 8192 + j * 1024);
        unsigned bad = 0; z4 = f32x4{0.f, 0.f, 0.f, 0.f};
#pragma unroll
        for (int j = 0; j < 8; ++j) bad |= take4(G[j], tag, z4);
        if (!__any((bad & 0xfffu) != 0u)) break;
        __builtin_amdgcn_s_sleep(32);
        asm volatile("" ::: "memory");
      }
      float hv[4];
#pragma unroll
      for (int j = 0; j < 4; ++j) {
        const float z = rbf(rbf(z4[j]) + ((j & 1) ? bfhi(b1v[j >> 1]) : bflo(b1v[j >> 1])));
        const float e2 = __builtin_amdgcn_exp2f((-2.0f * 0.7978845608028654f * 1.4426950408889634f) * (z + 0.044715f * z * z * z));
        hv[j] = z * __builtin_amdgcn_rcpf(1.0f + e2);
      }
      *(v2u*)(lds + L_H + r * 128 + c * 8) = v2u{pk_bf16(hv[0], hv[1]), pk_bf16(hv[2], hv[3])};
      LDS_WAIT();
      const v4u A0 = *(const v4u*)(lds + L_H + ((lane >> 2) & 3) * 128 + r * 16), A1 = *(const v4u*)(lds + L_H + ((lane >> 2) & 3) * 128 + r * 16 + 64);
      st128(tag4(tile64(A0, A1, U), tag), rws, r * (28 * 2048) + c * 16, WS_MP + h * 2048 + bs * 256);
    } else { __builtin_amdgcn_s_sleep(127); __builtin_amdgcn_s_sleep(64); }
    MARK(6);
    for (int i = 0; i < P_MD; ++i) __builtin_amdgcn_s_sleep(127);
    f32x4 m4;
#pragma unroll 1
    for (unsigned spin = 0; spin < SPIN_CAP; ++spin) {
      __builtin_amdgcn_s_sleep(127);
      const v4u G = ld128<AUX_SC1>(rws, lane < 56 ? (unsigned)((lane >> 1) * 2048 + (lane & 1) * 32) : OOB, WS_MP + b * (28 * 2048) + col0 * 4);
      m4 = f32x4{0.f, 0.f, 0.f, 0.f};
      const unsigned bad = take4(G, tag, m4);
      if (!__any(lane < 56 && (bad & 0xfffu) != 0u) || (ABL & 2)) break;
      asm volatile("" ::: "memory");
    }
    const float m = rbf(rbf(sum4_parity(m4)) + bflo(b2v));
    const float gate = rbf(m * __builtin_amdgcn_rcpf(1.0f + __builtin_amdgcn_exp2f(-1.4426950408889634f * m)));
    *(f32x2*)(lds + L_GX + co * 4) = f32x2{gate, bflo(xv)};
  }
  MARK(9);
#pragma unroll 1
  for (int pass = P_DRY ? 0 : 1; pass < 2; ++pass) {
    const unsigned dry = pass ? 0u : OOB;
    if (pass) { WG_BARRIER(); MARK(7); }
    v4u A[4];
#pragma unroll
    for (int s = 0; s < 4; ++s) A[s] = *(const v4u*)(lds + L_O + (lane & 7) * O_ROW + s * 64 + r * 16);
    const f32x4 f = *(const f32x4*)(lds + L_F);
    const f32x2 gx = *(const f32x2*)(lds + L_GX + co * 4);
    float t[8];
#pragma unroll
    for (int j = 0; j < 8; ++j) {
      f32x4 d = mfma(A[0], Bf[0][j], f32x4{0.f, 0.f, 0.f, 0.f});
#pragma unroll
      for (int s = 1; s < 4; ++s) d = mfma(A[s], Bf[s][j], d);
      t[j] = fmaf(d[3], f[3], d[2] * f[2]) + fmaf(d[1], f[1], d[0] * f[0]);
    }
    const f32x4 tv = {tr16(t[0], t[4]), tr16(t[1], t[5]), tr16(t[2], t[6]), tr16(t[3], t[7])};
    st128(tag4(tv, tag), rws, lane < 32 ? l16 | dry : OOB, WS_TB + (b * 64 + 2 * h + sp) * 2048 + a * 512);
    if (pass) MARK(10);
    if (a == 0) {
      const unsigned go = ((lane >> 1) * 2048 + (lane & 1) * 16) | dry;
      f32x4 s4;
      __builtin_amdgcn_s_sleep(P_PD);
      if (pass) MARK(11);
#pragma unroll 1
      for (unsigned spin = 0; spin < SPIN_CAP; ++spin) {
        const v4u G0 = ld128<AUX_SC1>(rws, go, WS_TB + b * 131072 + cons * 32), G1 = ld128<AUX_SC1>(rws, go, WS_TB + b * 131072 + 65536 + cons * 32);
        s4 = f32x4{0.f, 0.f, 0.f, 0.f};
        unsigned bad = take4(G0, tag, s4) | take4(G1, tag, s4);
        if (!__any((bad & 0xfffu) != 0u) || !pass) break;
        __builtin_amdgcn_s_sleep(P_PS);
        asm volatile("" ::: "memory");
      }
      const float o = gx[1] + rbf(rbf(sum4_parity(s4)) * gx[0]);
      __builtin_amdgcn_raw_buffer_store_b16((unsigned short)bf_bits(o), mk_rsrc(p.out, NB * DM * 2), (lane & 14) == 0 ? co | dry : OOB, (b * DM + col0) * 2, 0);
      __builtin_amdgcn_raw_buffer_store_b32(tag, rws, (blk == 0 && lane == 0) ? WS_E | dry : OOB, 0, AUX_SC1);
    }
  }
  MARK(8);
  FLUSH_STAMPS();
  if (DIAG && blk == DIAG_BLK && a == 0) { LDS_WAIT(); const unsigned tm = __builtin_amdgcn_readfirstlane(*(const unsigned*)(lds + L_DIAG));
    while ((unsigned)__builtin_amdgcn_s_memrealtime() - tm < 10000) __builtin_amdgcn_s_sleep(1); }
}

extern "C" void kernel_launch(void* const* d_in, const int* in_sizes, int n_in, void* d_out, int out_size, void* d_ws, size_t ws_size,
                              hipStream_t stream) {
  Params p;
  p.x = (const unsigned short*)d_in[0]; p.k = (const unsigned short*)d_in[1]; p.v = (const unsigned short*)d_in[2];
  p.wq = (const unsigned short*)d_in[3]; p.wo = (const unsigned short*)d_in[4];
  p.w1 = (const unsigned short*)d_in[6]; p.b1 = (const unsigned short*)d_in[7]; p.w2 = (const unsigned short*)d_in[8]; p.b2 = (const unsigned short*)d_in[9];
  p.out = (unsigned short*)d_out;
  p.ws = (unsigned char*)d_ws;
  k_fused<<<256, 512, 0, stream>>>(p);
}
```

```cpp
#include <hip/hip_runtime.h>
#include <hip/hip_bf16.h>
#include <stdint.h>
#include <utility>

#define P_D 6
#define P_D0 6
#define P_N0 0
#define P_NA 40
#define P_DO 3
#define AUX_KV 2
#define AUX_WQ 2
#define AUX_WO 0
#define AUX_W12 2
#define P_BAND 5
#define P_KFIX 2
#define P_TAUV 215
#define P_OFF 110
#define P_WAIT 100
#define P_PD 20
#define P_PS 1
#define P_DRY 0
#define P_QD 0
#define P_QS 0
#define P_WD 0
#define P_WD2 0
#define P_MD 0
#define P_TLB 0
#define AUX_OUT 0
#define P_FL 1
#define ABL 0
#define DIAG 1
#define DIAG_W 0
#define DIAG_BLK 0

typedef unsigned v4u __attribute__((ext_vector_type(4)));
typedef unsigned v2u __attribute__((ext_vector_type(2)));
typedef float f32x4 __attribute__((ext_vector_type(4)));
typedef float f32x2 __attribute__((ext_vector_type(2)));
typedef __bf16 bf16x2 __attribute__((ext_vector_type(2)));
typedef __bf16 bf16x8 __attribute__((ext_vector_type(8)));
typedef __amdgpu_buffer_rsrc_t rsrc_t;
typedef __attribute__((address_space(3))) unsigned lds_u32;

constexpr int NB = 4, DM = 512, NH = 32, SK = 4096, DH = 128, FF = 1792;
constexpr int AUX_SC1 = 16;
constexpr unsigned SENT = 0xFFFFFFFFu, OOB = 0x80000000u;
constexpr unsigned SPIN_CAP = 1u << 14;

template <class F, int... I> __device__ __forceinline__ void static_for_impl(F&& f, std::integer_sequence<int, I...>) { (f(std::integral_constant<int, I>{}), ...); }
template <int N, class F> __device__ __forceinline__ void static_for(F&& f) { static_for_impl(f, std::make_integer_sequence<int, N>{}); }

__device__ __forceinline__ float dot2(unsigned a, unsigned b, float acc) {
  return __builtin_amdgcn_fdot2_f32_bf16(__builtin_bit_cast(bf16x2, a), __builtin_bit_cast(bf16x2, b), acc, false);
}
__device__ __forceinline__ f32x4 mfma(v4u a, v4u b, f32x4 c) {
  return __builtin_amdgcn_mfma_f32_16x16x32_bf16(__builtin_bit_cast(bf16x8, a), __builtin_bit_cast(bf16x8, b), c, 0, 0, 0);
}
__device__ __forceinline__ float bflo(unsigned u) { return __uint_as_float(u << 16); }
__device__ __forceinline__ float bfhi(unsigned u) { return __uint_as_float(u & 0xffff0000u); }
__device__ __forceinline__ unsigned pk_bf16(float lo, float hi) { bf16x2 r; r.x = (__bf16)lo; r.y = (__bf16)hi; return __builtin_bit_cast(unsigned, r); }
__device__ __forceinline__ unsigned bf_bits(float x) { return (unsigned)__builtin_bit_cast(unsigned short, (__bf16)x); }
__device__ __forceinline__ float rbf(float x) { return bflo(bf_bits(x)); }
__device__ __forceinline__ unsigned pair_lo(unsigned e, unsigned o) { return __builtin_amdgcn_perm(o, e, 0x05040100u); }
__device__ __forceinline__ unsigned pair_hi(unsigned e, unsigned o) { return __builtin_amdgcn_perm(o, e, 0x07060302u); }
template <int CTRL> __device__ __forceinline__ float dpp(float x) { return __int_as_float(__builtin_amdgcn_mov_dpp(__float_as_int(x), CTRL, 0xf, 0xf, true)); }
constexpr int XOR1 = 0xB1, XOR2 = 0x4E, XOR7 = 0x141, XOR8 = 0x128, ROR4 = 0x124, BCAST = 0x150;
__device__ __forceinline__ float tr32(float a, float b) {
  auto s = __builtin_amdgcn_permlane32_swap(__float_as_uint(a), __float_as_uint(b), false, false);
  return __uint_as_float(s[0]) + __uint_as_float(s[1]);
}
__device__ __forceinline__ float tr16(float a, float b) {
  auto s = __builtin_amdgcn_permlane16_swap(__float_as_uint(a), __float_as_uint(b), false, false);
  return __uint_as_float(s[0]) + __uint_as_float(s[1]);
}
template <int CTRL> __device__ __forceinline__ float trd(float lo, float hi, bool up) {
  const float t1 = lo + dpp<CTRL>(lo), t2 = hi + dpp<CTRL>(hi);
  return up ? t2 : t1;
}
__device__ __forceinline__ float wave_sum(float x) {
  x += dpp<XOR1>(x); x += dpp<XOR2>(x); x += dpp<XOR7>(x); x += dpp<XOR8>(x);
  x = tr16(x, x); x = tr32(x, x);
  return x;
}
__device__ __forceinline__ float wave_max(float x) {
  x = fmaxf(x, dpp<XOR1>(x)); x = fmaxf(x, dpp<XOR2>(x)); x = fmaxf(x, dpp<XOR7>(x)); x = fmaxf(x, dpp<XOR8>(x));
  auto s = __builtin_amdgcn_permlane16_swap(__float_as_uint(x), __float_as_uint(x), false, false);
  x = fmaxf(__uint_as_float(s[0]), __uint_as_float(s[1]));
  auto t = __builtin_amdgcn_permlane32_swap(__float_as_uint(x), __float_as_uint(x), false, false);
  return fmaxf(__uint_as_float(t[0]), __uint_as_float(t[1]));
}
__device__ __forceinline__ float sum4_parity(f32x4 v) {
  float s = tr16(tr32(v[0], v[2]), tr32(v[1], v[3]));
  s += dpp<XOR8>(s); s += dpp<ROR4>(s); s += dpp<XOR2>(s);
  return s;
}
__device__ __forceinline__ v4u tag4(f32x4 v, unsigned tag) {
  return ((__builtin_bit_cast(v4u, v) + 0x800u) & 0xfffff000u) | (tag & 0xfffu);
}
__device__ __forceinline__ unsigned take4(v4u g, unsigned tag, f32x4& acc) {
  acc += __builtin_bit_cast(f32x4, g & 0xfffff000u);
  return (g[0] ^ tag) | (g[1] ^ tag) | (g[2] ^ tag) | (g[3] ^ tag);
}
__device__ __forceinline__ rsrc_t mk_rsrc(const void* p, unsigned bytes) { return __builtin_amdgcn_make_buffer_rsrc(const_cast<void*>(p), 0, bytes, 0x00020000); }
template <int AUX> __device__ __forceinline__ v4u ld128(rsrc_t r, unsigned v, unsigned s) { return __builtin_amdgcn_raw_buffer_load_b128(r, v, s, AUX); }
template <int AUX> __device__ __forceinline__ v2u ld64(rsrc_t r, unsigned v, unsigned s) { return __builtin_amdgcn_raw_buffer_load_b64(r, v, s, AUX); }
__device__ __forceinline__ void st128(v4u d, rsrc_t r, unsigned v, unsigned s) { __builtin_amdgcn_raw_buffer_store_b128(d, r, v + s, 0, AUX_SC1); }
__device__ __forceinline__ void st64(v2u d, rsrc_t r, unsigned v, unsigned s) { __builtin_amdgcn_raw_buffer_store_b64(d, r, v + s, 0, AUX_SC1); }
__device__ __forceinline__ float rows4_sum(float x) { x += dpp<XOR1>(x); x += dpp<XOR2>(x); x = tr16(x, x); x = tr32(x, x); return x; }
__device__ __forceinline__ void ln_stats(const v4u (&x)[4], float& mean, float& inv) {
  float s = 0.f;
#pragma unroll
  for (int t = 0; t < 4; ++t)
#pragma unroll
    for (int j = 0; j < 4; ++j) s += bflo(x[t][j]) + bfhi(x[t][j]);
  const float mu = rows4_sum(s) * (1.0f / DM);
  float q = 0.f;
#pragma unroll
  for (int t = 0; t < 4; ++t)
#pragma unroll
    for (int j = 0; j < 4; ++j) { const float d0 = bflo(x[t][j]) - mu, d1 = bfhi(x[t][j]) - mu; q = fmaf(d0, d0, q); q = fmaf(d1, d1, q); }
  const float var = rows4_sum(q) * (1.0f / DM);
  mean = rbf(mu);
  inv = __builtin_amdgcn_rcpf(rbf(__builtin_amdgcn_sqrtf(rbf(rbf(var) + 1e-5f))));
}
__device__ __forceinline__ v4u ln_apply(v4u x, float mean, float inv) {
  v4u o;
#pragma unroll
  for (int j = 0; j < 4; ++j) { const unsigned d = pk_bf16(bflo(x[j]) - mean, bfhi(x[j]) - mean); o[j] = pk_bf16(bflo(d) * inv, bfhi(d) * inv); }
  return o;
}
template <int J> __device__ __forceinline__ v4u bfrag(const v4u* t) {
  v4u f;
#pragma unroll
  for (int d = 0; d < 4; ++d) f[d] = (J & 1) ? pair_hi(t[2 * d][J >> 1], t[2 * d + 1][J >> 1]) : pair_lo(t[2 * d][J >> 1], t[2 * d + 1][J >> 1]);
  return f;
}
template <int J> __device__ __forceinline__ v4u bfrag2(const v2u* t) {
  v4u f;
#pragma unroll
  for (int d = 0; d < 4; ++d) f[d] = (J & 1) ? pair_hi(t[2 * d][J >> 1], t[2 * d + 1][J >> 1]) : pair_lo(t[2 * d][J >> 1], t[2 * d + 1][J >> 1]);
  return f;
}
__device__ __forceinline__ f32x4 tile64(v4u a0, v4u a1, const v2u* t) {
  f32x4 o;
  static_for<4>([&](auto J) { constexpr int j = J;
    f32x4 d = mfma(a0, bfrag2<j>(t), f32x4{0.f, 0.f, 0.f, 0.f}); d = mfma(a1, bfrag2<j>(t + 8), d); o[j] = d[0]; });
  return o;
}
#define TOUCH(x) asm volatile("" :: "v"(x))
#define PIN(x) asm volatile("" : "+v"(x))
#define THROTTLE(mask, i) asm volatile("s_bitcmp1_b32 %0, %1\n\ts_cbranch_scc0 1f\n\ts_waitcnt vmcnt(" "3" ")\n1:" :: "s"(mask), "n"(i) : "scc")
#define FENCE() __builtin_amdgcn_sched_barrier(0)
#define WG_BARRIER() asm volatile("s_waitcnt lgkmcnt(0)\n\ts_barrier" ::: "memory")
#define DRAIN() asm volatile("s_waitcnt vmcnt(0)" ::: "memory")
#define LDS_WAIT() asm volatile("s_waitcnt lgkmcnt(0)" ::: "memory")
#define MARK(k) do { if (DIAG == (k) + 1 && w == DIAG_W) { FENCE(); *(unsigned*)(lds + L_DIAG) = (unsigned)__builtin_amdgcn_s_memrealtime(); FENCE(); } } while (0)
#define FLUSH_STAMPS()

constexpr unsigned WS_E = 0;
constexpr unsigned WS_LS = 4096;
constexpr unsigned WS_X1 = 65536;
constexpr unsigned WS_MP = WS_X1 + 32 * 8192;
constexpr unsigned WS_TB = WS_MP + 4 * 32 * 2048;
constexpr unsigned WS_QX = WS_TB + 4 * 64 * 2048;
constexpr unsigned WS_BYTES = WS_QX + 32 * 4 * 8 * 512;

struct Params {
  const unsigned short* x; const unsigned short* k; const unsigned short* v; const unsigned short* wq; const unsigned short* wo;
  const unsigned short* w1; const unsigned short* b1; const unsigned short* w2; const unsigned short* b2;
  unsigned short* out; unsigned char* ws;
};

constexpr int D = P_D, NA = P_NA, DO = P_DO;
constexpr int LD = 8;
constexpr int NU = (NA <= 48 + LD - 12) ? 48 : 64;
static_assert((NU + LD - NA) * 4 >= NU && D <= LD, "the backlog is gone before the loop starts");
constexpr float C2 = 0.088388347648318440f * 1.4426950408889634f;

constexpr int L_Q = 0, L_M = 256, L_F = 272, L_TAG = 288, L_REF = 292, L_T = 304, L_GX = 320, L_DIAG = 512, L_H = 1024, L_O = 1536, O_ROW = 272, L_S = L_O + 8 * O_ROW, L_END = L_S + 8192;

__global__ void __launch_bounds__(512, 2) k_fused(Params p) {
  __shared__ __attribute__((aligned(16))) unsigned char lds[L_END];
  const int blk = blockIdx.x, tid = threadIdx.x, lane = tid & 63, w = __builtin_amdgcn_readfirstlane(tid >> 6);
  MARK(0);
  asm volatile("" :: "s"(p.x), "s"(p.k), "s"(p.v), "s"(p.wq), "s"(p.wo), "s"(p.w1), "s"(p.b1), "s"(p.w2), "s"(p.b2), "s"(p.out), "s"(p.ws));
  const int idx = blk >> 3, h = (blk & 7) * 4 + (idx & 3), bs = idx >> 2, b = bs >> 1, sp = bs & 1;
  const int cons = (blk & 7) * 8 + (idx & 7);
  const int col0 = 128 * (cons >> 4) + 16 * (cons & 7) + 4 * ((cons >> 3) & 1);
  const int r = lane >> 4, c = lane & 15;
  const unsigned l16 = lane * 16;
  const rsrc_t rws = mk_rsrc(p.ws, WS_BYTES);
  const unsigned rot = (bs >> 1) * 32 + (bs & 1) * 4;

  if (__builtin_expect(w < 4, 1)) {
    __builtin_amdgcn_s_setprio(2);
    const size_t kvoff = ((size_t)(b * NH + h) * SK + sp * 2048 + w * 4) * DH;
    const unsigned short* kp = p.k + kvoff; const unsigned short* vp = p.v + kvoff;
    const unsigned kvlen = 524288 - w * 1024;
    const rsrc_t rk = mk_rsrc(kp, kvlen);
    float* const scs = (float*)(lds + L_S + w * 2048) + lane;
    v4u S[NU + LD], q;
    auto pace = [&](int lv) {
      unsigned m = 0xffffu; m = lv > 0 ? 0x5555u : m; m = lv > 1 ? 0x9249u : m; m = lv > 2 ? 0x1111u : m;
      m = lv > 3 ? 0x0421u : m; m = lv > 4 ? 0x0101u : m; m = lv > 5 ? 0u : m;
      return (unsigned)__builtin_amdgcn_readfirstlane(m); };
    float ka[16], mw = -INFINITY;
    auto kdot = [&](v4u t, int slot) {
      float d = dot2(t[0], q[0], 0.f); d = dot2(t[1], q[1], d); d = dot2(t[2], q[2], d); d = dot2(t[3], q[3], d); PIN(d); ka[slot] = d; };
    auto kfold = [&](int g) {
#pragma unroll
      for (int j = 0; j < 8; ++j) ka[j] = trd<XOR8>(ka[j], ka[j + 8], lane & 8);
#pragma unroll
      for (int j = 0; j < 4; ++j) ka[j] = trd<XOR7>(ka[j], ka[j + 4], lane & 4);
#pragma unroll
      for (int j = 0; j < 2; ++j) ka[j] = trd<XOR2>(ka[j], ka[j + 2], lane & 2);
      const float s = trd<XOR1>(ka[0], ka[1], lane & 1) * C2;
      scs[g * 64] = s; mw = fmaxf(mw, s); PIN(mw); };
    static_for<NU + LD>([&](auto N) {
      constexpr int n = N;
      constexpr int dn = n < P_N0 ? P_D0 : D;
      if constexpr (n >= dn) TOUCH(S[n - dn]);
      FENCE();
      if constexpr (n == (P_N0 ? P_D0 : D)) MARK(1);
      S[n] = ld128<AUX_KV>(rk, l16, n * 4096);
      FENCE();
      if constexpr (n == NA - 1) {
        MARK(2);
        WG_BARRIER();
        MARK(3);
        q = *(const v4u*)(lds + L_Q + c * 16);
      }
      if constexpr (n >= NA) {
        constexpr int hi = (n - NA + 1) * 4 < n - LD + 1 ? (n - NA + 1) * 4 : n - LD + 1;
        constexpr int lo = (n - NA) * 4 < n - LD ? (n - NA) * 4 : n - LD;
        static_for<hi - lo>([&](auto J) { constexpr int i = lo + J; kdot(S[i], i & 15); if constexpr ((i & 15) == 15) kfold(i >> 4); });
      }
      FENCE();
    });
    v4u R[16];
#pragma unroll
    for (int i = 0; i < LD; ++i) R[i] = S[NU + i];
    float snext = scs[0];
#pragma unroll 1
    for (int g = NU / 16; g < 8; ++g) {
      const rsrc_t rn = mk_rsrc(g == 7 ? vp : kp, kvlen);
      const unsigned o0 = g * 65536, o1 = g == 7 ? 0u : o0 + 65536;
      const unsigned mask = pace(P_KFIX);
      static_for<16>([&](auto I) { constexpr int i = I;
        TOUCH(R[i]); THROTTLE(mask, i); FENCE();
        if constexpr (i + LD < 16) R[i + LD] = ld128<AUX_KV>(rk, l16, o0 + (i + LD) * 4096);
        else R[i + LD - 16] = ld128<AUX_KV>(rn, l16, o1 + (i + LD - 16) * 4096);
        FENCE();
        kdot(R[i], i);
        FENCE(); });
      kfold(g);
    }
    MARK(4);
    unsigned now = (unsigned)__builtin_amdgcn_s_memrealtime();
    mw = wave_max(mw);
    *(unsigned*)(lds + L_T + 4 * w) = now;
    unsigned refv = 0;
    asm volatile("" ::: "memory");
    __hip_atomic_store((lds_u32*)lds + L_M / 4 + w, min(__float_as_uint(mw), SENT - 1u), __ATOMIC_RELAXED, __HIP_MEMORY_SCOPE_WORKGROUP);
    float acc[8];
#pragma unroll
    for (int j = 0; j < 8; ++j) acc[j] = 0.f;
    const rsrc_t rv = mk_rsrc(vp, kvlen);
    MARK(5);
#pragma unroll 1
    for (int g = 0; g < 8; ++g) {
      const rsrc_t rn = mk_rsrc(vp, g == 7 ? 0u : kvlen);
      const unsigned o0 = g * 65536;
      const float pg = __builtin_amdgcn_exp2f(snext - mw);
      snext = scs[((g + 1) & 7) * 64];
      const unsigned rf = __builtin_amdgcn_readfirstlane(refv);
      const unsigned mask = pace(rf ? ((int)(now - rf) - g * P_TAUV) >> P_BAND : P_KFIX) & (g == 7 ? 0xffu : 0xffffu);
      static_for<16>([&](auto I) { constexpr int i = I;
        TOUCH(R[i]); THROTTLE(mask, i); FENCE();
        R[(i + LD) & 15] = ld128<AUX_KV>(i + LD < 16 ? rv : rn, l16, o0 + (i + LD) * 4096);
        if constexpr (i == 15) { now = (unsigned)__builtin_amdgcn_s_memrealtime();
          refv = __hip_atomic_load((lds_u32*)lds + L_REF / 4, __ATOMIC_RELAXED, __HIP_MEMORY_SCOPE_WORKGROUP); }
        FENCE();
        const v4u t = R[i]; const float pb = dpp<BCAST + i>(pg);
#pragma unroll
        for (int j = 0; j < 4; ++j) { acc[2 * j] = fmaf(pb, bflo(t[j]), acc[2 * j]); acc[2 * j + 1] = fmaf(pb, bfhi(t[j]), acc[2 * j + 1]); }
        asm volatile("" : "+v"(acc[0]), "+v"(acc[1]), "+v"(acc[2]), "+v"(acc[3]), "+v"(acc[4]), "+v"(acc[5]), "+v"(acc[6]), "+v"(acc[7]));
        FENCE(); });
    }
    MARK(6);
    {
      float t4[4];
#pragma unroll
      for (int j = 0; j < 4; ++j) t4[j] = tr32(acc[j], acc[j + 4]);
      const float o0 = tr16(t4[0], t4[2]), o1 = tr16(t4[1], t4[3]);
      const unsigned hi = pk_bf16(o0, o1), lo = pk_bf16(o0 - bflo(hi), o1 - bfhi(hi));
      *(unsigned*)(lds + L_O + w * O_ROW + ((c * 16 + r * 4) ^ (rot * 2))) = hi;
      *(unsigned*)(lds + L_O + (4 + w) * O_ROW + ((c * 16 + r * 4) ^ (rot * 2))) = lo;
    }
    WG_BARRIER();
    MARK(7);
    FLUSH_STAMPS();
    return;
  }

  const int a = w - 4;
  const rsrc_t rx = mk_rsrc(p.x, NB * DM * 2);
  const unsigned xrow = ((lane >> 2) & 3) * (DM * 2);
  const bool mlp = h < 28 && !(ABL & 2);
  if (P_TLB && a == 1) {
    const unsigned short* tp[8] = {p.v + ((size_t)(b * NH + h) * SK + sp * 2048) * DH, p.wo + (size_t)h * DH * DM, p.w1 + (size_t)bs * 64 * FF, p.w1 + (size_t)(bs * 64 + 63) * FF,
                                   p.w2 + (size_t)(h < 28 ? h : 0) * 64 * DM, p.b1, p.b2, p.out};
    if (P_TLB > 1) __builtin_amdgcn_s_sleep(P_TLB);
    unsigned t0, t1, t2, t3, t4, t5, t6, t7;
    asm volatile("s_load_dword %0, %8, 0x0\n\ts_load_dword %1, %9, 0x0\n\ts_load_dword %2, %10, 0x0\n\ts_load_dword %3, %11, 0x0\n\t"
                 "s_load_dword %4, %12, 0x0\n\ts_load_dword %5, %13, 0x0\n\ts_load_dword %6, %14, 0x0\n\ts_load_dword %7, %15, 0x0\n\ts_waitcnt lgkmcnt(0)"
                 : "=&s"(t0), "=&s"(t1), "=&s"(t2), "=&s"(t3), "=&s"(t4), "=&s"(t5), "=&s"(t6), "=&s"(t7)
                 : "s"(tp[0]), "s"(tp[1]), "s"(tp[2]), "s"(tp[3]), "s"(tp[4]), "s"(tp[5]), "s"(tp[6]), "s"(tp[7]));
  }
  if (a == 0) {
    v4u xs[4], xa[2], T[16];
#pragma unroll
    for (int t = 0; t < 4; ++t) xs[t] = ld128<0>(rx, xrow + ((lane & 3) + 4 * r) * 16, t * 256);
#pragma unroll
    for (int s = 0; s < 2; ++s) xa[s] = ld128<0>(rx, xrow + r * 16, bs * 128 + s * 64);
    FENCE();
    const rsrc_t rq = mk_rsrc(p.wq + (size_t)bs * 64 * (NH * DH) + h * DH, (ABL & 8) ? 0 : 64 * NH * DH * 2);
#pragma unroll
    for (int i = 0; i < 16; ++i) T[i] = ld128<AUX_WQ>(rq, r * 8 * 8192 + c * 16, ((i >> 3) * 32 + (i & 7)) * 8192);
    const unsigned ep = __builtin_amdgcn_raw_buffer_load_b32(rws, 0, WS_E, AUX_SC1);
    FENCE();
    MARK(10);
    float mean, inv;
    ln_stats(xs, mean, inv);
    const v4u A0 = ln_apply(xa[0], mean, inv), A1 = ln_apply(xa[1], mean, inv);
    f32x4 g[2];
    static_for<8>([&](auto J) { constexpr int j = J;
      f32x4 d = mfma(A0, bfrag<j>(T), f32x4{0.f, 0.f, 0.f, 0.f}); d = mfma(A1, bfrag<j>(T + 8), d); g[j >> 2][j & 3] = d[0]; });
    FENCE();
    const unsigned tg = __builtin_amdgcn_readfirstlane(ep) + 1u;
#pragma unroll
    for (int j = 0; j < 2; ++j) st128(tag4(g[j], tg), rws, (ABL & 4) ? OOB : r * 4096 + c * 16 + j * 256, WS_QX + h * 16384 + bs * 512);
    MARK(11);
    DRAIN();
    MARK(1);
    if (P_QD) __builtin_amdgcn_s_sleep(P_QD);
    f32x4 q4;
#pragma unroll 1
    for (unsigned spin = 0; spin < SPIN_CAP; ++spin) {
      v4u G[4];
#pragma unroll
      for (int j = 0; j < 4; ++j) G[j] = ld128<AUX_SC1>(rws, (ABL & 4) ? OOB : l16, WS_QX + h * 16384 + b * 4096 + j * 1024);
      unsigned bad = 0; q4 = f32x4{0.f, 0.f, 0.f, 0.f};
#pragma unroll
      for (int j = 0; j < 4; ++j) bad |= take4(G[j], tg, q4);
      if (!__any((bad & 0xfffu) != 0u) || (ABL & 4)) break;
      if (P_QS) __builtin_amdgcn_s_sleep(P_QS);
      asm volatile("" ::: "memory");
    }
    *(unsigned*)(lds + L_Q + c * 16 + (r & 1) * 8 + (r >> 1) * 4) = pk_bf16(tr32(q4[0], q4[2]), tr32(q4[1], q4[3]));
    if (lane == 0) { *(v4u*)(lds + L_M) = v4u{SENT, SENT, SENT, SENT}; *(v2u*)(lds + L_TAG) = v2u{tg, 0u}; }
  }
  MARK(2);
  WG_BARRIER();
  MARK(3);
  const unsigned tag = __builtin_amdgcn_readfirstlane(*(const unsigned*)(lds + L_TAG));
  if (a == 2 && mlp) {
    v4u xs[4], xa[2]; v2u U[16];
#pragma unroll
    for (int t = 0; t < 4; ++t) xs[t] = ld128<0>(rx, xrow + ((lane & 3) + 4 * r) * 16, t * 256);
#pragma unroll
    for (int s = 0; s < 2; ++s) xa[s] = ld128<0>(rx, xrow + r * 16, bs * 128 + s * 64);
    const rsrc_t r1 = mk_rsrc(p.w1 + (size_t)bs * 64 * FF + h * 64, 64 * FF * 2);
#pragma unroll
    for (int i = 0; i < 16; ++i) U[i] = ld64<AUX_W12>(r1, r * 8 * (FF * 2) + c * 8, ((i >> 3) * 32 + (i & 7)) * (FF * 2));
    FENCE();
#pragma unroll 1
    for (int it = 0; it < 2; ++it) {
      float mean, inv;
      ln_stats(xs, mean, inv);
#pragma unroll
      for (int t = 0; t < 4; ++t) xs[t] = ln_apply(xs[t], mean, inv);
      xa[0] = ln_apply(xa[0], mean, inv); xa[1] = ln_apply(xa[1], mean, inv);
    }
    st128(tag4(tile64(xa[0], xa[1], U), tag), rws, l16, WS_X1 + h * 8192 + bs * 1024);
  }
  for (int i = 0; i < (a == 2 && mlp ? P_WD2 : P_WD); ++i) __builtin_amdgcn_s_sleep(127);
  MARK(4);
  v4u Bf[4][8];
  {
    const rsrc_t ro = mk_rsrc(p.wo + (size_t)h * DH * DM + a * 128, (ABL & 1) ? 0 : DH * DM * 2);
    const unsigned vo = r * 8 * 1024 + c * 16;
    v4u T[32];
    static_for<32 + DO>([&](auto N) { constexpr int n = N;
      if constexpr (n >= DO) TOUCH(T[n - DO]);
      FENCE();
      if constexpr (n < 32) T[n] = ld128<AUX_WO>(ro, vo, (((n >> 3) * 32 + (n & 7)) ^ rot) * 1024);
      FENCE();
      if constexpr (n >= DO && ((n - DO) & 7) == 7) { constexpr int s = (n - DO) >> 3;
        static_for<8>([&](auto J) { constexpr int j = J; Bf[s][j] = bfrag<j>(T + 8 * s); PIN(Bf[s][j]); }); }
      FENCE(); });
  }
  MARK(5);
  const unsigned co = r * 2 + (lane & 1) * 16;
  if (a == 3) {
    unsigned mb;
#pragma unroll 1
    for (unsigned spin = 0; spin < SPIN_CAP; ++spin) {
      mb = __hip_atomic_load((lds_u32*)lds + L_M / 4 + (lane & 3), __ATOMIC_RELAXED, __HIP_MEMORY_SCOPE_WORKGROUP);
      if (__all(mb != SENT)) break;
      __builtin_amdgcn_s_sleep(4);
    }
    asm volatile("" ::: "memory");
    const float m4 = __uint_as_float(mb);
    float M = fmaxf(m4, dpp<XOR1>(m4)); M = fmaxf(M, dpp<XOR2>(M));
    float L = 0.f;
#pragma unroll
    for (int i = 0; i < 8; ++i) { const f32x4 s4 = *(const f32x4*)(lds + L_S + i * 1024 + l16);
#pragma unroll
      for (int j = 0; j < 4; ++j) L += __builtin_amdgcn_exp2f(s4[j] - M); }
    L = wave_sum(L);
    const float lse = M + __builtin_amdgcn_logf(L);
    const unsigned tk = *(const unsigned*)(lds + L_T);
    st128(v4u{__float_as_uint(lse), tag, tk, tag}, rws, lane == 0 ? 0u : OOB, WS_LS + (b * 64 + 2 * h + sp) * 16);
    MARK(6);
    float lo;
#pragma unroll 1
    for (unsigned spin = 0; spin < SPIN_CAP; ++spin) {
      __builtin_amdgcn_s_sleep(P_WAIT);
      const v4u m = ld128<AUX_SC1>(rws, l16, WS_LS + b * 1024);
      const bool ok = ((m[1] ^ tag) | (m[3] ^ tag)) == 0u;
      const unsigned tn = (unsigned)__builtin_amdgcn_s_memrealtime();
      const float dt = wave_sum((float)(int)((ok ? m[2] : tn) - tk)) * (1.0f / 64);
      __hip_atomic_store((lds_u32*)lds + L_REF / 4, (tk + (int)dt - P_OFF) | 1u, __ATOMIC_RELAXED, __HIP_MEMORY_SCOPE_WORKGROUP);
      lo = __uint_as_float(__builtin_amdgcn_readlane(m[0], 2 * h + (sp ^ 1)));
      if ((__ballot(ok) >> (2 * h + (sp ^ 1))) & 1) break;
      asm volatile("" ::: "memory");
    }
    *(float*)(lds + L_F + 4 * (lane & 3)) = __builtin_amdgcn_exp2f(m4 - M) * __builtin_amdgcn_rcpf(L) * __builtin_amdgcn_rcpf(1.0f + __builtin_amdgcn_exp2f(lo - lse));
  } else if (a == 2) {
    const unsigned b2v = __builtin_amdgcn_raw_buffer_load_b16(mk_rsrc(p.b2, DM * 2), co, col0 * 2, 0);
    const unsigned xv = __builtin_amdgcn_raw_buffer_load_b16(rx, co, (b * DM + col0) * 2, 0);
    if (mlp) {
      v2u U[16];
      const rsrc_t r2 = mk_rsrc(p.w2 + (size_t)h * 64 * DM + bs * 64, 64 * DM * 2);
#pragma unroll
      for (int i = 0; i < 16; ++i) U[i] = ld64<AUX_W12>(r2, r * 8 * 1024 + c * 8, ((i >> 3) * 32 + (i & 7)) * 1024);
      const v2u b1v = ld64<0>(mk_rsrc(p.b1 + h * 64, 128), c * 8, 0);
      DRAIN();
      f32x4 z4;
#pragma unroll 1
      for (unsigned spin = 0; spin < SPIN_CAP; ++spin) {
        v4u G[8];
#pragma unroll
        for (int j = 0; j < 8; ++j) G[j] = ld128<AUX_SC1>(rws, l16, WS_X1 + h * 8192 + j * 1024);
        unsigned bad = 0; z4 = f32x4{0.f, 0.f, 0.f, 0.f};
#pragma unroll
        for (int j = 0; j < 8; ++j) bad |= take4(G[j], tag, z4);
        if (!__any((bad & 0xfffu) != 0u)) break;
        __builtin_amdgcn_s_sleep(32);
        asm volatile("" ::: "memory");
      }
      float hv[4];
#pragma unroll
      for (int j = 0; j < 4; ++j) {
        const float z = rbf(rbf(z4[j]) + ((j & 1) ? bfhi(b1v[j >> 1]) : bflo(b1v[j >> 1])));
        const float e2 = __builtin_amdgcn_exp2f((-2.0f * 0.7978845608028654f * 1.4426950408889634f) * (z + 0.044715f * z * z * z));
        hv[j] = z * __builtin_amdgcn_rcpf(1.0f + e2);
      }
      *(v2u*)(lds + L_H + r * 128 + c * 8) = v2u{pk_bf16(hv[0], hv[1]), pk_bf16(hv[2], hv[3])};
      LDS_WAIT();
      const v4u A0 = *(const v4u*)(lds + L_H + ((lane >> 2) & 3) * 128 + r * 16), A1 = *(const v4u*)(lds + L_H + ((lane >> 2) & 3) * 128 + r * 16 + 64);
      st128(tag4(tile64(A0, A1, U), tag), rws, r * (28 * 2048) + c * 16, WS_MP + h * 2048 + bs * 256);
    } else { __builtin_amdgcn_s_sleep(127); __builtin_amdgcn_s_sleep(64); }
    MARK(6);
    for (int i = 0; i < P_MD; ++i) __builtin_amdgcn_s_sleep(127);
    f32x4 m4;
#pragma unroll 1
    for (unsigned spin = 0; spin < SPIN_CAP; ++spin) {
      __builtin_amdgcn_s_sleep(127);
      const v4u G = ld128<AUX_SC1>(rws, lane < 56 ? (unsigned)((lane >> 1) * 2048 + (lane & 1) * 32) : OOB, WS_MP + b * (28 * 2048) + col0 * 4);
      m4 = f32x4{0.f, 0.f, 0.f, 0.f};
      const unsigned bad = take4(G, tag, m4);
      if (!__any(lane < 56 && (bad & 0xfffu) != 0u) || (ABL & 2)) break;
      asm volatile("" ::: "memory");
    }
    const float m = rbf(rbf(sum4_parity(m4)) + bflo(b2v));
    const float gate = rbf(m * __builtin_amdgcn_rcpf(1.0f + __builtin_amdgcn_exp2f(-1.4426950408889634f * m)));
    *(f32x2*)(lds + L_GX + co * 4) = f32x2{gate, bflo(xv)};
  }
  MARK(9);
#pragma unroll 1
  for (int pass = P_DRY ? 0 : 1; pass < 2; ++pass) {
    const unsigned dry = pass ? 0u : OOB;
    if (pass) { WG_BARRIER(); MARK(7); }
    v4u A[4];
#pragma unroll
    for (int s = 0; s < 4; ++s) A[s] = *(const v4u*)(lds + L_O + (lane & 7) * O_ROW + s * 64 + r * 16);
    const f32x4 f = *(const f32x4*)(lds + L_F);
    const f32x2 gx = *(const f32x2*)(lds + L_GX + co * 4);
    float t[8];
#pragma unroll
    for (int j = 0; j < 8; ++j) {
      f32x4 d = mfma(A[0], Bf[0][j], f32x4{0.f, 0.f, 0.f, 0.f});
#pragma unroll
      for (int s = 1; s < 4; ++s) d = mfma(A[s], Bf[s][j], d);
      t[j] = fmaf(d[3], f[3], d[2] * f[2]) + fmaf(d[1], f[1], d[0] * f[0]);
    }
    const f32x4 tv = {tr16(t[0], t[4]), tr16(t[1], t[5]), tr16(t[2], t[6]), tr16(t[3], t[7])};
    st128(tag4(tv, tag), rws, lane < 32 ? l16 | dry : OOB, WS_TB + (b * 64 + 2 * h + sp) * 2048 + a * 512);
    if (pass) MARK(10);
    if (a == 0) {
      const unsigned go = ((lane >> 1) * 2048 + (lane & 1) * 16) | dry;
      f32x4 s4;
      __builtin_amdgcn_s_sleep(P_PD);
      if (pass) MARK(11);
#pragma unroll 1
      for (unsigned spin = 0; spin < SPIN_CAP; ++spin) {
        const v4u G0 = ld128<AUX_SC1>(rws, go, WS_TB + b * 131072 + cons * 32), G1 = ld128<AUX_SC1>(rws, go, WS_TB + b * 131072 + 65536 + cons * 32);
        s4 = f32x4{0.f, 0.f, 0.f, 0.f};
        unsigned bad = take4(G0, tag, s4) | take4(G1, tag, s4);
        if (!__any((bad & 0xfffu) != 0u) || !pass) break;
        __builtin_amdgcn_s_sleep(P_PS);
        asm volatile("" ::: "memory");
      }
      const float o = gx[1] + rbf(rbf(sum4_parity(s4)) * gx[0]);
      __builtin_amdgcn_raw_buffer_store_b16((unsigned short)bf_bits(o), mk_rsrc(p.out, NB * DM * 2), (lane & 14) == 0 ? co | dry : OOB, (b * DM + col0) * 2, AUX_OUT);
      __builtin_amdgcn_raw_buffer_store_b32(tag, rws, (blk == 0 && lane == 0) ? WS_E | dry : OOB, 0, AUX_SC1);
    }
  }
  if (P_FL && a == 0) { DRAIN(); if (P_FL & 1) asm volatile("buffer_wbl2 sc0 sc1\n\ts_waitcnt vmcnt(0)" ::: "memory"); if (P_FL & 2) asm volatile("buffer_inv sc0 sc1\n\ts_waitcnt vmcnt(0)" ::: "memory"); }
  MARK(8);
  FLUSH_STAMPS();
  if (DIAG && blk == DIAG_BLK && a == 0) { LDS_WAIT(); const unsigned tm = __builtin_amdgcn_readfirstlane(*(const unsigned*)(lds + L_DIAG));
    while ((unsigned)__builtin_amdgcn_s_memrealtime() - tm < 10000) __builtin_amdgcn_s_sleep(1); }
}

extern "C" void kernel_launch(void* const* d_in, const int* in_sizes, int n_in, void* d_out, int out_size, void* d_ws, size_t ws_size,
                              hipStream_t stream) {
  Params p;
  p.x = (const unsigned short*)d_in[0]; p.k = (const unsigned short*)d_in[1]; p.v = (const unsigned short*)d_in[2];
  p.wq = (const unsigned short*)d_in[3]; p.wo = (const unsigned short*)d_in[4];
  p.w1 = (const unsigned short*)d_in[6]; p.b1 = (const unsigned short*)d_in[7]; p.w2 = (const unsigned short*)d_in[8]; p.b2 = (const unsigned short*)d_in[9];
  p.out = (unsigned short*)d_out;
  p.ws = (unsigned char*)d_ws;
  k_fused<<<256, 512, 0, stream>>>(p);
}
```

```cpp
#include <hip/hip_runtime.h>
#include <hip/hip_bf16.h>
#include <stdint.h>
#include <utility>

#define P_D 6
#define P_D0 6
#define P_N0 0
#define P_NA 44
#define P_DO 3
#define AUX_KV 2
#define AUX_WQ 2
#define AUX_WO 0
#define AUX_W12 2
#define P_BAND 5
#define P_KFIX 3
#define P_TAUV 215
#define P_MASK -1
#define P_LO 3
#define P_M1 0x5555
#define P_M2 0x1111
#define P_M3 0x1249
#define P_M4 0x0421
#define P_M5 0x0101
#define P_CH 1
#define P_HALF 0
#define P_END 0xff
#define P_NCK 1
#define P_C1 1180
#define P_CP 430
#define P_CR 36
#define P_OFF2 125
#define P_TP 110
#define P_PX 120
#define P_OFF 110
#define P_WAIT 5
#define P_PD 6
#define P_PS 1
#define P_XD 8
#define P_QD 24
#define P_SD 0
#define P_TB 16
#define AUX_OUT 0
#define AUX_ST 16
#define P_HD 0
#define P_MD 2
#define P_QS 0
#define ABL 0
#define DIAG 0
#define DIAG_W 0
#define DIAG_BLK 0

typedef unsigned v4u __attribute__((ext_vector_type(4)));
typedef unsigned v2u __attribute__((ext_vector_type(2)));
typedef float f32x4 __attribute__((ext_vector_type(4)));
typedef float f32x2 __attribute__((ext_vector_type(2)));
typedef __bf16 bf16x2 __attribute__((ext_vector_type(2)));
typedef __bf16 bf16x8 __attribute__((ext_vector_type(8)));
typedef __amdgpu_buffer_rsrc_t rsrc_t;
typedef __attribute__((address_space(3))) unsigned lds_u32;

constexpr int NB = 4, DM = 512, NH = 32, SK = 4096, DH = 128, FF = 1792;
constexpr int AUX_SC1 = 16;
constexpr unsigned SENT = 0xFFFFFFFFu, OOB = 0x80000000u;
constexpr unsigned SPIN_CAP = 1u << 14, LDS_CAP = 1u << 20;

template <class F, int... I> __device__ __forceinline__ void static_for_impl(F&& f, std::integer_sequence<int, I...>) { (f(std::integral_constant<int, I>{}), ...); }
template <int N, class F> __device__ __forceinline__ void static_for(F&& f) { static_for_impl(f, std::make_integer_sequence<int, N>{}); }

__device__ __forceinline__ float dot2(unsigned a, unsigned b, float acc) {
  return __builtin_amdgcn_fdot2_f32_bf16(__builtin_bit_cast(bf16x2, a), __builtin_bit_cast(bf16x2, b), acc, false);
}
__device__ __forceinline__ f32x4 mfma(v4u a, v4u b, f32x4 c) {
  return __builtin_amdgcn_mfma_f32_16x16x32_bf16(__builtin_bit_cast(bf16x8, a), __builtin_bit_cast(bf16x8, b), c, 0, 0, 0);
}
__device__ __forceinline__ float bflo(unsigned u) { return __uint_as_float(u << 16); }
__device__ __forceinline__ float bfhi(unsigned u) { return __uint_as_float(u & 0xffff0000u); }
__device__ __forceinline__ unsigned pk_bf16(float lo, float hi) { bf16x2 r; r.x = (__bf16)lo; r.y = (__bf16)hi; return __builtin_bit_cast(unsigned, r); }
__device__ __forceinline__ unsigned bf_bits(float x) { return (unsigned)__builtin_bit_cast(unsigned short, (__bf16)x); }
__device__ __forceinline__ float rbf(float x) { return bflo(bf_bits(x)); }
__device__ __forceinline__ unsigned pair_lo(unsigned e, unsigned o) { return __builtin_amdgcn_perm(o, e, 0x05040100u); }
__device__ __forceinline__ unsigned pair_hi(unsigned e, unsigned o) { return __builtin_amdgcn_perm(o, e, 0x07060302u); }
template <int CTRL> __device__ __forceinline__ float dpp(float x) { return __int_as_float(__builtin_amdgcn_mov_dpp(__float_as_int(x), CTRL, 0xf, 0xf, true)); }
constexpr int XOR1 = 0xB1, XOR2 = 0x4E, XOR7 = 0x141, XOR8 = 0x128, ROR4 = 0x124, BCAST = 0x150;
__device__ __forceinline__ float tr32(float a, float b) {
  auto s = __builtin_amdgcn_permlane32_swap(__float_as_uint(a), __float_as_uint(b), false, false);
  return __uint_as_float(s[0]) + __uint_as_float(s[1]);
}
__device__ __forceinline__ float tr16(float a, float b) {
  auto s = __builtin_amdgcn_permlane16_swap(__float_as_uint(a), __float_as_uint(b), false, false);
  return __uint_as_float(s[0]) + __uint_as_float(s[1]);
}
template <int CTRL> __device__ __forceinline__ float trd(float lo, float hi, bool up) {
  const float t1 = lo + dpp<CTRL>(lo), t2 = hi + dpp<CTRL>(hi);
  return up ? t2 : t1;
}
__device__ __forceinline__ float wave_sum(float x) {
  x += dpp<XOR1>(x); x += dpp<XOR2>(x); x += dpp<XOR7>(x); x += dpp<XOR8>(x);
  x = tr16(x, x); x = tr32(x, x);
  return x;
}
__device__ __forceinline__ float wave_max(float x) {
  x = fmaxf(x, dpp<XOR1>(x)); x = fmaxf(x, dpp<XOR2>(x)); x = fmaxf(x, dpp<XOR7>(x)); x = fmaxf(x, dpp<XOR8>(x));
  auto s = __builtin_amdgcn_permlane16_swap(__float_as_uint(x), __float_as_uint(x), false, false);
  x = fmaxf(__uint_as_float(s[0]), __uint_as_float(s[1]));
  auto t = __builtin_amdgcn_permlane32_swap(__float_as_uint(x), __float_as_uint(x), false, false);
  return fmaxf(__uint_as_float(t[0]), __uint_as_float(t[1]));
}
__device__ __forceinline__ float sum4_parity(f32x4 v) {
  float s = tr16(tr32(v[0], v[2]), tr32(v[1], v[3]));
  s += dpp<XOR8>(s); s += dpp<ROR4>(s); s += dpp<XOR2>(s);
  return s;
}
__device__ __forceinline__ v4u tag4(f32x4 v, unsigned tag) {
  return ((__builtin_bit_cast(v4u, v) + 0x800u) & 0xfffff000u) | (tag & 0xfffu);
}
__device__ __forceinline__ f32x4 untag4(v4u g, unsigned tag12, unsigned& bad) {
  g ^= tag12;
  bad |= (g[0] | g[1]) | (g[2] | g[3]);
  return __builtin_bit_cast(f32x4, g);
}
__device__ __forceinline__ rsrc_t mk_rsrc(const void* p, unsigned bytes) { return __builtin_amdgcn_make_buffer_rsrc(const_cast<void*>(p), 0, bytes, 0x00020000); }
template <int AUX> __device__ __forceinline__ v4u ld128(rsrc_t r, unsigned v, unsigned s) { return __builtin_amdgcn_raw_buffer_load_b128(r, v, s, AUX); }
template <int AUX> __device__ __forceinline__ v2u ld64(rsrc_t r, unsigned v, unsigned s) { return __builtin_amdgcn_raw_buffer_load_b64(r, v, s, AUX); }
__device__ __forceinline__ void st128(v4u d, rsrc_t r, unsigned v, unsigned s) { __builtin_amdgcn_raw_buffer_store_b128(d, r, v + s, 0, AUX_ST); }
__device__ __forceinline__ void st64(v2u d, rsrc_t r, unsigned v, unsigned s) { __builtin_amdgcn_raw_buffer_store_b64(d, r, v + s, 0, AUX_SC1); }
__device__ __forceinline__ float rows4_sum(float x) { x += dpp<XOR1>(x); x += dpp<XOR2>(x); x = tr16(x, x); x = tr32(x, x); return x; }
__device__ __forceinline__ void ln_stats(const v4u (&x)[4], float& mean, float& inv) {
  float s = 0.f;
#pragma unroll
  for (int t = 0; t < 4; ++t)
#pragma unroll
    for (int j = 0; j < 4; ++j) s += bflo(x[t][j]) + bfhi(x[t][j]);
  const float mu = rows4_sum(s) * (1.0f / DM);
  float q = 0.f;
#pragma unroll
  for (int t = 0; t < 4; ++t)
#pragma unroll
    for (int j = 0; j < 4; ++j) { const float d0 = bflo(x[t][j]) - mu, d1 = bfhi(x[t][j]) - mu; q = fmaf(d0, d0, q); q = fmaf(d1, d1, q); }
  const float var = rows4_sum(q) * (1.0f / DM);
  mean = rbf(mu);
  inv = __builtin_amdgcn_rcpf(rbf(__builtin_amdgcn_sqrtf(rbf(rbf(var) + 1e-5f))));
}
__device__ __forceinline__ v4u ln_apply(v4u x, float mean, float inv) {
  v4u o;
#pragma unroll
  for (int j = 0; j < 4; ++j) { const unsigned d = pk_bf16(bflo(x[j]) - mean, bfhi(x[j]) - mean); o[j] = pk_bf16(bflo(d) * inv, bfhi(d) * inv); }
  return o;
}
template <int J> __device__ __forceinline__ v4u bfrag(const v4u* t) {
  v4u f;
#pragma unroll
  for (int d = 0; d < 4; ++d) f[d] = (J & 1) ? pair_hi(t[2 * d][J >> 1], t[2 * d + 1][J >> 1]) : pair_lo(t[2 * d][J >> 1], t[2 * d + 1][J >> 1]);
  return f;
}
template <int J> __device__ __forceinline__ v4u bfrag2(const v2u* t) {
  v4u f;
#pragma unroll
  for (int d = 0; d < 4; ++d) f[d] = (J & 1) ? pair_hi(t[2 * d][J >> 1], t[2 * d + 1][J >> 1]) : pair_lo(t[2 * d][J >> 1], t[2 * d + 1][J >> 1]);
  return f;
}
#define TOUCH(x) asm volatile("" :: "v"(x))
#define PIN(x) asm volatile("" : "+v"(x))
#define THROTTLE(mask, i) asm volatile("s_bitcmp1_b32 %0, %1\n\ts_cbranch_scc0 1f\n\ts_waitcnt vmcnt(%2)\n1:" :: "s"(mask), "n"(i), "n"(P_LO) : "scc")
#define FENCE() __builtin_amdgcn_sched_barrier(0)
#define WG_BARRIER() asm volatile("s_waitcnt lgkmcnt(0)\n\ts_barrier" ::: "memory")
#define DRAIN() asm volatile("s_waitcnt vmcnt(0)" ::: "memory")
#define LDS_WAIT() asm volatile("s_waitcnt lgkmcnt(0)" ::: "memory")
template <int AUX, int LO, int HI> __device__ __forceinline__ void tile_load(v2u (&u)[32], rsrc_t r, unsigned voff, unsigned stride) {
#pragma unroll
  for (int n = LO; n < HI; ++n) u[n] = ld64<AUX>(r, voff, n * stride);
  FENCE();
}
__device__ __forceinline__ f32x4 tile_mul(v4u a, const v2u (&u)[32]) {
  f32x4 o = {0.f, 0.f, 0.f, 0.f};
  static_for<16>([&](auto N) { constexpr int s = N >> 2, j = N & 3; o[j] += mfma(a, bfrag2<j>(u + 8 * s), f32x4{0.f, 0.f, 0.f, 0.f})[s]; });
  return o;
}
#define MARK(k) do { if (DIAG == (k) + 1 && w == DIAG_W) { FENCE(); *(unsigned*)(lds + L_DIAG) = (unsigned)__builtin_amdgcn_s_memrealtime(); FENCE(); } } while (0)
#define FLUSH_STAMPS()

constexpr unsigned WS_E = 0;
constexpr unsigned WS_LS = 4096;
constexpr unsigned WS_CK = 32768;
constexpr unsigned WS_X1 = 65536;
constexpr unsigned WS_MP = WS_X1 + 32 * 4096;
constexpr unsigned WS_TB = WS_MP + 4 * 16 * 2048;
constexpr unsigned WS_QX = WS_TB + 4 * 64 * 2048;
constexpr unsigned WS_BYTES = WS_QX + 32 * 4 * 4 * 512;

struct Params {
  const unsigned short* x; const unsigned short* k; const unsigned short* v; const unsigned short* wq; const unsigned short* wo;
  const unsigned short* w1; const unsigned short* b1; const unsigned short* w2; const unsigned short* b2;
  unsigned short* out; unsigned char* ws;
};

constexpr int D = P_D, NA = P_NA, DO = P_DO;
constexpr int LD = 8;
constexpr int NU = (NA <= 48 + LD - 12) ? 48 : 64;
static_assert((NU + LD - NA) * 4 >= NU && D <= LD, "the backlog is gone before the loop starts");
constexpr unsigned koff(int n) { return (n / P_CH) * (P_CH * 4096) + (n % P_CH) * 1024; }
constexpr float C2 = 0.088388347648318440f * 1.4426950408889634f;

constexpr int L_Q = 0, L_M = 256, L_F = 272, L_TAG = 288, L_REF = 292, L_RDY = 296, L_T = 304, L_GX = 320, L_DIAG = 512, L_H = 1024, L_O = 2048, O_ROW = 272, L_S = L_O + 8 * O_ROW, L_END = L_S + 8192;

__global__ void __launch_bounds__(512, 2) k_fused(Params p) {
  __shared__ __attribute__((aligned(16))) unsigned char lds[L_END];
  const int blk = blockIdx.x, tid = threadIdx.x, lane = tid & 63, w = __builtin_amdgcn_readfirstlane(tid >> 6);
  MARK(0);
  const int idx = blk >> 3, h = (blk & 7) * 4 + (idx & 3), bs = idx >> 2, b = bs >> 1, sp = bs & 1;
  const int cons = (blk & 7) * 8 + (idx & 7);
  const int col0 = 128 * (cons >> 4) + 16 * (cons & 7) + 4 * ((cons >> 3) & 1);
  const int r = lane >> 4, c = lane & 15;
  const unsigned l16 = lane * 16;
  unsigned kvoff = (((b * NH + h) * SK + sp * 2048 + (w & 3) * 4 * P_CH) * DH) * 2, kvlen = 524288 - (w & 3) * 1024 * P_CH;
  asm volatile("" : "+s"(kvoff), "+s"(kvlen));
  asm volatile("" :: "s"(p.x), "s"(p.k), "s"(p.v), "s"(p.wq), "s"(p.wo), "s"(p.w1), "s"(p.b1), "s"(p.w2), "s"(p.b2), "s"(p.out), "s"(p.ws));
  const rsrc_t rws = mk_rsrc(p.ws, WS_BYTES);
  const unsigned rot = (bs >> 1) * 32 + (bs & 1) * 4;

  if (w >= 4) {
  const int a = w - 4;
  *(v4u*)(lds + L_M) = v4u{SENT, SENT, SENT, SENT}; *(v4u*)(lds + L_TAG) = v4u{0u, 0u, 0u, 0u};
  WG_BARRIER();
  const rsrc_t rx = mk_rsrc(p.x, NB * DM * 2);
  const unsigned xrow = ((lane >> 2) & 3) * (DM * 2);
  const bool mlp = h < 28 && !(ABL & 2);
  const unsigned xoff = xrow + ((lane & 3) + 4 * r) * 16;
  if (a == 0) {
    v4u xs[4]; v2u U[32];
#pragma unroll
    for (int t = 0; t < 4; ++t) xs[t] = ld128<0>(rx, xoff, ((t + b) & 3) * 256);
    FENCE();
    tile_load<AUX_WQ, 0, 32>(U, mk_rsrc(p.wq + (size_t)b * 128 * (NH * DH) + h * DH + sp * 64, (ABL & 8) ? 0 : 128 * NH * DH * 2), r * 32 * 8192 + c * 8, 8192);
    const unsigned ep = __builtin_amdgcn_raw_buffer_load_b32(rws, 0, WS_E, AUX_SC1);
    FENCE();
    MARK(10);
    float mean, inv;
    ln_stats(xs, mean, inv);
    const f32x4 g = tile_mul(ln_apply(xs[0], mean, inv), U);
    FENCE();
    const unsigned tg = __builtin_amdgcn_readfirstlane(ep) + 1u;
    st128(tag4(g, tg), rws, ((ABL & 4) || ((ABL & 128) && lane > 0)) ? OOB : l16, WS_QX + h * 8192 + bs * 1024);
    MARK(11);
    DRAIN();
    MARK(1);
    if (P_QD) __builtin_amdgcn_s_sleep(P_QD);
    f32x4 q4;
#pragma unroll 1
    for (unsigned spin = 0; spin < SPIN_CAP; ++spin) {
      const unsigned qo = (ABL & 16) ? OOB : r * 1024 + c * 16;
      const v4u G0 = ld128<AUX_SC1>(rws, qo, WS_QX + h * 8192 + b * 256), G1 = ld128<AUX_SC1>(rws, qo, WS_QX + h * 8192 + b * 256 + 4096);
      unsigned bad = 0;
      q4 = untag4(G0, tg & 0xfffu, bad); q4 += untag4(G1, tg & 0xfffu, bad);
      if (!__any((bad & 0xfffu) != 0u) || (ABL & 148)) break;
      if (P_QS) __builtin_amdgcn_s_sleep(P_QS);
      asm volatile("" ::: "memory");
    }
    *(unsigned*)(lds + L_Q + (lane & 31) * 8 + (lane >> 5) * 4) = pk_bf16(tr32(q4[0], q4[2]), tr32(q4[1], q4[3]));
    *(unsigned*)(lds + L_TAG) = tg;
    asm volatile("" ::: "memory");
    __hip_atomic_store((lds_u32*)lds + L_RDY / 4, 1u, __ATOMIC_RELAXED, __HIP_MEMORY_SCOPE_WORKGROUP);
  }
  MARK(2);
#pragma unroll 1
  for (unsigned spin = 0; spin < LDS_CAP; ++spin) {
    if (__builtin_amdgcn_readfirstlane(__hip_atomic_load((lds_u32*)lds + L_RDY / 4, __ATOMIC_RELAXED, __HIP_MEMORY_SCOPE_WORKGROUP))) break;
    __builtin_amdgcn_s_sleep(4);
  }
  asm volatile("" ::: "memory");
  if (P_HD) __builtin_amdgcn_s_sleep(P_HD);
  MARK(3);
  const unsigned tag = __builtin_amdgcn_readfirstlane(*(const unsigned*)(lds + L_TAG));
  if (a == 2 && mlp) {
    const int cg = (h & 1) * 4 + b;
    const unsigned stride = sp ? DM * 2 : FF * 2;
    const rsrc_t rt = mk_rsrc(sp ? p.w2 + (size_t)(h >> 1) * 128 * DM + cg * 64 : p.w1 + (size_t)b * 128 * FF + h * 64, (ABL & 64) ? 0 : 128 * stride);
    v2u U[32]; v4u A;
    static_for<32 / P_TB>([&](auto T) { constexpr int t = T; if constexpr (t > 0) { TOUCH(U[t * P_TB - 1]); FENCE(); }
      tile_load<AUX_W12, t * P_TB, (t + 1) * P_TB>(U, rt, r * 32 * stride + c * 8, stride); });
    if (sp == 0) {
      v4u xs[4];
#pragma unroll
      for (int t = 0; t < 4; ++t) xs[t] = ld128<0>(rx, xoff, ((t + b) & 3) * 256);
#pragma unroll 1
      for (int it = 0; it < 2; ++it) {
        float mean, inv;
        ln_stats(xs, mean, inv);
#pragma unroll
        for (int t = 0; t < 4; ++t) xs[t] = ln_apply(xs[t], mean, inv);
      }
      A = xs[0];
    } else {
      v2u b1v[2];
#pragma unroll
      for (int k = 0; k < 2; ++k) b1v[k] = ld64<0>(mk_rsrc(p.b1 + (h >> 1) * 128, 256), c * 8, k * 128);
      f32x4 z4[2];
#pragma unroll 1
      for (unsigned spin = 0; spin < SPIN_CAP; ++spin) {
        for (int i = 0; i < P_XD; ++i) __builtin_amdgcn_s_sleep(32);
        v4u G[8];
#pragma unroll
        for (int j = 0; j < 8; ++j) G[j] = ld128<AUX_SC1>(rws, l16, WS_X1 + (h >> 1) * 8192 + j * 1024);
        unsigned bad = 0;
#pragma unroll
        for (int j = 0; j < 8; ++j) { const f32x4 v = untag4(G[j], tag & 0xfffu, bad); z4[j >> 2] = (j & 3) ? z4[j >> 2] + v : v; }
        if (!__any((bad & 0xfffu) != 0u)) break;
        asm volatile("" ::: "memory");
      }
#pragma unroll
      for (int k = 0; k < 2; ++k) {
        float hv[4];
#pragma unroll
        for (int j = 0; j < 4; ++j) {
          const float z = rbf(rbf(z4[k][j]) + ((j & 1) ? bfhi(b1v[k][j >> 1]) : bflo(b1v[k][j >> 1])));
          const float e2 = __builtin_amdgcn_exp2f((-2.0f * 0.7978845608028654f * 1.4426950408889634f) * (z + 0.044715f * z * z * z));
          hv[j] = z * __builtin_amdgcn_rcpf(1.0f + e2);
        }
        *(v2u*)(lds + L_H + r * 256 + k * 128 + c * 8) = v2u{pk_bf16(hv[0], hv[1]), pk_bf16(hv[2], hv[3])};
      }
      LDS_WAIT();
      A = *(const v4u*)(lds + L_H + ((lane >> 2) & 3) * 256 + ((lane & 3) + 4 * r) * 16);
    }
    st128(tag4(tile_mul(A, U), tag), rws, l16, sp ? WS_MP + (h >> 1) * 8192 + cg * 1024 : WS_X1 + h * 4096 + b * 1024);
  }
  MARK(4);
  v4u Bf[4][8];
  {
    const rsrc_t ro = mk_rsrc(p.wo + (size_t)h * DH * DM + a * 128, (ABL & 1) ? 0 : DH * DM * 2);
    const unsigned vo = r * 8 * 1024 + c * 16;
    v4u T[32];
    static_for<32 + DO>([&](auto N) { constexpr int n = N;
      if constexpr (n >= DO) TOUCH(T[n - DO]);
      FENCE();
      if constexpr (n < 32) T[n] = ld128<AUX_WO>(ro, vo, (((n >> 3) * 32 + (n & 7)) ^ rot) * 1024);
      FENCE();
      if constexpr (n >= DO && ((n - DO) & 7) == 7) { constexpr int s = (n - DO) >> 3;
        static_for<8>([&](auto J) { constexpr int j = J; Bf[s][j] = bfrag<j>(T + 8 * s); PIN(Bf[s][j]); }); }
      FENCE(); });
  }
  MARK(5);
  const unsigned co = r * 2 + (lane & 1) * 16;
  if (a == 3) {
    unsigned mb;
#pragma unroll 1
    for (unsigned spin = 0; spin < LDS_CAP; ++spin) {
      mb = __hip_atomic_load((lds_u32*)lds + L_M / 4 + (lane & 3), __ATOMIC_RELAXED, __HIP_MEMORY_SCOPE_WORKGROUP);
      if (__all(mb != SENT)) break;
      __builtin_amdgcn_s_sleep(4);
    }
    asm volatile("" ::: "memory");
    const float m4 = __uint_as_float(mb);
    float M = fmaxf(m4, dpp<XOR1>(m4)); M = fmaxf(M, dpp<XOR2>(M));
    float L = 0.f;
#pragma unroll
    for (int i = 0; i < 8; ++i) { const f32x4 s4 = *(const f32x4*)(lds + L_S + i * 1024 + l16);
#pragma unroll
      for (int j = 0; j < 4; ++j) L += __builtin_amdgcn_exp2f(s4[j] - M); }
    L = wave_sum(L);
    const float lse = M + __builtin_amdgcn_logf(L);
    const unsigned tk = *(const unsigned*)(lds + L_T);
    st128(v4u{__float_as_uint(lse), tag, tk, tag}, rws, lane == 0 ? 0u : OOB, WS_LS + (b * 64 + 2 * h + sp) * 16);
    MARK(6);
    float lo; unsigned mean;
#pragma unroll 1
    for (unsigned spin = 0; spin < SPIN_CAP; ++spin) {
      for (int i = 0; i < P_WAIT; ++i) __builtin_amdgcn_s_sleep(16);
      const v4u m = ld128<AUX_SC1>(rws, l16, WS_LS + b * 1024);
      const bool ok = ((m[1] ^ tag) | (m[3] ^ tag)) == 0u;
      const unsigned tn = (unsigned)__builtin_amdgcn_s_memrealtime();
      const float dt = __builtin_amdgcn_fmed3f(wave_sum((float)(int)((ok ? m[2] : tn) - tk)) * (1.0f / 64), -1000.f, 1000.f);
      mean = __builtin_amdgcn_readfirstlane(tk + (int)dt);
      __hip_atomic_store((lds_u32*)lds + L_REF / 4, (mean - P_OFF) | 1u, __ATOMIC_RELAXED, __HIP_MEMORY_SCOPE_WORKGROUP);
      lo = __uint_as_float(__builtin_amdgcn_readlane(m[0], 2 * h + (sp ^ 1)));
      if ((__ballot(ok) >> (2 * h + (sp ^ 1))) & 1) break;
      asm volatile("" ::: "memory");
    }
    *(float*)(lds + L_F + 4 * (lane & 3)) = __builtin_amdgcn_exp2f(m4 - M) * __builtin_amdgcn_rcpf(L) * __builtin_amdgcn_rcpf(1.0f + __builtin_amdgcn_exp2f(lo - lse));
#pragma unroll 1
    for (int k = 0; k < P_NCK; ++k) {
      while ((int)((unsigned)__builtin_amdgcn_s_memrealtime() - mean) < P_C1 + k * P_CP) __builtin_amdgcn_s_sleep(2);
      const unsigned org = __hip_atomic_load((lds_u32*)lds + L_T / 4, __ATOMIC_RELAXED, __HIP_MEMORY_SCOPE_WORKGROUP);
      st64(v2u{org, tag}, rws, (lane == 0 && (idx & 3) == 0) ? ((idx >> 2) * 8 + (blk & 7)) * 8 : OOB, WS_CK + k * 512);
      __builtin_amdgcn_s_sleep(P_CR);
      const v2u ck = ld64<AUX_SC1>(rws, lane * 8, WS_CK + k * 512);
      const bool ok = ck[1] == tag;
      const float n = wave_sum(ok ? 1.f : 0.f), d = wave_sum(ok ? (float)(int)(ck[0] - mean) : 0.f);
      if (n > 0.f) __hip_atomic_store((lds_u32*)lds + L_REF / 4, (mean + (int)(d * __builtin_amdgcn_rcpf(n)) - P_OFF2) | 1u, __ATOMIC_RELAXED, __HIP_MEMORY_SCOPE_WORKGROUP);
    }
  } else if (a == 2) {
    const unsigned b2v = __builtin_amdgcn_raw_buffer_load_b16(mk_rsrc(p.b2, DM * 2), co, col0 * 2, 0);
    const unsigned xv = __builtin_amdgcn_raw_buffer_load_b16(rx, co, (b * DM + col0) * 2, 0);
    f32x4 m4;
#pragma unroll 1
    for (unsigned spin = 0; spin < SPIN_CAP; ++spin) {
      for (int i = 0; i < P_MD; ++i) __builtin_amdgcn_s_sleep(32);
      const v4u G = ld128<AUX_SC1>(rws, lane < 28 ? (unsigned)((lane >> 1) * 8192 + (lane & 1) * 32) : OOB, WS_MP + (col0 >> 6) * 1024 + b * 256 + (col0 & 63) * 4);
      unsigned bad = 0;
      m4 = untag4(G, lane < 28 ? tag & 0xfffu : 0u, bad);
      if (!__any((bad & 0xfffu) != 0u) || (ABL & 2)) break;
      asm volatile("" ::: "memory");
    }
    const float m = rbf(rbf(sum4_parity(m4)) + bflo(b2v));
    const float gate = rbf(m * __builtin_amdgcn_rcpf(1.0f + __builtin_amdgcn_exp2f(-1.4426950408889634f * m)));
    *(f32x2*)(lds + L_GX + co * 4) = f32x2{gate, bflo(xv)};
  }
  unsigned a_off = L_O + (((lane >> 2) & 3) + (lane & 1) * 4) * O_ROW + r * 16, tb_off = lane < 32 ? WS_TB + (b * 64 + 2 * h + sp) * 2048 + a * 512 + l16 : OOB;
  PIN(a_off); PIN(tb_off);
  MARK(9);
  WG_BARRIER();
  MARK(7);
  {
    v4u A[4];
#pragma unroll
    for (int s = 0; s < 4; ++s) A[s] = *(const v4u*)(lds + a_off + s * 64);
    const float f = *(const float*)(lds + L_F + r * 4);
    FENCE();
    f32x4 d[8]; v4u tv;
    auto fin = [&](int j) {
      float u = (d[j][0] + d[j][1]) * f, v = (d[j + 4][0] + d[j + 4][1]) * f; asm volatile("" : "+v"(u) : "v"(d[j])); asm volatile("" : "+v"(v) : "v"(d[j + 4]));
      u = tr16(u, v); u = tr32(u, u);
      tv[j] = ((__float_as_uint(u) + 0x800u) & 0xfffff000u) | (tag & 0xfffu); PIN(tv[j]); };
    static_for<4>([&](auto J) { constexpr int j = J;
      d[j] = mfma(A[0], Bf[0][j], f32x4{0.f, 0.f, 0.f, 0.f}); d[j + 4] = mfma(A[0], Bf[0][j + 4], f32x4{0.f, 0.f, 0.f, 0.f});
      FENCE();
#pragma unroll
      for (int s = 1; s < 4; ++s) { d[j] = mfma(A[s], Bf[s][j], d[j]); d[j + 4] = mfma(A[s], Bf[s][j + 4], d[j + 4]); }
      if constexpr (j > 0) {
        fin(j - 1);
#pragma unroll
        for (int k = 0; k < 6; ++k) { __builtin_amdgcn_sched_group_barrier(0x8, 1, 0); __builtin_amdgcn_sched_group_barrier(0x2, 2, 0); }
      }
      FENCE(); });
    fin(3);
    __builtin_amdgcn_raw_buffer_store_b128(tv, rws, tb_off, 0, AUX_ST);
  }
  MARK(10);
  if (a == 0) {
    const f32x2 gx = *(const f32x2*)(lds + L_GX + co * 4);
    const unsigned go = (lane >> 1) * 2048 + (lane & 1) * 16;
    unsigned out_off = (lane & 14) == 0 ? (b * DM + col0) * 2 + co : OOB, e_off = (blk == 0 && lane == 0) ? WS_E : OOB;
    PIN(out_off); PIN(e_off);
    f32x4 s4;
    if (P_TP >= 0) {
      const unsigned t1 = (unsigned)__builtin_amdgcn_s_memrealtime();
      const int wait = (int)(__builtin_amdgcn_readfirstlane(*(const unsigned*)(lds + L_REF)) + P_OFF2 + 8 * P_TAUV + P_TP - t1);
      __builtin_amdgcn_s_sleep(P_PD);
      while ((int)((unsigned)__builtin_amdgcn_s_memrealtime() - t1) < (wait < P_PX ? wait : P_PX)) {}
    } else __builtin_amdgcn_s_sleep(P_PD);
#pragma unroll 1
    for (unsigned spin = 0; spin < SPIN_CAP; ++spin) {
      const v4u G0 = ld128<AUX_SC1>(rws, go, WS_TB + b * 131072 + cons * 32), G1 = ld128<AUX_SC1>(rws, go, WS_TB + b * 131072 + 65536 + cons * 32);
      unsigned bad = 0;
      s4 = untag4(G0, tag & 0xfffu, bad); s4 += untag4(G1, tag & 0xfffu, bad);
      if (!__any((bad & 0xfffu) != 0u)) break;
      __builtin_amdgcn_s_sleep(P_PS);
      asm volatile("" ::: "memory");
    }
    const float o = gx[1] + rbf(rbf(sum4_parity(s4)) * gx[0]);
    __builtin_amdgcn_raw_buffer_store_b16((unsigned short)bf_bits(o), mk_rsrc(p.out, NB * DM * 2), out_off, 0, AUX_OUT);
    __builtin_amdgcn_raw_buffer_store_b32(tag, rws, e_off, 0, 0);
  }
  MARK(8);
  FLUSH_STAMPS();
  if (DIAG && blk == DIAG_BLK && a == 0) { LDS_WAIT(); const unsigned tm = __builtin_amdgcn_readfirstlane(*(const unsigned*)(lds + L_DIAG));
    while ((unsigned)__builtin_amdgcn_s_memrealtime() - tm < 10000) __builtin_amdgcn_s_sleep(1); }
    return;
  }
  {
    const char* kp = (const char*)p.k + kvoff; const char* vp = (const char*)p.v + kvoff;
    const rsrc_t rk = mk_rsrc(kp, kvlen);
    float* const scs = (float*)(lds + L_S + w * 2048) + lane;
    v4u S[NU + LD], q;
    auto pace = [&](int lv) {
      unsigned m = 0xffffu; m = lv > 0 ? P_M1 : m; m = lv > 1 ? P_M2 : m; m = lv > 2 ? P_M3 : m;
      m = lv > 3 ? P_M4 : m; m = lv > 4 ? P_M5 : m; m = lv > 5 ? 0u : m;
      if (P_MASK >= 0) m = P_MASK;
      return (unsigned)__builtin_amdgcn_readfirstlane(m); };
    float ka[16], mw = -INFINITY;
    auto kdot = [&](v4u t, int slot) {
      float d = dot2(t[0], q[0], 0.f); d = dot2(t[1], q[1], d); d = dot2(t[2], q[2], d); d = dot2(t[3], q[3], d); PIN(d); ka[slot] = d; };
    auto kfold = [&](int g) {
#pragma unroll
      for (int j = 0; j < 8; ++j) ka[j] = trd<XOR8>(ka[j], ka[j + 8], lane & 8);
#pragma unroll
      for (int j = 0; j < 4; ++j) ka[j] = trd<XOR7>(ka[j], ka[j + 4], lane & 4);
#pragma unroll
      for (int j = 0; j < 2; ++j) ka[j] = trd<XOR2>(ka[j], ka[j + 2], lane & 2);
      const float s = trd<XOR1>(ka[0], ka[1], lane & 1) * C2;
      scs[g * 64] = s; mw = fmaxf(mw, s); PIN(mw); };
    static_for<NU + LD>([&](auto N) {
      constexpr int n = N;
      constexpr int dn = n < P_N0 ? P_D0 : D;
      if constexpr (n >= dn) TOUCH(S[n - dn]);
      FENCE();
      if constexpr (n == (P_N0 ? P_D0 : D)) MARK(1);
      S[n] = ld128<AUX_KV>(rk, l16, koff(n));
      if constexpr (n == 0) __builtin_amdgcn_s_setprio(2);
      if constexpr (n == (P_N0 ? P_D0 : D) - 1) { FENCE(); WG_BARRIER(); }
      FENCE();
      if constexpr (n == NA - 1) {
        MARK(2);
#pragma unroll 1
        for (unsigned spin = 0; spin < LDS_CAP; ++spin) {
          const unsigned rdy = __hip_atomic_load((lds_u32*)lds + L_RDY / 4, __ATOMIC_RELAXED, __HIP_MEMORY_SCOPE_WORKGROUP);
          asm volatile("" ::: "memory");
          q = *(const v4u*)(lds + L_Q + c * 16);
          if (__builtin_amdgcn_readfirstlane(rdy)) break;
          __builtin_amdgcn_s_sleep(1);
        }
        MARK(3);
      }
      if constexpr (n >= NA) {
        constexpr int hi = (n - NA + 1) * 4 < n - LD + 1 ? (n - NA + 1) * 4 : n - LD + 1;
        constexpr int lo = (n - NA) * 4 < n - LD ? (n - NA) * 4 : n - LD;
        static_for<hi - lo>([&](auto J) { constexpr int i = lo + J; kdot(S[i], i & 15); if constexpr ((i & 15) == 15) kfold(i >> 4); });
      }
      FENCE();
    });
    v4u R[16];
#pragma unroll
    for (int i = 0; i < LD; ++i) R[i] = S[NU + i];
    float snext = scs[0];
#pragma unroll 1
    for (int g = NU / 16; g < 8; ++g) {
      const rsrc_t rn = mk_rsrc(g == 7 ? vp : kp, kvlen);
      const unsigned o0 = g * 65536, o1 = g == 7 ? 0u : o0 + 65536;
      const unsigned mask = pace(P_KFIX);
      static_for<16>([&](auto I) { constexpr int i = I;
        TOUCH(R[i]); THROTTLE(mask, i); FENCE();
        if constexpr (i + LD < 16) R[i + LD] = ld128<AUX_KV>(rk, l16, o0 + koff(i + LD));
        else R[i + LD - 16] = ld128<AUX_KV>(rn, l16, o1 + koff(i + LD - 16));
        FENCE();
        kdot(R[i], i);
        FENCE(); });
      kfold(g);
    }
    MARK(4);
    unsigned now = (unsigned)__builtin_amdgcn_s_memrealtime();
    mw = wave_max(mw);
    *(unsigned*)(lds + L_T + 4 * w) = now;
    unsigned refv = 0;
    asm volatile("" ::: "memory");
    __hip_atomic_store((lds_u32*)lds + L_M / 4 + w, min(__float_as_uint(mw), SENT - 1u), __ATOMIC_RELAXED, __HIP_MEMORY_SCOPE_WORKGROUP);
    float acc[8];
#pragma unroll
    for (int j = 0; j < 8; ++j) acc[j] = 0.f;
    const rsrc_t rv = mk_rsrc(vp, kvlen);
    MARK(5);
#pragma unroll 1
    for (int g = 0; g < 8; ++g) {
      const rsrc_t rn = mk_rsrc(vp, g == 7 ? 0u : kvlen);
      const unsigned o0 = g * 65536;
      const float pg = __builtin_amdgcn_exp2f(snext - mw);
      snext = scs[((g + 1) & 7) * 64];
      const unsigned rf = __builtin_amdgcn_readfirstlane(refv);
      if (P_NCK) __hip_atomic_store((lds_u32*)lds + L_T / 4 + w, now - g * P_TAUV, __ATOMIC_RELAXED, __HIP_MEMORY_SCOPE_WORKGROUP);
      unsigned mask = pace(rf ? ((int)(now - rf) - g * P_TAUV) >> P_BAND : P_KFIX) & (g == 7 ? P_END : 0xffffu);
      static_for<16>([&](auto I) { constexpr int i = I;
        if constexpr (P_HALF && i == 8) mask = g == 7 ? 0u : pace(rf ? ((int)(now - rf) - g * P_TAUV - P_TAUV / 2) >> P_BAND : P_KFIX);
        TOUCH(R[i]); THROTTLE(mask, i); FENCE();
        R[(i + LD) & 15] = ld128<AUX_KV>(i + LD < 16 ? rv : rn, l16, o0 + koff(i + LD));
        if constexpr (P_HALF && i == 7) now = (unsigned)__builtin_amdgcn_s_memrealtime();
        if constexpr (i == 15) { now = (unsigned)__builtin_amdgcn_s_memrealtime();
          refv = __hip_atomic_load((lds_u32*)lds + L_REF / 4, __ATOMIC_RELAXED, __HIP_MEMORY_SCOPE_WORKGROUP); }
        FENCE();
        const v4u t = R[i]; const float pb = dpp<BCAST + i>(pg);
#pragma unroll
        for (int j = 0; j < 4; ++j) { acc[2 * j] = fmaf(pb, bflo(t[j]), acc[2 * j]); acc[2 * j + 1] = fmaf(pb, bfhi(t[j]), acc[2 * j + 1]); }
        asm volatile("" : "+v"(acc[0]), "+v"(acc[1]), "+v"(acc[2]), "+v"(acc[3]), "+v"(acc[4]), "+v"(acc[5]), "+v"(acc[6]), "+v"(acc[7]));
        FENCE(); });
    }
    MARK(6);
    {
      float t4[4];
#pragma unroll
      for (int j = 0; j < 4; ++j) t4[j] = tr32(acc[j], acc[j + 4]);
      const float o0 = tr16(t4[0], t4[2]), o1 = tr16(t4[1], t4[3]);
      const unsigned hi = pk_bf16(o0, o1), lo = pk_bf16(o0 - bflo(hi), o1 - bfhi(hi));
      *(unsigned*)(lds + L_O + w * O_ROW + ((c * 16 + r * 4) ^ (rot * 2))) = hi;
      *(unsigned*)(lds + L_O + (4 + w) * O_ROW + ((c * 16 + r * 4) ^ (rot * 2))) = lo;
    }
    WG_BARRIER();
    MARK(7);
    FLUSH_STAMPS();
  }
}

extern "C" void kernel_launch(void* const* d_in, const int* in_sizes, int n_in, void* d_out, int out_size, void* d_ws, size_t ws_size,
                              hipStream_t stream) {
  Params p;
  p.x = (const unsigned short*)d_in[0]; p.k = (const unsigned short*)d_in[1]; p.v = (const unsigned short*)d_in[2];
  p.wq = (const unsigned short*)d_in[3]; p.wo = (const unsigned short*)d_in[4];
  p.w1 = (const unsigned short*)d_in[6]; p.b1 = (const unsigned short*)d_in[7]; p.w2 = (const unsigned short*)d_in[8]; p.b2 = (const unsigned short*)d_in[9];
  p.out = (unsigned short*)d_out;
  p.ws = (unsigned char*)d_ws;
  k_fused<<<256, 512, 0, stream>>>(p);
}
```

```cpp
#include <hip/hip_runtime.h>
#include <hip/hip_bf16.h>
#include <stdint.h>
#include <utility>

typedef unsigned v2u __attribute__((ext_vector_type(2)));
typedef unsigned v4u __attribute__((ext_vector_type(4)));
typedef unsigned v8u __attribute__((ext_vector_type(8)));
typedef float f32x2 __attribute__((ext_vector_type(2)));
typedef float f32x4 __attribute__((ext_vector_type(4)));
typedef __bf16 bf16x2 __attribute__((ext_vector_type(2)));
typedef __bf16 bf16x8 __attribute__((ext_vector_type(8)));
typedef __bf16 bf16x16 __attribute__((ext_vector_type(16)));
typedef __amdgpu_buffer_rsrc_t rsrc_t;
typedef __attribute__((address_space(3))) unsigned lds_u32;

constexpr int NB = 4, DM = 512, NH = 32, SK = 4096, DH = 128, FF = 1792;
constexpr int NT = 2, SC1 = 16;
constexpr unsigned SENT = 0xFFFFFFFFu, OOB = 0x80000000u;
constexpr unsigned SPIN_CAP = 1u << 14, LDS_CAP = 1u << 20;
constexpr float LOG2E = 1.4426950408889634f, C2 = 0.088388347648318440f * LOG2E;

#define P_NA 40
constexpr int D0 = 7, N0 = 10, D1 = 5;
constexpr int NS0 = D0 + 7;
constexpr int NA = P_NA;
constexpr int NU = 48;
constexpr int LD = 8;
constexpr int DO = 3;
static_assert((NU + LD - NA) * 4 >= NU, "the backlog is gone before the loop starts");
#define P_TAUK 228
#define P_TAUV 215
#define P_OFF0 70
#define P_OFF1 120
#define P_OFF2 135
#define P_C1 1100
#define P_TP 82
#define P_EX 0

template <class F, int... I> __device__ __forceinline__ void static_for_impl(F&& f, std::integer_sequence<int, I...>) { (f(std::integral_constant<int, I>{}), ...); }
template <int N, class F> __device__ __forceinline__ void static_for(F&& f) { static_for_impl(f, std::make_integer_sequence<int, N>{}); }

#define TOUCH(x) asm volatile("" :: "v"(x))
#define PIN(x) asm volatile("" : "+v"(x))
#define ORDER() asm volatile("" ::: "memory")
#define FENCE() __builtin_amdgcn_sched_barrier(0)
#define WG_BARRIER() asm volatile("s_waitcnt lgkmcnt(0)\n\ts_barrier" ::: "memory")
#define THROTTLE(mask, i) asm volatile("s_bitcmp1_b32 %0, %1\n\ts_cbranch_scc0 1f\n\ts_waitcnt vmcnt(3)\n1:" :: "s"(mask), "n"(i) : "scc")

__device__ __forceinline__ unsigned clock10ns() { return (unsigned)__builtin_amdgcn_s_memrealtime(); }
__device__ __forceinline__ float dot2(unsigned a, unsigned b, float acc) {
  return __builtin_amdgcn_fdot2_f32_bf16(__builtin_bit_cast(bf16x2, a), __builtin_bit_cast(bf16x2, b), acc, false);
}
__device__ __forceinline__ f32x4 mfma(v4u a, v4u b, f32x4 c) {
  return __builtin_amdgcn_mfma_f32_16x16x32_bf16(__builtin_bit_cast(bf16x8, a), __builtin_bit_cast(bf16x8, b), c, 0, 0, 0);
}
__device__ __forceinline__ f32x4 smfma(v4u a, v8u b, f32x4 c, unsigned idx) {
  return __builtin_amdgcn_smfmac_f32_16x16x64_bf16(__builtin_bit_cast(bf16x8, a), __builtin_bit_cast(bf16x16, b), c, (int)idx, 0, 0);
}
__device__ __forceinline__ float bflo(unsigned u) { return __uint_as_float(u << 16); }
__device__ __forceinline__ float bfhi(unsigned u) { return __uint_as_float(u & 0xffff0000u); }
__device__ __forceinline__ unsigned pk_bf16(float lo, float hi) { bf16x2 r; r.x = (__bf16)lo; r.y = (__bf16)hi; return __builtin_bit_cast(unsigned, r); }
__device__ __forceinline__ unsigned bf_bits(float x) { return (unsigned)__builtin_bit_cast(unsigned short, (__bf16)x); }
__device__ __forceinline__ float rbf(float x) { return bflo(bf_bits(x)); }
__device__ __forceinline__ float exp2_(float x) { return __builtin_amdgcn_exp2f(x); }
__device__ __forceinline__ float rcp_(float x) { return __builtin_amdgcn_rcpf(x); }

template <int CTRL> __device__ __forceinline__ float dpp(float x) { return __int_as_float(__builtin_amdgcn_mov_dpp(__float_as_int(x), CTRL, 0xf, 0xf, true)); }
constexpr int XOR1 = 0xB1, XOR2 = 0x4E, XOR7 = 0x141, XOR8 = 0x128, ROR4 = 0x124, BCAST = 0x150;
__device__ __forceinline__ float tr32(float a, float b) {
  auto s = __builtin_amdgcn_permlane32_swap(__float_as_uint(a), __float_as_uint(b), false, false);
  return __uint_as_float(s[0]) + __uint_as_float(s[1]);
}
__device__ __forceinline__ float tr16(float a, float b) {
  auto s = __builtin_amdgcn_permlane16_swap(__float_as_uint(a), __float_as_uint(b), false, false);
  return __uint_as_float(s[0]) + __uint_as_float(s[1]);
}
template <int CTRL> __device__ __forceinline__ float trd(float lo, float hi, bool up) {
  const float t1 = lo + dpp<CTRL>(lo), t2 = hi + dpp<CTRL>(hi);
  return up ? t2 : t1;
}
__device__ __forceinline__ float wave_sum(float x) {
  x += dpp<XOR1>(x); x += dpp<XOR2>(x); x += dpp<XOR7>(x); x += dpp<XOR8>(x);
  x = tr16(x, x);
  return tr32(x, x);
}
__device__ __forceinline__ float wave_max(float x) {
  x = fmaxf(x, dpp<XOR1>(x)); x = fmaxf(x, dpp<XOR2>(x)); x = fmaxf(x, dpp<XOR7>(x)); x = fmaxf(x, dpp<XOR8>(x));
  auto s = __builtin_amdgcn_permlane16_swap(__float_as_uint(x), __float_as_uint(x), false, false);
  x = fmaxf(__uint_as_float(s[0]), __uint_as_float(s[1]));
  auto t = __builtin_amdgcn_permlane32_swap(__float_as_uint(x), __float_as_uint(x), false, false);
  return fmaxf(__uint_as_float(t[0]), __uint_as_float(t[1]));
}
__device__ __forceinline__ float sum4_parity(f32x4 v) {
  float s = tr16(tr32(v[0], v[2]), tr32(v[1], v[3]));
  s += dpp<XOR8>(s); s += dpp<ROR4>(s); s += dpp<XOR2>(s);
  return s;
}

__device__ __forceinline__ rsrc_t mk_rsrc(const void* p, unsigned bytes) { return __builtin_amdgcn_make_buffer_rsrc(const_cast<void*>(p), 0, bytes, 0x00020000); }
template <int AUX> __device__ __forceinline__ v4u ld128(rsrc_t r, unsigned v, unsigned s) { return __builtin_amdgcn_raw_buffer_load_b128(r, v, s, AUX); }
template <int AUX> __device__ __forceinline__ v2u ld64(rsrc_t r, unsigned v, unsigned s) { return __builtin_amdgcn_raw_buffer_load_b64(r, v, s, AUX); }
__device__ __forceinline__ void st128(v4u d, rsrc_t r, unsigned off) { __builtin_amdgcn_raw_buffer_store_b128(d, r, off, 0, SC1); }
__device__ __forceinline__ void st64(v2u d, rsrc_t r, unsigned off) { __builtin_amdgcn_raw_buffer_store_b64(d, r, off, 0, SC1); }
__device__ __forceinline__ unsigned tag1(float v, unsigned tag) { return ((__float_as_uint(v) + 0x800u) & 0xfffff000u) | (tag & 0xfffu); }
__device__ __forceinline__ v4u tag4(f32x4 v, unsigned tag) { return v4u{tag1(v[0], tag), tag1(v[1], tag), tag1(v[2], tag), tag1(v[3], tag)}; }
__device__ __forceinline__ f32x4 untag4(v4u g, unsigned tag12, unsigned& bad) {
  g ^= tag12;
  bad |= (g[0] | g[1]) | (g[2] | g[3]);
  return __builtin_bit_cast(f32x4, g);
}
__device__ __forceinline__ bool all_fresh(unsigned bad) { return !__any((bad & 0xfffu) != 0u); }

__device__ __forceinline__ float row_sum(float x) { x += dpp<XOR1>(x); x += dpp<XOR2>(x); x = tr16(x, x); return tr32(x, x); }
__device__ __forceinline__ void ln_stats(const v4u (&x)[4], float& mean, float& inv) {
  float s = 0.f;
#pragma unroll
  for (int t = 0; t < 4; ++t)
#pragma unroll
    for (int j = 0; j < 4; ++j) s += bflo(x[t][j]) + bfhi(x[t][j]);
  const float mu = row_sum(s) * (1.0f / DM);
  float q = 0.f;
#pragma unroll
  for (int t = 0; t < 4; ++t)
#pragma unroll
    for (int j = 0; j < 4; ++j) { const float d0 = bflo(x[t][j]) - mu, d1 = bfhi(x[t][j]) - mu; q = fmaf(d0, d0, q); q = fmaf(d1, d1, q); }
  const float var = row_sum(q) * (1.0f / DM);
  mean = rbf(mu);
  inv = rcp_(rbf(__builtin_amdgcn_sqrtf(rbf(rbf(var) + 1e-5f))));
}
__device__ __forceinline__ v4u ln_apply(v4u x, float mean, float inv) {
  v4u o;
#pragma unroll
  for (int j = 0; j < 4; ++j) { const unsigned d = pk_bf16(bflo(x[j]) - mean, bfhi(x[j]) - mean); o[j] = pk_bf16(bflo(d) * inv, bfhi(d) * inv); }
  return o;
}

template <int J, class T> __device__ __forceinline__ v4u bfrag(const T* t) {
  v4u f;
#pragma unroll
  for (int d = 0; d < 4; ++d) f[d] = __builtin_amdgcn_perm(t[2 * d + 1][J >> 1], t[2 * d][J >> 1], (J & 1) ? 0x07060302u : 0x05040100u);
  return f;
}
template <int LO, int HI> __device__ __forceinline__ void tile_load(v2u (&u)[32], rsrc_t r, unsigned voff, unsigned stride) {
#pragma unroll
  for (int n = LO; n < HI; ++n) u[n] = ld64<NT>(r, voff, n * stride);
  FENCE();
}
__device__ __forceinline__ f32x4 tile_mul(v4u a, const v2u (&u)[32]) {
  f32x4 o = {0.f, 0.f, 0.f, 0.f};
  static_for<16>([&](auto N) { constexpr int s = N >> 2, j = N & 3; o[j] += mfma(a, bfrag<j>(u + 8 * s), f32x4{0.f, 0.f, 0.f, 0.f})[s]; });
  return o;
}

constexpr unsigned WS_E = 0;
constexpr unsigned WS_LS = 4096;
constexpr unsigned WS_CK = 32768;
constexpr unsigned WS_X1 = 65536;
constexpr unsigned WS_MP = WS_X1 + 32 * 4096;
constexpr unsigned WS_TB = WS_MP + 16 * 8192;
constexpr unsigned WS_QX = WS_TB + 4 * 64 * 2048;
constexpr unsigned WS_BYTES = WS_QX + 32 * 8192;
constexpr int L_Q = 0, L_M = 256, L_F = 272, L_TAG = 288, L_REF = 292, L_RDY = 296, L_T0 = 300, L_T = 304, L_GX = 320, L_END_T = 384,
              L_H = 1024, L_O = 2048, O_ROW = 144, L_S = L_O + 16 * O_ROW, L_BYTES = L_S + 8192;

struct Params {
  const unsigned short* x; const unsigned short* k; const unsigned short* v; const unsigned short* wq; const unsigned short* wo;
  const unsigned short* w1; const unsigned short* b1; const unsigned short* w2; const unsigned short* b2;
  unsigned short* out; unsigned char* ws;
};

__global__ void __launch_bounds__(512, 2) k_fused(Params p) {
  __shared__ __attribute__((aligned(16))) unsigned char lds[L_BYTES];
  auto flag_get = [&](int off) { return __hip_atomic_load((lds_u32*)lds + off / 4, __ATOMIC_RELAXED, __HIP_MEMORY_SCOPE_WORKGROUP); };
  auto flag_set = [&](int off, unsigned v) { __hip_atomic_store((lds_u32*)lds + off / 4, v, __ATOMIC_RELAXED, __HIP_MEMORY_SCOPE_WORKGROUP); };
  const int blk = blockIdx.x, lane = threadIdx.x & 63, w = __builtin_amdgcn_readfirstlane(threadIdx.x >> 6);
  const int idx = blk >> 3, h = (blk & 7) * 4 + (idx & 3), bs = idx >> 2, b = bs >> 1, sp = bs & 1;
  const int r = lane >> 4, c = lane & 15;
  const unsigned l16 = lane * 16;
  unsigned kvoff = ((b * NH + h) * SK + sp * 2048) * (DH * 2), kx = (w & 3) * 8192 + bs * 65536;
  asm volatile("" : "+s"(kvoff), "+s"(kx));
  asm volatile("" :: "s"(p.x), "s"(p.k), "s"(p.v), "s"(p.wq), "s"(p.wo), "s"(p.w1), "s"(p.b1), "s"(p.w2), "s"(p.b2), "s"(p.out), "s"(p.ws));
  const unsigned rot = b * 32 + sp * 4;

  if (w >= 4) {
    const int a = w - 4, prod = 2 * h + sp;
    *(v4u*)(lds + L_M) = v4u{SENT, SENT, SENT, SENT}; *(v4u*)(lds + L_TAG) = v4u{0u, 0u, 0u, 0u};
    if (a != 0) WG_BARRIER();
    FENCE();
    const rsrc_t rws = mk_rsrc(p.ws, WS_BYTES), rx = mk_rsrc(p.x, NB * DM * 2);
    const unsigned xoff = ((lane >> 2) & 3) * (DM * 2) + ((lane & 3) + 4 * r) * 16;
    if (a == 0) {
      v4u xs[4]; v2u U[32];
#pragma unroll
      for (int t = 0; t < 4; ++t) xs[t] = ld128<0>(rx, xoff, ((t + b) & 3) * 256);
      FENCE();
      tile_load<0, 32>(U, mk_rsrc(p.wq + (size_t)b * 128 * (NH * DH) + h * DH + sp * 64, 128 * NH * DH * 2), r * 32 * 8192 + c * 8, 8192);
      const unsigned ep = __builtin_amdgcn_raw_buffer_load_b32(rws, 0, WS_E, SC1);
      FENCE();
      WG_BARRIER();
      float mean, inv;
      ln_stats(xs, mean, inv);
      const f32x4 g = tile_mul(ln_apply(xs[0], mean, inv), U);
      FENCE();
      const unsigned tg = __builtin_amdgcn_readfirstlane(ep) + 1u;
      st128(tag4(g, tg), rws, WS_QX + h * 8192 + bs * 1024 + l16);
      asm volatile("s_waitcnt vmcnt(0)" ::: "memory");
      __builtin_amdgcn_s_sleep(16);
      f32x4 q4;
#pragma unroll 1
      for (unsigned spin = 0; spin < SPIN_CAP; ++spin) {
        const v4u G0 = ld128<SC1>(rws, r * 1024 + c * 16, WS_QX + h * 8192 + b * 256), G1 = ld128<SC1>(rws, r * 1024 + c * 16, WS_QX + h * 8192 + b * 256 + 4096);
        unsigned bad = 0;
        q4 = untag4(G0, tg & 0xfffu, bad); q4 += untag4(G1, tg & 0xfffu, bad);
        if (all_fresh(bad)) break;
        ORDER();
      }
      *(unsigned*)(lds + L_Q + (lane & 31) * 8 + (lane >> 5) * 4) = pk_bf16(tr32(q4[0], q4[2]), tr32(q4[1], q4[3]));
      *(unsigned*)(lds + L_TAG) = tg;
      ORDER();
      flag_set(L_RDY, 1u);
    }
#pragma unroll 1
    for (unsigned spin = 0; spin < LDS_CAP; ++spin) {
      if (__builtin_amdgcn_readfirstlane(flag_get(L_RDY))) break;
      __builtin_amdgcn_s_sleep(4);
    }
    ORDER();
    const unsigned tag = __builtin_amdgcn_readfirstlane(*(const unsigned*)(lds + L_TAG));

    if (a == 2 && h < 28) {
      const int cg = (h & 1) * 4 + b;
      const unsigned stride = sp ? DM * 2 : FF * 2;
      const rsrc_t rt = mk_rsrc(sp ? p.w2 + (size_t)(h >> 1) * 128 * DM + cg * 64 : p.w1 + (size_t)b * 128 * FF + h * 64, 128 * stride);
      v2u U[32]; v4u A;
      tile_load<0, 16>(U, rt, r * 32 * stride + c * 8, stride);
      TOUCH(U[15]); FENCE();
      tile_load<16, 32>(U, rt, r * 32 * stride + c * 8, stride);
      if (sp == 0) {
        v4u xs[4];
#pragma unroll
        for (int t = 0; t < 4; ++t) xs[t] = ld128<0>(rx, xoff, ((t + b) & 3) * 256);
#pragma unroll 1
        for (int it = 0; it < 2; ++it) {
          float mean, inv;
          ln_stats(xs, mean, inv);
#pragma unroll
          for (int t = 0; t < 4; ++t) xs[t] = ln_apply(xs[t], mean, inv);
        }
        A = xs[0];
      } else {
        v2u b1v[2];
#pragma unroll
        for (int k = 0; k < 2; ++k) b1v[k] = ld64<0>(mk_rsrc(p.b1 + (h >> 1) * 128, 256), c * 8, k * 128);
        f32x4 z4[2];
#pragma unroll 1
        for (unsigned spin = 0; spin < SPIN_CAP; ++spin) {
          __builtin_amdgcn_s_sleep(112); __builtin_amdgcn_s_sleep(112);
          v4u G[8];
#pragma unroll
          for (int j = 0; j < 8; ++j) G[j] = ld128<SC1>(rws, l16, WS_X1 + (h >> 1) * 8192 + j * 1024);
          unsigned bad = 0;
#pragma unroll
          for (int j = 0; j < 8; ++j) { const f32x4 v = untag4(G[j], tag & 0xfffu, bad); z4[j >> 2] = (j & 3) ? z4[j >> 2] + v : v; }
          if (all_fresh(bad)) break;
          ORDER();
        }
#pragma unroll
        for (int k = 0; k < 2; ++k) {
          float hv[4];
#pragma unroll
          for (int j = 0; j < 4; ++j) {
            const float z = rbf(rbf(z4[k][j]) + ((j & 1) ? bfhi(b1v[k][j >> 1]) : bflo(b1v[k][j >> 1])));
            hv[j] = z * rcp_(1.0f + exp2_((-2.0f * 0.7978845608028654f * LOG2E) * (z + 0.044715f * z * z * z)));
          }
          *(v2u*)(lds + L_H + r * 256 + k * 128 + c * 8) = v2u{pk_bf16(hv[0], hv[1]), pk_bf16(hv[2], hv[3])};
        }
        asm volatile("s_waitcnt lgkmcnt(0)" ::: "memory");
        A = *(const v4u*)(lds + L_H + ((lane >> 2) & 3) * 256 + ((lane & 3) + 4 * r) * 16);
      }
      st128(tag4(tile_mul(A, U), tag), rws, (sp ? WS_MP + (h >> 1) * 8192 + cg * 1024 : WS_X1 + h * 4096 + b * 1024) + l16);
    }

    const unsigned ck_st = (lane == 0 && (idx & 3) == 0) ? WS_CK + (bs * 8 + (blk & 7)) * 8 : OOB;
    unsigned t0 = 0;
    if (a == 3) {
#pragma unroll 1
      for (unsigned spin = 0; spin < LDS_CAP; ++spin) {
        t0 = __builtin_amdgcn_readfirstlane(flag_get(L_T0));
        if (t0) break;
        __builtin_amdgcn_s_sleep(4);
      }
      st64(v2u{t0, tag}, rws, ck_st);
    }

    v8u Bf[2][8];
    {
      const rsrc_t ro = mk_rsrc(p.wo + (size_t)h * DH * DM + a * 128, DH * DM * 2);
      const unsigned vo = r * 8 * 1024 + c * 16;
      v4u T[32]; v2u ck;
      static_for<32 + DO>([&](auto N) { constexpr int n = N;
        if constexpr (n >= DO) TOUCH(T[n - DO]);
        FENCE();
        if constexpr (n == 10) ck = ld64<SC1>(rws, a == 3 ? lane * 8 : OOB, WS_CK);
        if constexpr (n == 10 + DO) { TOUCH(ck);
          if (a == 3) {
            const unsigned tn = clock10ns();
            const float dt = wave_sum((float)(int)((ck[1] == tag ? ck[0] : tn) - t0)) * (1.0f / 64);
            flag_set(L_REF, (t0 + (int)dt + (128 + LD - NA) * P_TAUK / 16 - P_OFF0) | 1u); } }
        FENCE();
        if constexpr (n < 32) T[n] = ld128<0>(ro, vo, (((n >> 3) * 32 + (n & 7)) ^ rot) * 1024);
        FENCE();
        if constexpr (n >= DO && ((n - DO) & 7) == 7) { constexpr int s = (n - DO) >> 3;
          static_for<8>([&](auto J) { constexpr int j = J; const v4u f = bfrag<j>(T + 8 * s);
            if constexpr (s & 1) Bf[s >> 1][j].hi = f; else Bf[s >> 1][j].lo = f;
            PIN(Bf[s >> 1][j]); }); }
        FENCE(); });
    }

    const int cons = (blk & 7) * 8 + (idx & 7), col0 = 32 * (cons >> 2) + 4 * (cons & 1) + (cons & 2);
    const unsigned co = (lane & 1) * 32 + (r >> 1) * 16 + (r & 1) * 2;
    if (a == 3) {
      unsigned mb;
#pragma unroll 1
      for (unsigned spin = 0; spin < LDS_CAP; ++spin) {
        mb = flag_get(L_M + 4 * (lane & 3));
        if (__all(mb != SENT)) break;
        __builtin_amdgcn_s_sleep(4);
      }
      ORDER();
      const float m4 = __uint_as_float(mb);
      float M = fmaxf(m4, dpp<XOR1>(m4)); M = fmaxf(M, dpp<XOR2>(M));
      float L = 0.f;
#pragma unroll
      for (int i = 0; i < 8; ++i) { const f32x4 s4 = *(const f32x4*)(lds + L_S + i * 1024 + l16);
#pragma unroll
        for (int j = 0; j < 4; ++j) L += exp2_(s4[j] - M); }
      L = wave_sum(L);
      const float lse = M + __builtin_amdgcn_logf(L);
      const unsigned tk = flag_get(L_T);
      st128(v4u{__float_as_uint(lse), tag, tk, tag}, rws, lane == 0 ? WS_LS + (b * 64 + prod) * 16 : OOB);
      const unsigned long long need = blk == 0 ? ~0ull : 1ull << (prod ^ 1);
      float other; unsigned mean;
#pragma unroll 1
      for (unsigned spin = 0; spin < SPIN_CAP; ++spin) {
        __builtin_amdgcn_s_sleep(80);
        const v4u m = ld128<SC1>(rws, l16, WS_LS + b * 1024);
        const bool ok = ((m[1] ^ tag) | (m[3] ^ tag)) == 0u;
        const unsigned tn = clock10ns();
        const float dt = __builtin_amdgcn_fmed3f(wave_sum((float)(int)((ok ? m[2] : tn) - tk)) * (1.0f / 64), -1000.f, 1000.f);
        mean = __builtin_amdgcn_readfirstlane(tk + (int)dt);
        flag_set(L_REF, (mean - P_OFF1) | 1u);
        other = __uint_as_float(__builtin_amdgcn_readlane(m[0], prod ^ 1));
        if ((__ballot(ok) & need) == need) break;
        ORDER();
      }
      *(float*)(lds + L_F + 4 * (lane & 3)) = exp2_(m4 - M) * rcp_(L) * rcp_(1.0f + exp2_(other - lse));
      __builtin_amdgcn_raw_buffer_store_b32(tag, rws, (blk == 0 && lane == 0) ? WS_E : OOB, 0, SC1);
      while ((int)(clock10ns() - mean) < P_C1) __builtin_amdgcn_s_sleep(2);
      st64(v2u{flag_get(L_T), tag}, rws, ck_st + 512);
      __builtin_amdgcn_s_sleep(36);
      const v2u ck = ld64<SC1>(rws, lane * 8, WS_CK + 512);
      const bool ok = ck[1] == tag;
      const int dt = (int)(wave_sum(ok ? (float)(int)(ck[0] - mean) : 0.f) * rcp_(fmaxf(wave_sum(ok ? 1.f : 0.f), 1.f)));
      flag_set(L_REF, (mean + dt - P_OFF2) | 1u);
      flag_set(L_END_T, mean + dt + dt * P_EX / 4 + 8 * P_TAUV + P_TP);
    } else if (a == 2) {
      const unsigned b2v = __builtin_amdgcn_raw_buffer_load_b16(mk_rsrc(p.b2, DM * 2), co, col0 * 2, 0);
      const unsigned xv = __builtin_amdgcn_raw_buffer_load_b16(rx, co, (b * DM + col0) * 2, 0);
      const unsigned mo = lane < 28 ? (unsigned)((lane >> 1) * 8192 + (lane & 1) * 64) : OOB, ms = WS_MP + (col0 >> 6) * 1024 + b * 256 + (col0 & 63) * 4;
      f32x4 m4;
#pragma unroll 1
      for (unsigned spin = 0; spin < SPIN_CAP; ++spin) {
        __builtin_amdgcn_s_sleep(64);
        const v2u G0 = ld64<SC1>(rws, mo, ms), G1 = ld64<SC1>(rws, mo, ms + 32);
        unsigned bad = 0;
        m4 = untag4(v4u{G0[0], G0[1], G1[0], G1[1]}, lane < 28 ? tag & 0xfffu : 0u, bad);
        if (all_fresh(bad)) break;
        ORDER();
      }
      const float m = rbf(rbf(sum4_parity(m4)) + bflo(b2v));
      *(f32x2*)(lds + L_GX + (r * 2 + (lane & 1)) * 8) = f32x2{rbf(m * rcp_(1.0f + exp2_(-LOG2E * m))), bflo(xv)};
    }

    unsigned a_off = L_O + c * O_ROW + r * 16, sidx = (lane & 2) ? 0xeeeeu : 0x4444u;
    unsigned tb_off = WS_TB + (b * 64 + prod) * 2048 + a * 512 + (c >> 2) * 128 + r * 32 + (c & 3) * 8;
    unsigned go = (lane >> 1) * 2048 + (lane & 1) * 16, out_off = (lane & 14) == 0 ? (b * DM + col0) * 2 + co : OOB;
    f32x4 d[8];
#pragma unroll
    for (int j = 0; j < 8; ++j) { d[j] = f32x4{0.f, 0.f, 0.f, 0.f}; PIN(d[j]); }
    PIN(a_off); PIN(sidx); PIN(tb_off); PIN(go); PIN(out_off);
    asm volatile("s_branch 1f\n\t.p2align 11\n1:");
    WG_BARRIER();
    const v4u A0 = *(const v4u*)(lds + a_off), A1 = *(const v4u*)(lds + a_off + 64);
    const float f = *(const float*)(lds + L_F + r * 4);
    FENCE();
    float t[8];
    auto mul = [&](int j) { d[j] = smfma(A0, Bf[0][j], d[j], sidx); d[j + 4] = smfma(A0, Bf[0][j + 4], d[j + 4], sidx);
      d[j] = smfma(A1, Bf[1][j], d[j], sidx); d[j + 4] = smfma(A1, Bf[1][j + 4], d[j + 4], sidx); };
    auto sum = [&](int j) { const f32x2 u2 = d[j].lo + d[j].hi; t[j] = (u2[0] + u2[1]) * f; PIN(t[j]); };
    mul(0); mul(2); mul(1); mul(3);
    sum(0); sum(4); sum(2); sum(6);
    unsigned w0 = tag1(tr32(tr16(t[0], t[4]), tr16(t[2], t[6])), tag); PIN(w0);
    sum(1); sum(5);
    float x1 = tr16(t[1], t[5]); PIN(x1);
    sum(3); sum(7);
    const unsigned w1 = tag1(tr32(x1, tr16(t[3], t[7])), tag);
    __builtin_amdgcn_sched_group_barrier(0x8, 7, 0);
#pragma unroll
    for (int k = 0; k < 9; ++k) { __builtin_amdgcn_sched_group_barrier(0x2, 3, 0); __builtin_amdgcn_sched_group_barrier(0x8, 1, 0); }
    FENCE();
    st64(v2u{w0, w1}, rws, tb_off);
    if (a == 0) {
      const f32x2 gx = *(const f32x2*)(lds + L_GX + (r * 2 + (lane & 1)) * 8);
      const unsigned t1 = clock10ns();
      const int wait = (int)(__builtin_amdgcn_readfirstlane(flag_get(L_END_T)) - t1);
      __builtin_amdgcn_s_sleep(4);
      while ((int)(clock10ns() - t1) < (wait < 120 ? wait : 120)) {}
      f32x4 s4;
#pragma unroll 1
      for (unsigned spin = 0; spin < SPIN_CAP; ++spin) {
        const v4u G0 = ld128<SC1>(rws, go, WS_TB + b * 131072 + cons * 32), G1 = ld128<SC1>(rws, go, WS_TB + b * 131072 + 65536 + cons * 32);
        unsigned bad = 0;
        s4 = untag4(G0, tag & 0xfffu, bad); s4 += untag4(G1, tag & 0xfffu, bad);
        if (all_fresh(bad)) break;
        __builtin_amdgcn_s_sleep(1);
        ORDER();
      }
      const float o = gx[1] + rbf(rbf(sum4_parity(s4)) * gx[0]);
      __builtin_amdgcn_raw_buffer_store_b16((unsigned short)bf_bits(o), mk_rsrc(p.out, NB * DM * 2), out_off, 0, 0);
    }
    return;
  }

  constexpr unsigned KVLEN = 2048 * DH * 2;
  const char* kp = (const char*)p.k + kvoff; const char* vp = (const char*)p.v + kvoff;
  const rsrc_t rk = mk_rsrc(kp, KVLEN);
  auto kvload = [&](rsrc_t rs, unsigned o, int i) { return ld128<NT>(rs, l16 + (i & 3) * 1024, o ^ (((i >> 2) & 1) * 4096 + (i >> 3) * 32768)); };
  float* const scs = (float*)(lds + L_S + w * 2048) + lane;
  v4u S[NU + LD], q;
  unsigned now, refv = 0;
  auto pace = [&](int sched) {
    const unsigned rf = __builtin_amdgcn_readfirstlane(refv);
    int lv = ((int)(now - rf) - sched) >> 5;
    asm volatile("" : "+s"(lv));
    lv = rf ? lv : 3;
    unsigned m = 0xffffu; m = lv > 0 ? 0x5555u : m; m = lv > 1 ? 0x1111u : m; m = lv > 2 ? 0x1249u : m;
    m = lv > 3 ? 0x0421u : m; m = lv > 4 ? 0x0101u : m; m = lv > 5 ? 0u : m;
    return (unsigned)__builtin_amdgcn_readfirstlane(m); };
  float ka[8], mw = -INFINITY;
  auto kdot = [&](v4u t, int i) {
    float d = dot2(t[0], q[0], 0.f); d = dot2(t[1], q[1], d); d = dot2(t[2], q[2], d); d = dot2(t[3], q[3], d);
    if (i >= 8) { i -= 8; d = trd<XOR8>(ka[i], d, lane & 8);
      if (i >= 4) { i -= 4; d = trd<XOR7>(ka[i], d, lane & 4);
        if (i >= 2) { i -= 2; d = trd<XOR2>(ka[i], d, lane & 2);
          if (i >= 1) { i -= 1; d = trd<XOR1>(ka[i], d, lane & 1); } } } }
    PIN(d); ka[i] = d; };
  auto kdone = [&](int g) { const float s = ka[0] * C2; scs[g * 64] = s; mw = fmaxf(mw, s); PIN(mw); };

  static_for<NU + LD>([&](auto N) {
    constexpr int n = N, dn = n < N0 ? D0 : D1;
    if constexpr (n >= dn) TOUCH(S[n - dn]);
    FENCE();
    S[n] = kvload(rk, kx, n);
    if constexpr (n == 0) __builtin_amdgcn_s_setprio(2);
    if constexpr (n == NS0) { FENCE(); WG_BARRIER(); }
    FENCE();
    if constexpr (n == NA - 1) {
#pragma unroll 1
      for (unsigned spin = 0; spin < LDS_CAP; ++spin) {
        const unsigned rdy = flag_get(L_RDY);
        ORDER();
        q = *(const v4u*)(lds + L_Q + c * 16);
        if (__builtin_amdgcn_readfirstlane(rdy)) break;
        __builtin_amdgcn_s_sleep(1);
      }
      now = clock10ns();
    }
    if constexpr (n == NA) flag_set(L_T0, now | 1u);
    if constexpr (n >= NA) {
      constexpr int hi = (n - NA + 1) * 4 < n - LD + 1 ? (n - NA + 1) * 4 : n - LD + 1;
      constexpr int lo = (n - NA) * 4 < n - LD ? (n - NA) * 4 : n - LD;
      static_for<hi - lo>([&](auto J) { constexpr int i = lo + J; kdot(S[i], i & 15); if constexpr ((i & 15) == 15) kdone(i >> 4); });
    }
    FENCE();
  });
  v4u R[16];
#pragma unroll
  for (int i = 0; i < LD; ++i) R[i] = S[NU + i];
  float snext = scs[0];
  v4u sel;
#pragma unroll
  for (int j = 0; j < 4; ++j) { const unsigned pick = (c & 8) ? 0x0302u : 0x0100u;
    sel[j] = ((c >> 1) & 3) != j ? 0x0c0c0c0cu : (c & 1) ? (pick << 16) | 0x0c0cu : 0x0c0c0000u | pick; }
  unsigned o_off = L_O + (4 * w + (r & 2)) * O_ROW + ((c * 8 + (r & 1) * 4) ^ rot);
  PIN(o_off); PIN(sel);
#pragma unroll 1
  for (int g = NU / 16; g < 8; ++g) {
    const rsrc_t rn = mk_rsrc(g == 7 ? vp : kp, KVLEN);
    const unsigned o0 = (g * 65536) ^ kx, o1 = (((g + 1) & 7) * 65536) ^ kx;
    const unsigned mask = pace((g - 8) * P_TAUK);
    static_for<16>([&](auto I) { constexpr int i = I;
      TOUCH(R[i]); THROTTLE(mask, i); FENCE();
      R[(i + LD) & 15] = i + LD < 16 ? kvload(rk, o0, i + LD) : kvload(rn, o1, i + LD - 16);
      if constexpr (i == 15) { now = clock10ns(); refv = flag_get(L_REF); }
      FENCE();
      kdot(R[i], i);
      FENCE(); });
    kdone(g);
  }
  mw = wave_max(mw);
  flag_set(L_T + 4 * w, now);
  ORDER();
  flag_set(L_M + 4 * w, min(__float_as_uint(mw), SENT - 1u));
  f32x4 acc = {0.f, 0.f, 0.f, 0.f};
  const rsrc_t rv = mk_rsrc(vp, KVLEN);
#pragma unroll 1
  for (int g = 0; g < 8; ++g) {
    const rsrc_t rn = mk_rsrc(vp, g == 7 ? 0u : KVLEN);
    const unsigned o0 = (g * 65536) ^ kx, o1 = (((g + 1) & 7) * 65536) ^ kx;
    const float pg = exp2_(snext - mw), ph = rbf(pg);
    const unsigned pp = pk_bf16(ph, pg - ph);
    snext = scs[((g + 1) & 7) * 64];
    flag_set(L_T + 4 * w, now - g * P_TAUV);
    const unsigned mask = pace(g * P_TAUV) & (g == 7 ? 0xffu : 0xffffu);
    static_for<16>([&](auto I) { constexpr int i = I;
      const unsigned pb = __builtin_amdgcn_mov_dpp(pp, BCAST + i, 0xf, 0xf, true);
      v4u A;
#pragma unroll
      for (int j = 0; j < 4; ++j) A[j] = __builtin_amdgcn_perm(pb, pb, sel[j]);
      PIN(A);
      FENCE();
      TOUCH(R[i]); THROTTLE(mask, i); FENCE();
      R[(i + LD) & 15] = i + LD < 16 ? kvload(rv, o0, i + LD) : kvload(rn, o1, i + LD - 16);
      if constexpr (i == 15) { now = clock10ns(); refv = flag_get(L_REF); }
      FENCE();
      acc = mfma(A, R[i], acc);
      FENCE(); });
    const float e0 = tr32(acc[0], acc[2]), e1 = tr32(acc[1], acc[3]);
    const unsigned hi = pk_bf16(e0, e1), lo = pk_bf16(e0 - bflo(hi), e1 - bfhi(hi));
    *(unsigned*)(lds + o_off) = hi;
    *(unsigned*)(lds + o_off + O_ROW) = lo;
    ORDER();
  }
  WG_BARRIER();
}

extern "C" void kernel_launch(void* const* d_in, const int* in_sizes, int n_in, void* d_out, int out_size, void* d_ws, size_t ws_size,
                              hipStream_t stream) {
  Params p;
  p.x = (const unsigned short*)d_in[0]; p.k = (const unsigned short*)d_in[1]; p.v = (const unsigned short*)d_in[2];
  p.wq = (const unsigned short*)d_in[3]; p.wo = (const unsigned short*)d_in[4];
  p.w1 = (const unsigned short*)d_in[6]; p.b1 = (const unsigned short*)d_in[7]; p.w2 = (const unsigned short*)d_in[8]; p.b2 = (const unsigned short*)d_in[9];
  p.out = (unsigned short*)d_out;
  p.ws = (unsigned char*)d_ws;
  k_fused<<<256, 512, 0, stream>>>(p);
}
```

```cpp
#include <hip/hip_runtime.h>
#include <hip/hip_bf16.h>
#include <stdint.h>
#include <utility>

typedef unsigned v2u __attribute__((ext_vector_type(2)));
typedef unsigned v4u __attribute__((ext_vector_type(4)));
typedef unsigned v8u __attribute__((ext_vector_type(8)));
typedef float f32x2 __attribute__((ext_vector_type(2)));
typedef float f32x4 __attribute__((ext_vector_type(4)));
typedef __bf16 bf16x2 __attribute__((ext_vector_type(2)));
typedef __bf16 bf16x8 __attribute__((ext_vector_type(8)));
typedef __bf16 bf16x16 __attribute__((ext_vector_type(16)));
typedef __amdgpu_buffer_rsrc_t rsrc_t;
typedef __attribute__((address_space(3))) unsigned lds_u32;

constexpr int NB = 4, DM = 512, NH = 32, SK = 4096, DH = 128, FF = 1792;
constexpr int NT = 2, SC1 = 16;
constexpr unsigned SENT = 0xFFFFFFFFu, OOB = 0x80000000u;
constexpr unsigned SPIN_CAP = 1u << 14, LDS_CAP = 1u << 20;
constexpr float LOG2E = 1.4426950408889634f, C2 = 0.088388347648318440f * LOG2E;

#define P_NA 40
constexpr int D0 = 7, N0 = 10, D1 = 5;
constexpr int NS0 = D0 + 7;
constexpr int NA = P_NA;
constexpr int NU = 48;
constexpr int LD = 8;
constexpr int DO = 3;
static_assert((NU + LD - NA) * 4 >= NU, "the backlog is gone before the loop starts");
#define P_TAUK 228
#define P_TAUV 215
#define P_OFF0 70
#define P_OFF1 120
#define P_OFF2 135
#define P_C1 1100
#define P_TP 82
#define P_EX 0

template <class F, int... I> __device__ __forceinline__ void static_for_impl(F&& f, std::integer_sequence<int, I...>) { (f(std::integral_constant<int, I>{}), ...); }
template <int N, class F> __device__ __forceinline__ void static_for(F&& f) { static_for_impl(f, std::make_integer_sequence<int, N>{}); }

#define TOUCH(x) asm volatile("" :: "v"(x))
#define PIN(x) asm volatile("" : "+v"(x))
#define ORDER() asm volatile("" ::: "memory")
#define FENCE() __builtin_amdgcn_sched_barrier(0)
#define WG_BARRIER() asm volatile("s_waitcnt lgkmcnt(0)\n\ts_barrier" ::: "memory")
#define THROTTLE(mask, i) asm volatile("s_bitcmp1_b32 %0, %1\n\ts_cbranch_scc0 1f\n\ts_waitcnt vmcnt(3)\n1:" :: "s"(mask), "n"(i) : "scc")

__device__ __forceinline__ unsigned clock10ns() { return (unsigned)__builtin_amdgcn_s_memrealtime(); }
__device__ __forceinline__ float dot2(unsigned a, unsigned b, float acc) {
  return __builtin_amdgcn_fdot2_f32_bf16(__builtin_bit_cast(bf16x2, a), __builtin_bit_cast(bf16x2, b), acc, false);
}
__device__ __forceinline__ f32x4 mfma(v4u a, v4u b, f32x4 c) {
  return __builtin_amdgcn_mfma_f32_16x16x32_bf16(__builtin_bit_cast(bf16x8, a), __builtin_bit_cast(bf16x8, b), c, 0, 0, 0);
}
__device__ __forceinline__ f32x4 smfma(v4u a, v8u b, f32x4 c, unsigned idx) {
  return __builtin_amdgcn_smfmac_f32_16x16x64_bf16(__builtin_bit_cast(bf16x8, a), __builtin_bit_cast(bf16x16, b), c, (int)idx, 0, 0);
}
__device__ __forceinline__ float bflo(unsigned u) { return __uint_as_float(u << 16); }
__device__ __forceinline__ float bfhi(unsigned u) { return __uint_as_float(u & 0xffff0000u); }
__device__ __forceinline__ unsigned pk_bf16(float lo, float hi) { bf16x2 r; r.x = (__bf16)lo; r.y = (__bf16)hi; return __builtin_bit_cast(unsigned, r); }
__device__ __forceinline__ unsigned bf_bits(float x) { return (unsigned)__builtin_bit_cast(unsigned short, (__bf16)x); }
__device__ __forceinline__ float rbf(float x) { return bflo(bf_bits(x)); }
__device__ __forceinline__ float exp2_(float x) { return __builtin_amdgcn_exp2f(x); }
__device__ __forceinline__ float rcp_(float x) { return __builtin_amdgcn_rcpf(x); }

template <int CTRL> __device__ __forceinline__ float dpp(float x) { return __int_as_float(__builtin_amdgcn_mov_dpp(__float_as_int(x), CTRL, 0xf, 0xf, true)); }
constexpr int XOR1 = 0xB1, XOR2 = 0x4E, XOR7 = 0x141, XOR8 = 0x128, ROR4 = 0x124, BCAST = 0x150;
__device__ __forceinline__ float tr32(float a, float b) {
  auto s = __builtin_amdgcn_permlane32_swap(__float_as_uint(a), __float_as_uint(b), false, false);
  return __uint_as_float(s[0]) + __uint_as_float(s[1]);
}
__device__ __forceinline__ float tr16(float a, float b) {
  auto s = __builtin_amdgcn_permlane16_swap(__float_as_uint(a), __float_as_uint(b), false, false);
  return __uint_as_float(s[0]) + __uint_as_float(s[1]);
}
template <int CTRL> __device__ __forceinline__ float trd(float lo, float hi, bool up) {
  const float t1 = lo + dpp<CTRL>(lo), t2 = hi + dpp<CTRL>(hi);
  return up ? t2 : t1;
}
__device__ __forceinline__ float wave_sum(float x) {
  x += dpp<XOR1>(x); x += dpp<XOR2>(x); x += dpp<XOR7>(x); x += dpp<XOR8>(x);
  x = tr16(x, x);
  return tr32(x, x);
}
__device__ __forceinline__ float wave_max(float x) {
  x = fmaxf(x, dpp<XOR1>(x)); x = fmaxf(x, dpp<XOR2>(x)); x = fmaxf(x, dpp<XOR7>(x)); x = fmaxf(x, dpp<XOR8>(x));
  auto s = __builtin_amdgcn_permlane16_swap(__float_as_uint(x), __float_as_uint(x), false, false);
  x = fmaxf(__uint_as_float(s[0]), __uint_as_float(s[1]));
  auto t = __builtin_amdgcn_permlane32_swap(__float_as_uint(x), __float_as_uint(x), false, false);
  return fmaxf(__uint_as_float(t[0]), __uint_as_float(t[1]));
}
__device__ __forceinline__ float sum4_parity(f32x4 v) {
  float s = tr16(tr32(v[0], v[2]), tr32(v[1], v[3]));
  s += dpp<XOR8>(s); s += dpp<ROR4>(s); s += dpp<XOR2>(s);
  return s;
}

__device__ __forceinline__ rsrc_t mk_rsrc(const void* p, unsigned bytes) { return __builtin_amdgcn_make_buffer_rsrc(const_cast<void*>(p), 0, bytes, 0x00020000); }
template <int AUX> __device__ __forceinline__ v4u ld128(rsrc_t r, unsigned v, unsigned s) { return __builtin_amdgcn_raw_buffer_load_b128(r, v, s, AUX); }
template <int AUX> __device__ __forceinline__ v2u ld64(rsrc_t r, unsigned v, unsigned s) { return __builtin_amdgcn_raw_buffer_load_b64(r, v, s, AUX); }
__device__ __forceinline__ void st128(v4u d, rsrc_t r, unsigned off) { __builtin_amdgcn_raw_buffer_store_b128(d, r, off, 0, SC1); }
__device__ __forceinline__ void st64(v2u d, rsrc_t r, unsigned off) { __builtin_amdgcn_raw_buffer_store_b64(d, r, off, 0, SC1); }
__device__ __forceinline__ unsigned tag1(float v, unsigned tag) { return ((__float_as_uint(v) + 0x800u) & 0xfffff000u) | (tag & 0xfffu); }
__device__ __forceinline__ v4u tag4(f32x4 v, unsigned tag) { return v4u{tag1(v[0], tag), tag1(v[1], tag), tag1(v[2], tag), tag1(v[3], tag)}; }
__device__ __forceinline__ f32x4 untag4(v4u g, unsigned tag12, unsigned& bad) {
  g ^= tag12;
  bad |= (g[0] | g[1]) | (g[2] | g[3]);
  return __builtin_bit_cast(f32x4, g);
}
__device__ __forceinline__ bool all_fresh(unsigned bad) { return !__any((bad & 0xfffu) != 0u); }

__device__ __forceinline__ float row_sum(float x) { x += dpp<XOR1>(x); x += dpp<XOR2>(x); x = tr16(x, x); return tr32(x, x); }
__device__ __forceinline__ void ln_stats(const v4u (&x)[4], float& mean, float& inv) {
  float s = 0.f;
#pragma unroll
  for (int t = 0; t < 4; ++t)
#pragma unroll
    for (int j = 0; j < 4; ++j) s += bflo(x[t][j]) + bfhi(x[t][j]);
  const float mu = row_sum(s) * (1.0f / DM);
  float q = 0.f;
#pragma unroll
  for (int t = 0; t < 4; ++t)
#pragma unroll
    for (int j = 0; j < 4; ++j) { const float d0 = bflo(x[t][j]) - mu, d1 = bfhi(x[t][j]) - mu; q = fmaf(d0, d0, q); q = fmaf(d1, d1, q); }
  const float var = row_sum(q) * (1.0f / DM);
  mean = rbf(mu);
  inv = rcp_(rbf(__builtin_amdgcn_sqrtf(rbf(rbf(var) + 1e-5f))));
}
__device__ __forceinline__ v4u ln_apply(v4u x, float mean, float inv) {
  v4u o;
#pragma unroll
  for (int j = 0; j < 4; ++j) { const unsigned d = pk_bf16(bflo(x[j]) - mean, bfhi(x[j]) - mean); o[j] = pk_bf16(bflo(d) * inv, bfhi(d) * inv); }
  return o;
}

template <int J, class T> __device__ __forceinline__ v4u bfrag(const T* t) {
  v4u f;
#pragma unroll
  for (int d = 0; d < 4; ++d) f[d] = __builtin_amdgcn_perm(t[2 * d + 1][J >> 1], t[2 * d][J >> 1], (J & 1) ? 0x07060302u : 0x05040100u);
  return f;
}
template <int LO, int HI> __device__ __forceinline__ void tile_load(v2u (&u)[32], rsrc_t r, unsigned voff, unsigned stride) {
#pragma unroll
  for (int n = LO; n < HI; ++n) u[n] = ld64<NT>(r, voff, n * stride);
  FENCE();
}
__device__ __forceinline__ f32x4 tile_mul(v4u a, const v2u (&u)[32]) {
  f32x4 o = {0.f, 0.f, 0.f, 0.f};
  static_for<16>([&](auto N) { constexpr int s = N >> 2, j = N & 3; o[j] += mfma(a, bfrag<j>(u + 8 * s), f32x4{0.f, 0.f, 0.f, 0.f})[s]; });
  return o;
}

constexpr unsigned WS_E = 0;
constexpr unsigned WS_LS = 4096;
constexpr unsigned WS_CK = 32768;
constexpr unsigned WS_X1 = 65536;
constexpr unsigned WS_MP = WS_X1 + 32 * 4096;
constexpr unsigned WS_TB = WS_MP + 16 * 8192;
constexpr unsigned WS_QX = WS_TB + 4 * 64 * 2048;
constexpr unsigned WS_BYTES = WS_QX + 32 * 8192;
constexpr int L_Q = 0, L_M = 256, L_F = 272, L_TAG = 288, L_REF = 292, L_RDY = 296, L_T0 = 300, L_T = 304, L_GX = 320, L_END_T = 384,
              L_H = 1024, L_O = 2048, O_ROW = 144, L_S = L_O + 16 * O_ROW, L_BYTES = L_S + 8192;

struct Params {
  const unsigned short* x; const unsigned short* k; const unsigned short* v; const unsigned short* wq; const unsigned short* wo;
  const unsigned short* w1; const unsigned short* b1; const unsigned short* w2; const unsigned short* b2;
  unsigned short* out; unsigned char* ws;
};

#define DIAG 2
__global__ void __launch_bounds__(512, 2) k_fused(Params p_arg) {
  unsigned long long tmark = 0;
  const __attribute__((address_space(4))) Params* kap = (const __attribute__((address_space(4))) Params*)__builtin_amdgcn_kernarg_segment_ptr();
  if (DIAG == 1) asm volatile("s_memrealtime %0\n\ts_waitcnt lgkmcnt(0)" : "=s"(tmark), "+s"(kap));
  Params p; p.x = kap->x; p.k = kap->k; p.v = kap->v; p.wq = kap->wq; p.wo = kap->wo; p.w1 = kap->w1; p.b1 = kap->b1; p.w2 = kap->w2; p.b2 = kap->b2; p.out = kap->out; p.ws = kap->ws;
  if (DIAG == 2) asm volatile("s_waitcnt lgkmcnt(0)\n\ts_memrealtime %0\n\ts_waitcnt lgkmcnt(0)" : "=s"(tmark), "+s"(p.x), "+s"(p.ws), "+s"(p.b2));
  __shared__ __attribute__((aligned(16))) unsigned char lds[L_BYTES];
  auto flag_get = [&](int off) { return __hip_atomic_load((lds_u32*)lds + off / 4, __ATOMIC_RELAXED, __HIP_MEMORY_SCOPE_WORKGROUP); };
  auto flag_set = [&](int off, unsigned v) { __hip_atomic_store((lds_u32*)lds + off / 4, v, __ATOMIC_RELAXED, __HIP_MEMORY_SCOPE_WORKGROUP); };
  const int blk = blockIdx.x, lane = threadIdx.x & 63, w = __builtin_amdgcn_readfirstlane(threadIdx.x >> 6);
  const int idx = blk >> 3, h = (blk & 7) * 4 + (idx & 3), bs = idx >> 2, b = bs >> 1, sp = bs & 1;
  const int r = lane >> 4, c = lane & 15;
  const unsigned l16 = lane * 16;
  unsigned kvoff = ((b * NH + h) * SK + sp * 2048) * (DH * 2), kx = (w & 3) * 8192 + bs * 65536;
  asm volatile("" : "+s"(kvoff), "+s"(kx));
  asm volatile("" :: "s"(p.x), "s"(p.k), "s"(p.v), "s"(p.wq), "s"(p.wo), "s"(p.w1), "s"(p.b1), "s"(p.w2), "s"(p.b2), "s"(p.out), "s"(p.ws));
  const unsigned rot = b * 32 + sp * 4;

  if (w >= 4) {
    const int a = w - 4, prod = 2 * h + sp;
    *(v4u*)(lds + L_M) = v4u{SENT, SENT, SENT, SENT}; *(v4u*)(lds + L_TAG) = v4u{0u, 0u, 0u, 0u};
    if (a != 0) WG_BARRIER();
    FENCE();
    const rsrc_t rws = mk_rsrc(p.ws, WS_BYTES), rx = mk_rsrc(p.x, NB * DM * 2);
    const unsigned xoff = ((lane >> 2) & 3) * (DM * 2) + ((lane & 3) + 4 * r) * 16;
    if (a == 0) {
      v4u xs[4]; v2u U[32];
#pragma unroll
      for (int t = 0; t < 4; ++t) xs[t] = ld128<0>(rx, xoff, ((t + b) & 3) * 256);
      FENCE();
      tile_load<0, 32>(U, mk_rsrc(p.wq + (size_t)b * 128 * (NH * DH) + h * DH + sp * 64, 128 * NH * DH * 2), r * 32 * 8192 + c * 8, 8192);
      const unsigned ep = __builtin_amdgcn_raw_buffer_load_b32(rws, 0, WS_E, SC1);
      FENCE();
      WG_BARRIER();
      if (DIAG == 3) { TOUCH(U[31]); FENCE(); tmark = __builtin_amdgcn_s_memrealtime(); FENCE(); }
      float mean, inv;
      ln_stats(xs, mean, inv);
      const f32x4 g = tile_mul(ln_apply(xs[0], mean, inv), U);
      FENCE();
      const unsigned tg = __builtin_amdgcn_readfirstlane(ep) + 1u;
      st128(tag4(g, tg), rws, WS_QX + h * 8192 + bs * 1024 + l16);
      asm volatile("s_waitcnt vmcnt(0)" ::: "memory");
      __builtin_amdgcn_s_sleep(16);
      f32x4 q4;
#pragma unroll 1
      for (unsigned spin = 0; spin < SPIN_CAP; ++spin) {
        const v4u G0 = ld128<SC1>(rws, r * 1024 + c * 16, WS_QX + h * 8192 + b * 256), G1 = ld128<SC1>(rws, r * 1024 + c * 16, WS_QX + h * 8192 + b * 256 + 4096);
        unsigned bad = 0;
        q4 = untag4(G0, tg & 0xfffu, bad); q4 += untag4(G1, tg & 0xfffu, bad);
        if (all_fresh(bad)) break;
        ORDER();
      }
      *(unsigned*)(lds + L_Q + (lane & 31) * 8 + (lane >> 5) * 4) = pk_bf16(tr32(q4[0], q4[2]), tr32(q4[1], q4[3]));
      *(unsigned*)(lds + L_TAG) = tg;
      ORDER();
      flag_set(L_RDY, 1u);
    }
#pragma unroll 1
    for (unsigned spin = 0; spin < LDS_CAP; ++spin) {
      if (__builtin_amdgcn_readfirstlane(flag_get(L_RDY))) break;
      __builtin_amdgcn_s_sleep(4);
    }
    ORDER();
    const unsigned tag = __builtin_amdgcn_readfirstlane(*(const unsigned*)(lds + L_TAG));

    if (a == 2 && h < 28) {
      const int cg = (h & 1) * 4 + b;
      const unsigned stride = sp ? DM * 2 : FF * 2;
      const rsrc_t rt = mk_rsrc(sp ? p.w2 + (size_t)(h >> 1) * 128 * DM + cg * 64 : p.w1 + (size_t)b * 128 * FF + h * 64, 128 * stride);
      v2u U[32]; v4u A;
      tile_load<0, 16>(U, rt, r * 32 * stride + c * 8, stride);
      TOUCH(U[15]); FENCE();
      tile_load<16, 32>(U, rt, r * 32 * stride + c * 8, stride);
      if (sp == 0) {
        v4u xs[4];
#pragma unroll
        for (int t = 0; t < 4; ++t) xs[t] = ld128<0>(rx, xoff, ((t + b) & 3) * 256);
#pragma unroll 1
        for (int it = 0; it < 2; ++it) {
          float mean, inv;
          ln_stats(xs, mean, inv);
#pragma unroll
          for (int t = 0; t < 4; ++t) xs[t] = ln_apply(xs[t], mean, inv);
        }
        A = xs[0];
      } else {
        v2u b1v[2];
#pragma unroll
        for (int k = 0; k < 2; ++k) b1v[k] = ld64<0>(mk_rsrc(p.b1 + (h >> 1) * 128, 256), c * 8, k * 128);
        f32x4 z4[2];
#pragma unroll 1
        for (unsigned spin = 0; spin < SPIN_CAP; ++spin) {
          __builtin_amdgcn_s_sleep(112); __builtin_amdgcn_s_sleep(112);
          v4u G[8];
#pragma unroll
          for (int j = 0; j < 8; ++j) G[j] = ld128<SC1>(rws, l16, WS_X1 + (h >> 1) * 8192 + j * 1024);
          unsigned bad = 0;
#pragma unroll
          for (int j = 0; j < 8; ++j) { const f32x4 v = untag4(G[j], tag & 0xfffu, bad); z4[j >> 2] = (j & 3) ? z4[j >> 2] + v : v; }
          if (all_fresh(bad)) break;
          ORDER();
        }
#pragma unroll
        for (int k = 0; k < 2; ++k) {
          float hv[4];
#pragma unroll
          for (int j = 0; j < 4; ++j) {
            const float z = rbf(rbf(z4[k][j]) + ((j & 1) ? bfhi(b1v[k][j >> 1]) : bflo(b1v[k][j >> 1])));
            hv[j] = z * rcp_(1.0f + exp2_((-2.0f * 0.7978845608028654f * LOG2E) * (z + 0.044715f * z * z * z)));
          }
          *(v2u*)(lds + L_H + r * 256 + k * 128 + c * 8) = v2u{pk_bf16(hv[0], hv[1]), pk_bf16(hv[2], hv[3])};
        }
        asm volatile("s_waitcnt lgkmcnt(0)" ::: "memory");
        A = *(const v4u*)(lds + L_H + ((lane >> 2) & 3) * 256 + ((lane & 3) + 4 * r) * 16);
      }
      st128(tag4(tile_mul(A, U), tag), rws, (sp ? WS_MP + (h >> 1) * 8192 + cg * 1024 : WS_X1 + h * 4096 + b * 1024) + l16);
    }

    const unsigned ck_st = (lane == 0 && (idx & 3) == 0) ? WS_CK + (bs * 8 + (blk & 7)) * 8 : OOB;
    unsigned t0 = 0;
    if (a == 3) {
#pragma unroll 1
      for (unsigned spin = 0; spin < LDS_CAP; ++spin) {
        t0 = __builtin_amdgcn_readfirstlane(flag_get(L_T0));
        if (t0) break;
        __builtin_amdgcn_s_sleep(4);
      }
      st64(v2u{t0, tag}, rws, ck_st);
    }

    v8u Bf[2][8];
    {
      const rsrc_t ro = mk_rsrc(p.wo + (size_t)h * DH * DM + a * 128, DH * DM * 2);
      const unsigned vo = r * 8 * 1024 + c * 16;
      v4u T[32]; v2u ck;
      static_for<32 + DO>([&](auto N) { constexpr int n = N;
        if constexpr (n >= DO) TOUCH(T[n - DO]);
        FENCE();
        if constexpr (n == 10) ck = ld64<SC1>(rws, a == 3 ? lane * 8 : OOB, WS_CK);
        if constexpr (n == 10 + DO) { TOUCH(ck);
          if (a == 3) {
            const unsigned tn = clock10ns();
            const float dt = wave_sum((float)(int)((ck[1] == tag ? ck[0] : tn) - t0)) * (1.0f / 64);
            flag_set(L_REF, (t0 + (int)dt + (128 + LD - NA) * P_TAUK / 16 - P_OFF0) | 1u); } }
        FENCE();
        if constexpr (n < 32) T[n] = ld128<0>(ro, vo, (((n >> 3) * 32 + (n & 7)) ^ rot) * 1024);
        FENCE();
        if constexpr (n >= DO && ((n - DO) & 7) == 7) { constexpr int s = (n - DO) >> 3;
          static_for<8>([&](auto J) { constexpr int j = J; const v4u f = bfrag<j>(T + 8 * s);
            if constexpr (s & 1) Bf[s >> 1][j].hi = f; else Bf[s >> 1][j].lo = f;
            PIN(Bf[s >> 1][j]); }); }
        FENCE(); });
    }

    const int cons = (blk & 7) * 8 + (idx & 7), col0 = 32 * (cons >> 2) + 4 * (cons & 1) + (cons & 2);
    const unsigned co = (lane & 1) * 32 + (r >> 1) * 16 + (r & 1) * 2;
    if (a == 3) {
      unsigned mb;
#pragma unroll 1
      for (unsigned spin = 0; spin < LDS_CAP; ++spin) {
        mb = flag_get(L_M + 4 * (lane & 3));
        if (__all(mb != SENT)) break;
        __builtin_amdgcn_s_sleep(4);
      }
      ORDER();
      const float m4 = __uint_as_float(mb);
      float M = fmaxf(m4, dpp<XOR1>(m4)); M = fmaxf(M, dpp<XOR2>(M));
      float L = 0.f;
#pragma unroll
      for (int i = 0; i < 8; ++i) { const f32x4 s4 = *(const f32x4*)(lds + L_S + i * 1024 + l16);
#pragma unroll
        for (int j = 0; j < 4; ++j) L += exp2_(s4[j] - M); }
      L = wave_sum(L);
      const float lse = M + __builtin_amdgcn_logf(L);
      const unsigned tk = flag_get(L_T);
      st128(v4u{__float_as_uint(lse), tag, tk, tag}, rws, lane == 0 ? WS_LS + (b * 64 + prod) * 16 : OOB);
      const unsigned long long need = blk == 0 ? ~0ull : 1ull << (prod ^ 1);
      float other; unsigned mean;
#pragma unroll 1
      for (unsigned spin = 0; spin < SPIN_CAP; ++spin) {
        __builtin_amdgcn_s_sleep(80);
        const v4u m = ld128<SC1>(rws, l16, WS_LS + b * 1024);
        const bool ok = ((m[1] ^ tag) | (m[3] ^ tag)) == 0u;
        const unsigned tn = clock10ns();
        const float dt = __builtin_amdgcn_fmed3f(wave_sum((float)(int)((ok ? m[2] : tn) - tk)) * (1.0f / 64), -1000.f, 1000.f);
        mean = __builtin_amdgcn_readfirstlane(tk + (int)dt);
        flag_set(L_REF, (mean - P_OFF1) | 1u);
        other = __uint_as_float(__builtin_amdgcn_readlane(m[0], prod ^ 1));
        if ((__ballot(ok) & need) == need) break;
        ORDER();
      }
      *(float*)(lds + L_F + 4 * (lane & 3)) = exp2_(m4 - M) * rcp_(L) * rcp_(1.0f + exp2_(other - lse));
      __builtin_amdgcn_raw_buffer_store_b32(tag, rws, (blk == 0 && lane == 0) ? WS_E : OOB, 0, SC1);
      while ((int)(clock10ns() - mean) < P_C1) __builtin_amdgcn_s_sleep(2);
      st64(v2u{flag_get(L_T), tag}, rws, ck_st + 512);
      __builtin_amdgcn_s_sleep(36);
      const v2u ck = ld64<SC1>(rws, lane * 8, WS_CK + 512);
      const bool ok = ck[1] == tag;
      const int dt = (int)(wave_sum(ok ? (float)(int)(ck[0] - mean) : 0.f) * rcp_(fmaxf(wave_sum(ok ? 1.f : 0.f), 1.f)));
      flag_set(L_REF, (mean + dt - P_OFF2) | 1u);
      flag_set(L_END_T, mean + dt + dt * P_EX / 4 + 8 * P_TAUV + P_TP);
    } else if (a == 2) {
      const unsigned b2v = __builtin_amdgcn_raw_buffer_load_b16(mk_rsrc(p.b2, DM * 2), co, col0 * 2, 0);
      const unsigned xv = __builtin_amdgcn_raw_buffer_load_b16(rx, co, (b * DM + col0) * 2, 0);
      const unsigned mo = lane < 28 ? (unsigned)((lane >> 1) * 8192 + (lane & 1) * 64) : OOB, ms = WS_MP + (col0 >> 6) * 1024 + b * 256 + (col0 & 63) * 4;
      f32x4 m4;
#pragma unroll 1
      for (unsigned spin = 0; spin < SPIN_CAP; ++spin) {
        __builtin_amdgcn_s_sleep(64);
        const v2u G0 = ld64<SC1>(rws, mo, ms), G1 = ld64<SC1>(rws, mo, ms + 32);
        unsigned bad = 0;
        m4 = untag4(v4u{G0[0], G0[1], G1[0], G1[1]}, lane < 28 ? tag & 0xfffu : 0u, bad);
        if (all_fresh(bad)) break;
        ORDER();
      }
      const float m = rbf(rbf(sum4_parity(m4)) + bflo(b2v));
      *(f32x2*)(lds + L_GX + (r * 2 + (lane & 1)) * 8) = f32x2{rbf(m * rcp_(1.0f + exp2_(-LOG2E * m))), bflo(xv)};
    }

    unsigned a_off = L_O + c * O_ROW + r * 16, sidx = (lane & 2) ? 0xeeeeu : 0x4444u;
    unsigned tb_off = WS_TB + (b * 64 + prod) * 2048 + a * 512 + (c >> 2) * 128 + r * 32 + (c & 3) * 8;
    unsigned go = (lane >> 1) * 2048 + (lane & 1) * 16, out_off = (lane & 14) == 0 ? (b * DM + col0) * 2 + co : OOB;
    f32x4 d[8];
#pragma unroll
    for (int j = 0; j < 8; ++j) { d[j] = f32x4{0.f, 0.f, 0.f, 0.f}; PIN(d[j]); }
    PIN(a_off); PIN(sidx); PIN(tb_off); PIN(go); PIN(out_off);
    asm volatile("s_branch 1f\n\t.p2align 11\n1:");
    WG_BARRIER();
    const v4u A0 = *(const v4u*)(lds + a_off), A1 = *(const v4u*)(lds + a_off + 64);
    const float f = *(const float*)(lds + L_F + r * 4);
    FENCE();
    float t[8];
    auto mul = [&](int j) { d[j] = smfma(A0, Bf[0][j], d[j], sidx); d[j + 4] = smfma(A0, Bf[0][j + 4], d[j + 4], sidx);
      d[j] = smfma(A1, Bf[1][j], d[j], sidx); d[j + 4] = smfma(A1, Bf[1][j + 4], d[j + 4], sidx); };
    auto sum = [&](int j) { const f32x2 u2 = d[j].lo + d[j].hi; t[j] = (u2[0] + u2[1]) * f; PIN(t[j]); };
    mul(0); mul(2); mul(1); mul(3);
    sum(0); sum(4); sum(2); sum(6);
    unsigned w0 = tag1(tr32(tr16(t[0], t[4]), tr16(t[2], t[6])), tag); PIN(w0);
    sum(1); sum(5);
    float x1 = tr16(t[1], t[5]); PIN(x1);
    sum(3); sum(7);
    const unsigned w1 = tag1(tr32(x1, tr16(t[3], t[7])), tag);
    __builtin_amdgcn_sched_group_barrier(0x8, 7, 0);
#pragma unroll
    for (int k = 0; k < 9; ++k) { __builtin_amdgcn_sched_group_barrier(0x2, 3, 0); __builtin_amdgcn_sched_group_barrier(0x8, 1, 0); }
    FENCE();
    st64(v2u{w0, w1}, rws, tb_off);
    if (a == 0) {
      const f32x2 gx = *(const f32x2*)(lds + L_GX + (r * 2 + (lane & 1)) * 8);
      const unsigned t1 = clock10ns();
      const int wait = (int)(__builtin_amdgcn_readfirstlane(flag_get(L_END_T)) - t1);
      __builtin_amdgcn_s_sleep(4);
      while ((int)(clock10ns() - t1) < (wait < 120 ? wait : 120)) {}
      f32x4 s4;
#pragma unroll 1
      for (unsigned spin = 0; spin < SPIN_CAP; ++spin) {
        const v4u G0 = ld128<SC1>(rws, go, WS_TB + b * 131072 + cons * 32), G1 = ld128<SC1>(rws, go, WS_TB + b * 131072 + 65536 + cons * 32);
        unsigned bad = 0;
        s4 = untag4(G0, tag & 0xfffu, bad); s4 += untag4(G1, tag & 0xfffu, bad);
        if (all_fresh(bad)) break;
        __builtin_amdgcn_s_sleep(1);
        ORDER();
      }
      const float o = gx[1] + rbf(rbf(sum4_parity(s4)) * gx[0]);
      __builtin_amdgcn_raw_buffer_store_b16((unsigned short)bf_bits(o), mk_rsrc(p.out, NB * DM * 2), out_off, 0, 0);
      if (blk == 0) { while (clock10ns() - (unsigned)tmark < 10000) __builtin_amdgcn_s_sleep(1); }
    }
    return;
  }

  constexpr unsigned KVLEN = 2048 * DH * 2;
  const char* kp = (const char*)p.k + kvoff; const char* vp = (const char*)p.v + kvoff;
  const rsrc_t rk = mk_rsrc(kp, KVLEN);
  auto kvload = [&](rsrc_t rs, unsigned o, int i) { return ld128<NT>(rs, l16 + (i & 3) * 1024, o ^ (((i >> 2) & 1) * 4096 + (i >> 3) * 32768)); };
  float* const scs = (float*)(lds + L_S + w * 2048) + lane;
  v4u S[NU + LD], q;
  unsigned now, refv = 0;
  auto pace = [&](int sched) {
    const unsigned rf = __builtin_amdgcn_readfirstlane(refv);
    int lv = ((int)(now - rf) - sched) >> 5;
    asm volatile("" : "+s"(lv));
    lv = rf ? lv : 3;
    unsigned m = 0xffffu; m = lv > 0 ? 0x5555u : m; m = lv > 1 ? 0x1111u : m; m = lv > 2 ? 0x1249u : m;
    m = lv > 3 ? 0x0421u : m; m = lv > 4 ? 0x0101u : m; m = lv > 5 ? 0u : m;
    return (unsigned)__builtin_amdgcn_readfirstlane(m); };
  float ka[8], mw = -INFINITY;
  auto kdot = [&](v4u t, int i) {
    float d = dot2(t[0], q[0], 0.f); d = dot2(t[1], q[1], d); d = dot2(t[2], q[2], d); d = dot2(t[3], q[3], d);
    if (i >= 8) { i -= 8; d = trd<XOR8>(ka[i], d, lane & 8);
      if (i >= 4) { i -= 4; d = trd<XOR7>(ka[i], d, lane & 4);
        if (i >= 2) { i -= 2; d = trd<XOR2>(ka[i], d, lane & 2);
          if (i >= 1) { i -= 1; d = trd<XOR1>(ka[i], d, lane & 1); } } } }
    PIN(d); ka[i] = d; };
  auto kdone = [&](int g) { const float s = ka[0] * C2; scs[g * 64] = s; mw = fmaxf(mw, s); PIN(mw); };

  static_for<NU + LD>([&](auto N) {
    constexpr int n = N, dn = n < N0 ? D0 : D1;
    if constexpr (n >= dn) TOUCH(S[n - dn]);
    FENCE();
    S[n] = kvload(rk, kx, n);
    if constexpr (n == 0) __builtin_amdgcn_s_setprio(2);
    if constexpr (n == NS0) { FENCE(); WG_BARRIER(); }
    FENCE();
    if constexpr (n == NA - 1) {
#pragma unroll 1
      for (unsigned spin = 0; spin < LDS_CAP; ++spin) {
        const unsigned rdy = flag_get(L_RDY);
        ORDER();
        q = *(const v4u*)(lds + L_Q + c * 16);
        if (__builtin_amdgcn_readfirstlane(rdy)) break;
        __builtin_amdgcn_s_sleep(1);
      }
      now = clock10ns();
    }
    if constexpr (n == NA) flag_set(L_T0, now | 1u);
    if constexpr (n >= NA) {
      constexpr int hi = (n - NA + 1) * 4 < n - LD + 1 ? (n - NA + 1) * 4 : n - LD + 1;
      constexpr int lo = (n - NA) * 4 < n - LD ? (n - NA) * 4 : n - LD;
      static_for<hi - lo>([&](auto J) { constexpr int i = lo + J; kdot(S[i], i & 15); if constexpr ((i & 15) == 15) kdone(i >> 4); });
    }
    FENCE();
  });
  v4u R[16];
#pragma unroll
  for (int i = 0; i < LD; ++i) R[i] = S[NU + i];
  float snext = scs[0];
  v4u sel;
#pragma unroll
  for (int j = 0; j < 4; ++j) { const unsigned pick = (c & 8) ? 0x0302u : 0x0100u;
    sel[j] = ((c >> 1) & 3) != j ? 0x0c0c0c0cu : (c & 1) ? (pick << 16) | 0x0c0cu : 0x0c0c0000u | pick; }
  unsigned o_off = L_O + (4 * w + (r & 2)) * O_ROW + ((c * 8 + (r & 1) * 4) ^ rot);
  PIN(o_off); PIN(sel);
#pragma unroll 1
  for (int g = NU / 16; g < 8; ++g) {
    const rsrc_t rn = mk_rsrc(g == 7 ? vp : kp, KVLEN);
    const unsigned o0 = (g * 65536) ^ kx, o1 = (((g + 1) & 7) * 65536) ^ kx;
    const unsigned mask = pace((g - 8) * P_TAUK);
    static_for<16>([&](auto I) { constexpr int i = I;
      TOUCH(R[i]); THROTTLE(mask, i); FENCE();
      R[(i + LD) & 15] = i + LD < 16 ? kvload(rk, o0, i + LD) : kvload(rn, o1, i + LD - 16);
      if constexpr (i == 15) { now = clock10ns(); refv = flag_get(L_REF); }
      FENCE();
      kdot(R[i], i);
      FENCE(); });
    kdone(g);
  }
  mw = wave_max(mw);
  flag_set(L_T + 4 * w, now);
  ORDER();
  flag_set(L_M + 4 * w, min(__float_as_uint(mw), SENT - 1u));
  f32x4 acc = {0.f, 0.f, 0.f, 0.f};
  const rsrc_t rv = mk_rsrc(vp, KVLEN);
#pragma unroll 1
  for (int g = 0; g < 8; ++g) {
    const rsrc_t rn = mk_rsrc(vp, g == 7 ? 0u : KVLEN);
    const unsigned o0 = (g * 65536) ^ kx, o1 = (((g + 1) & 7) * 65536) ^ kx;
    const float pg = exp2_(snext - mw), ph = rbf(pg);
    const unsigned pp = pk_bf16(ph, pg - ph);
    snext = scs[((g + 1) & 7) * 64];
    flag_set(L_T + 4 * w, now - g * P_TAUV);
    const unsigned mask = pace(g * P_TAUV) & (g == 7 ? 0xffu : 0xffffu);
    static_for<16>([&](auto I) { constexpr int i = I;
      const unsigned pb = __builtin_amdgcn_mov_dpp(pp, BCAST + i, 0xf, 0xf, true);
      v4u A;
#pragma unroll
      for (int j = 0; j < 4; ++j) A[j] = __builtin_amdgcn_perm(pb, pb, sel[j]);
      PIN(A);
      FENCE();
      TOUCH(R[i]); THROTTLE(mask, i); FENCE();
      R[(i + LD) & 15] = i + LD < 16 ? kvload(rv, o0, i + LD) : kvload(rn, o1, i + LD - 16);
      if constexpr (i == 15) { now = clock10ns(); refv = flag_get(L_REF); }
      FENCE();
      acc = mfma(A, R[i], acc);
      FENCE(); });
    const float e0 = tr32(acc[0], acc[2]), e1 = tr32(acc[1], acc[3]);
    const unsigned hi = pk_bf16(e0, e1), lo = pk_bf16(e0 - bflo(hi), e1 - bfhi(hi));
    *(unsigned*)(lds + o_off) = hi;
    *(unsigned*)(lds + o_off + O_ROW) = lo;
    ORDER();
  }
  WG_BARRIER();
}

extern "C" void kernel_launch(void* const* d_in, const int* in_sizes, int n_in, void* d_out, int out_size, void* d_ws, size_t ws_size,
                              hipStream_t stream) {
  Params p;
  p.x = (const unsigned short*)d_in[0]; p.k = (const unsigned short*)d_in[1]; p.v = (const unsigned short*)d_in[2];
  p.wq = (const unsigned short*)d_in[3]; p.wo = (const unsigned short*)d_in[4];
  p.w1 = (const unsigned short*)d_in[6]; p.b1 = (const unsigned short*)d_in[7]; p.w2 = (const unsigned short*)d_in[8]; p.b2 = (const unsigned short*)d_in[9];
  p.out = (unsigned short*)d_out;
  p.ws = (unsigned char*)d_ws;
  k_fused<<<256, 512, 0, stream>>>(p);
}
```

```cpp
#include <hip/hip_runtime.h>
#include <hip/hip_bf16.h>
#include <stdint.h>
#include <utility>

typedef unsigned v2u __attribute__((ext_vector_type(2)));
typedef unsigned v4u __attribute__((ext_vector_type(4)));
typedef unsigned v8u __attribute__((ext_vector_type(8)));
typedef float f32x2 __attribute__((ext_vector_type(2)));
typedef float f32x4 __attribute__((ext_vector_type(4)));
typedef __bf16 bf16x2 __attribute__((ext_vector_type(2)));
typedef __bf16 bf16x8 __attribute__((ext_vector_type(8)));
typedef __bf16 bf16x16 __attribute__((ext_vector_type(16)));
typedef __amdgpu_buffer_rsrc_t rsrc_t;
typedef __attribute__((address_space(3))) unsigned lds_u32;

constexpr int NB = 4, DM = 512, NH = 32, SK = 4096, DH = 128, FF = 1792;
constexpr int NT = 2, SC1 = 16;
constexpr unsigned SENT = 0xFFFFFFFFu, OOB = 0x80000000u;
constexpr unsigned CAP_FAST = 1u << 19, CAP_SLOW = 1u << 16, CAP_LDS = 1u << 22;
constexpr float LOG2E = 1.4426950408889634f, C2 = 0.088388347648318440f * LOG2E;

constexpr int D0 = 7, N0 = 10, D1 = 6;
constexpr int NS0 = D0 + 7;
constexpr int NA = 40;
constexpr int NU = 48;
constexpr int LD = 8;
constexpr int DO = 3;
static_assert((NU + LD - NA) * 4 >= NU, "the backlog is gone before the loop starts");
constexpr int TAU_K = 228, TAU_V = 215;
constexpr int LEAD0 = 70, LEAD1 = 120, LEAD2 = 135;
constexpr int T_LAST = 1100;
constexpr int T_LOOK = 82;

template <class F, int... I> __device__ __forceinline__ void static_for_impl(F&& f, std::integer_sequence<int, I...>) { (f(std::integral_constant<int, I>{}), ...); }
template <int N, class F> __device__ __forceinline__ void static_for(F&& f) { static_for_impl(f, std::make_integer_sequence<int, N>{}); }

#define TOUCH(x) asm volatile("" :: "v"(x))
#define PIN(x) asm volatile("" : "+v"(x))
#define ORDER() asm volatile("" ::: "memory")
#define FENCE() __builtin_amdgcn_sched_barrier(0)
#define WG_BARRIER() asm volatile("s_waitcnt lgkmcnt(0)\n\ts_barrier" ::: "memory")
#define THROTTLE(mask, i) asm volatile("s_bitcmp1_b32 %0, %1\n\ts_cbranch_scc0 1f\n\ts_waitcnt vmcnt(3)\n1:" :: "s"(mask), "n"(i) : "scc")

__device__ __forceinline__ unsigned clock10ns() { return (unsigned)__builtin_amdgcn_s_memrealtime(); }
__device__ __forceinline__ float dot2(unsigned a, unsigned b, float acc) {
  return __builtin_amdgcn_fdot2_f32_bf16(__builtin_bit_cast(bf16x2, a), __builtin_bit_cast(bf16x2, b), acc, false);
}
__device__ __forceinline__ f32x4 mfma(v4u a, v4u b, f32x4 c) {
  return __builtin_amdgcn_mfma_f32_16x16x32_bf16(__builtin_bit_cast(bf16x8, a), __builtin_bit_cast(bf16x8, b), c, 0, 0, 0);
}
__device__ __forceinline__ f32x4 smfma(v4u a, v8u b, f32x4 c, unsigned idx) {
  return __builtin_amdgcn_smfmac_f32_16x16x64_bf16(__builtin_bit_cast(bf16x8, a), __builtin_bit_cast(bf16x16, b), c, (int)idx, 0, 0);
}
__device__ __forceinline__ float bflo(unsigned u) { return __uint_as_float(u << 16); }
__device__ __forceinline__ float bfhi(unsigned u) { return __uint_as_float(u & 0xffff0000u); }
__device__ __forceinline__ unsigned pk_bf16(float lo, float hi) { bf16x2 r; r.x = (__bf16)lo; r.y = (__bf16)hi; return __builtin_bit_cast(unsigned, r); }
__device__ __forceinline__ unsigned bf_bits(float x) { return (unsigned)__builtin_bit_cast(unsigned short, (__bf16)x); }
__device__ __forceinline__ float rbf(float x) { return bflo(bf_bits(x)); }
__device__ __forceinline__ float exp2_(float x) { return __builtin_amdgcn_exp2f(x); }
__device__ __forceinline__ float rcp_(float x) { return __builtin_amdgcn_rcpf(x); }

template <int CTRL> __device__ __forceinline__ float dpp(float x) { return __int_as_float(__builtin_amdgcn_mov_dpp(__float_as_int(x), CTRL, 0xf, 0xf, true)); }
constexpr int XOR1 = 0xB1, XOR2 = 0x4E, XOR7 = 0x141, XOR8 = 0x128, ROR4 = 0x124, BCAST = 0x150;
__device__ __forceinline__ float tr32(float a, float b) {
  auto s = __builtin_amdgcn_permlane32_swap(__float_as_uint(a), __float_as_uint(b), false, false);
  return __uint_as_float(s[0]) + __uint_as_float(s[1]);
}
__device__ __forceinline__ float tr16(float a, float b) {
  auto s = __builtin_amdgcn_permlane16_swap(__float_as_uint(a), __float_as_uint(b), false, false);
  return __uint_as_float(s[0]) + __uint_as_float(s[1]);
}
template <int CTRL> __device__ __forceinline__ float trd(float lo, float hi, bool up) {
  const float t1 = lo + dpp<CTRL>(lo), t2 = hi + dpp<CTRL>(hi);
  return up ? t2 : t1;
}
__device__ __forceinline__ float wave_sum(float x) {
  x += dpp<XOR1>(x); x += dpp<XOR2>(x); x += dpp<XOR7>(x); x += dpp<XOR8>(x);
  x = tr16(x, x);
  return tr32(x, x);
}
__device__ __forceinline__ float wave_max(float x) {
  x = fmaxf(x, dpp<XOR1>(x)); x = fmaxf(x, dpp<XOR2>(x)); x = fmaxf(x, dpp<XOR7>(x)); x = fmaxf(x, dpp<XOR8>(x));
  auto s = __builtin_amdgcn_permlane16_swap(__float_as_uint(x), __float_as_uint(x), false, false);
  x = fmaxf(__uint_as_float(s[0]), __uint_as_float(s[1]));
  auto t = __builtin_amdgcn_permlane32_swap(__float_as_uint(x), __float_as_uint(x), false, false);
  return fmaxf(__uint_as_float(t[0]), __uint_as_float(t[1]));
}
__device__ __forceinline__ float sum4_parity(f32x4 v) {
  float s = tr16(tr32(v[0], v[2]), tr32(v[1], v[3]));
  s += dpp<XOR8>(s); s += dpp<ROR4>(s); s += dpp<XOR2>(s);
  return s;
}

__device__ __forceinline__ rsrc_t mk_rsrc(const void* p, unsigned bytes) { return __builtin_amdgcn_make_buffer_rsrc(const_cast<void*>(p), 0, bytes, 0x00020000); }
template <int AUX> __device__ __forceinline__ v4u ld128(rsrc_t r, unsigned v, unsigned s) { return __builtin_amdgcn_raw_buffer_load_b128(r, v, s, AUX); }
template <int AUX> __device__ __forceinline__ v2u ld64(rsrc_t r, unsigned v, unsigned s) { return __builtin_amdgcn_raw_buffer_load_b64(r, v, s, AUX); }
__device__ __forceinline__ void st128(v4u d, rsrc_t r, unsigned off) { __builtin_amdgcn_raw_buffer_store_b128(d, r, off, 0, SC1); }
__device__ __forceinline__ void st64(v2u d, rsrc_t r, unsigned off) { __builtin_amdgcn_raw_buffer_store_b64(d, r, off, 0, SC1); }
__device__ __forceinline__ unsigned tag1(float v, unsigned tag) { return ((__float_as_uint(v) + 0x800u) & 0xfffff000u) | (tag & 0xfffu); }
__device__ __forceinline__ v4u tag4(f32x4 v, unsigned tag) { return v4u{tag1(v[0], tag), tag1(v[1], tag), tag1(v[2], tag), tag1(v[3], tag)}; }
__device__ __forceinline__ f32x4 untag4(v4u g, unsigned tag12, unsigned& bad) {
  g ^= tag12;
  bad |= (g[0] | g[1]) | (g[2] | g[3]);
  return __builtin_bit_cast(f32x4, g);
}
__device__ __forceinline__ bool all_fresh(unsigned bad) { return !__any((bad & 0xfffu) != 0u); }

__device__ __forceinline__ float row_sum(float x) { x += dpp<XOR1>(x); x += dpp<XOR2>(x); x = tr16(x, x); return tr32(x, x); }
__device__ __forceinline__ void ln_stats(const v4u (&x)[4], float& mean, float& inv) {
  float s = 0.f;
#pragma unroll
  for (int t = 0; t < 4; ++t)
#pragma unroll
    for (int j = 0; j < 4; ++j) s += bflo(x[t][j]) + bfhi(x[t][j]);
  const float mu = row_sum(s) * (1.0f / DM);
  float q = 0.f;
#pragma unroll
  for (int t = 0; t < 4; ++t)
#pragma unroll
    for (int j = 0; j < 4; ++j) { const float d0 = bflo(x[t][j]) - mu, d1 = bfhi(x[t][j]) - mu; q = fmaf(d0, d0, q); q = fmaf(d1, d1, q); }
  const float var = row_sum(q) * (1.0f / DM);
  mean = rbf(mu);
  inv = rcp_(rbf(__builtin_amdgcn_sqrtf(rbf(rbf(var) + 1e-5f))));
}
__device__ __forceinline__ v4u ln_apply(v4u x, float mean, float inv) {
  v4u o;
#pragma unroll
  for (int j = 0; j < 4; ++j) { const unsigned d = pk_bf16(bflo(x[j]) - mean, bfhi(x[j]) - mean); o[j] = pk_bf16(bflo(d) * inv, bfhi(d) * inv); }
  return o;
}

template <int J, class T> __device__ __forceinline__ v4u bfrag(const T* t) {
  v4u f;
#pragma unroll
  for (int d = 0; d < 4; ++d) f[d] = __builtin_amdgcn_perm(t[2 * d + 1][J >> 1], t[2 * d][J >> 1], (J & 1) ? 0x07060302u : 0x05040100u);
  return f;
}
template <int LO, int HI> __device__ __forceinline__ void tile_load(v2u (&u)[32], rsrc_t r, unsigned voff, unsigned stride) {
#pragma unroll
  for (int n = LO; n < HI; ++n) u[n] = ld64<NT>(r, voff, n * stride);
  FENCE();
}
__device__ __forceinline__ f32x4 tile_mul(v4u a, const v2u (&u)[32]) {
  f32x4 o = {0.f, 0.f, 0.f, 0.f};
  static_for<16>([&](auto N) { constexpr int s = N >> 2, j = N & 3; o[j] += mfma(a, bfrag<j>(u + 8 * s), f32x4{0.f, 0.f, 0.f, 0.f})[s]; });
  return o;
}

constexpr unsigned WS_E = 0;
constexpr unsigned WS_LS = 4096;
constexpr unsigned WS_CK = 32768;
constexpr unsigned WS_X1 = 65536;
constexpr unsigned WS_MP = WS_X1 + 32 * 4096;
constexpr unsigned WS_TB = WS_MP + 16 * 8192;
constexpr unsigned WS_QX = WS_TB + 4 * 64 * 2048;
constexpr unsigned WS_BYTES = WS_QX + 32 * 8192;
constexpr int L_Q = 0, L_M = 256, L_F = 272, L_TAG = 288, L_REF = 292, L_RDY = 296, L_T0 = 300, L_T = 304, L_GX = 320,
              L_H = 1024, L_O = 2048, O_ROW = 144, L_S = L_O + 16 * O_ROW, L_BYTES = L_S + 8192;

struct Params {
  const unsigned short* x; const unsigned short* k; const unsigned short* v; const unsigned short* wq; const unsigned short* wo;
  const unsigned short* w1; const unsigned short* b1; const unsigned short* w2; const unsigned short* b2;
  unsigned short* out; unsigned char* ws;
};

__global__ void __launch_bounds__(512, 2) k_fused(Params p) {
  __shared__ __attribute__((aligned(16))) unsigned char lds[L_BYTES];
  auto flag_get = [&](int off) { return __hip_atomic_load((lds_u32*)lds + off / 4, __ATOMIC_RELAXED, __HIP_MEMORY_SCOPE_WORKGROUP); };
  auto flag_set = [&](int off, unsigned v) { __hip_atomic_store((lds_u32*)lds + off / 4, v, __ATOMIC_RELAXED, __HIP_MEMORY_SCOPE_WORKGROUP); };
  const int blk = blockIdx.x, lane = threadIdx.x & 63, w = __builtin_amdgcn_readfirstlane(threadIdx.x >> 6);
  const int idx = blk >> 3, h = (blk & 7) * 4 + (idx & 3), bs = idx >> 2, b = bs >> 1, sp = bs & 1;
  const int r = lane >> 4, c = lane & 15;
  const unsigned l16 = lane * 16;
  unsigned kvoff = ((b * NH + h) * SK + sp * 2048) * (DH * 2), kx = (w & 3) * 8192 + bs * 65536;
  asm volatile("" : "+s"(kvoff), "+s"(kx));
  asm volatile("" :: "s"(p.x), "s"(p.k), "s"(p.v), "s"(p.wq), "s"(p.wo), "s"(p.w1), "s"(p.b1), "s"(p.w2), "s"(p.b2), "s"(p.out), "s"(p.ws));
  const unsigned rot = b * 32 + sp * 4;

  if (w >= 4) {
    const int a = w - 4, prod = 2 * h + sp;
    *(v4u*)(lds + L_M) = v4u{SENT, SENT, SENT, SENT}; *(v4u*)(lds + L_TAG) = v4u{0u, 0u, 0u, 0u};
    if (a != 0) WG_BARRIER();
    FENCE();
    const rsrc_t rws = mk_rsrc(p.ws, WS_BYTES), rx = mk_rsrc(p.x, NB * DM * 2);
    const unsigned xoff = ((lane >> 2) & 3) * (DM * 2) + ((lane & 3) + 4 * r) * 16;
    if (a == 0) {
      v4u xs[4]; v2u U[32];
#pragma unroll
      for (int t = 0; t < 4; ++t) xs[t] = ld128<0>(rx, xoff, ((t + b) & 3) * 256);
      FENCE();
      tile_load<0, 32>(U, mk_rsrc(p.wq + (size_t)b * 128 * (NH * DH) + h * DH + sp * 64, 128 * NH * DH * 2), r * 32 * 8192 + c * 8, 8192);
      const unsigned ep = __builtin_amdgcn_raw_buffer_load_b32(rws, 0, WS_E, SC1);
      FENCE();
      WG_BARRIER();
      float mean, inv;
      ln_stats(xs, mean, inv);
      const f32x4 g = tile_mul(ln_apply(xs[0], mean, inv), U);
      FENCE();
      const unsigned tg = __builtin_amdgcn_readfirstlane(ep) * 0x9E3779B1u + 0x7F4A7C15u;
      st128(tag4(g, tg), rws, WS_QX + h * 8192 + bs * 1024 + l16);
      asm volatile("s_waitcnt vmcnt(0)" ::: "memory");
      __builtin_amdgcn_s_sleep(16);
      f32x4 q4;
#pragma unroll 1
      for (unsigned spin = 0; spin < CAP_FAST; ++spin) {
        const v4u G0 = ld128<SC1>(rws, r * 1024 + c * 16, WS_QX + h * 8192 + b * 256), G1 = ld128<SC1>(rws, r * 1024 + c * 16, WS_QX + h * 8192 + b * 256 + 4096);
        unsigned bad = 0;
        q4 = untag4(G0, tg & 0xfffu, bad); q4 += untag4(G1, tg & 0xfffu, bad);
        if (all_fresh(bad)) break;
        ORDER();
      }
      *(unsigned*)(lds + L_Q + (lane & 31) * 8 + (lane >> 5) * 4) = pk_bf16(tr32(q4[0], q4[2]), tr32(q4[1], q4[3]));
      *(unsigned*)(lds + L_TAG) = tg;
      ORDER();
      flag_set(L_RDY, 1u);
    }
#pragma unroll 1
    for (unsigned spin = 0; spin < CAP_LDS; ++spin) {
      if (__builtin_amdgcn_readfirstlane(flag_get(L_RDY))) break;
      __builtin_amdgcn_s_sleep(4);
    }
    ORDER();
    const unsigned tag = __builtin_amdgcn_readfirstlane(*(const unsigned*)(lds + L_TAG));

    if (a == 2 && h < 28) {
      const int cg = (h & 1) * 4 + b;
      const unsigned stride = sp ? DM * 2 : FF * 2;
      const rsrc_t rt = mk_rsrc(sp ? p.w2 + (size_t)(h >> 1) * 128 * DM + cg * 64 : p.w1 + (size_t)b * 128 * FF + h * 64, 128 * stride);
      v2u U[32]; v4u A;
      tile_load<0, 16>(U, rt, r * 32 * stride + c * 8, stride);
      TOUCH(U[15]); FENCE();
      tile_load<16, 32>(U, rt, r * 32 * stride + c * 8, stride);
      if (sp == 0) {
        v4u xs[4];
#pragma unroll
        for (int t = 0; t < 4; ++t) xs[t] = ld128<0>(rx, xoff, ((t + b) & 3) * 256);
#pragma unroll 1
        for (int it = 0; it < 2; ++it) {
          float mean, inv;
          ln_stats(xs, mean, inv);
#pragma unroll
          for (int t = 0; t < 4; ++t) xs[t] = ln_apply(xs[t], mean, inv);
        }
        A = xs[0];
      } else {
        v2u b1v[2];
#pragma unroll
        for (int k = 0; k < 2; ++k) b1v[k] = ld64<0>(mk_rsrc(p.b1 + (h >> 1) * 128, 256), c * 8, k * 128);
        f32x4 z4[2];
#pragma unroll 1
        for (unsigned spin = 0; spin < CAP_SLOW; ++spin) {
          __builtin_amdgcn_s_sleep(112); __builtin_amdgcn_s_sleep(112);
          v4u G[8];
#pragma unroll
          for (int j = 0; j < 8; ++j) G[j] = ld128<SC1>(rws, l16, WS_X1 + (h >> 1) * 8192 + j * 1024);
          unsigned bad = 0;
#pragma unroll
          for (int j = 0; j < 8; ++j) { const f32x4 v = untag4(G[j], tag & 0xfffu, bad); z4[j >> 2] = (j & 3) ? z4[j >> 2] + v : v; }
          if (all_fresh(bad)) break;
          ORDER();
        }
#pragma unroll
        for (int k = 0; k < 2; ++k) {
          float hv[4];
#pragma unroll
          for (int j = 0; j < 4; ++j) {
            const float z = rbf(rbf(z4[k][j]) + ((j & 1) ? bfhi(b1v[k][j >> 1]) : bflo(b1v[k][j >> 1])));
            hv[j] = z * rcp_(1.0f + exp2_((-2.0f * 0.7978845608028654f * LOG2E) * (z + 0.044715f * z * z * z)));
          }
          *(v2u*)(lds + L_H + r * 256 + k * 128 + c * 8) = v2u{pk_bf16(hv[0], hv[1]), pk_bf16(hv[2], hv[3])};
        }
        asm volatile("s_waitcnt lgkmcnt(0)" ::: "memory");
        A = *(const v4u*)(lds + L_H + ((lane >> 2) & 3) * 256 + ((lane & 3) + 4 * r) * 16);
      }
      st128(tag4(tile_mul(A, U), tag), rws, (sp ? WS_MP + (h >> 1) * 8192 + cg * 1024 : WS_X1 + h * 4096 + b * 1024) + l16);
    }

    const unsigned ck_st = (lane == 0 && (idx & 3) == 0) ? WS_CK + (bs * 8 + (blk & 7)) * 8 : OOB;
    unsigned t0 = 0;
    if (a == 3) {
#pragma unroll 1
      for (unsigned spin = 0; spin < CAP_LDS; ++spin) {
        t0 = __builtin_amdgcn_readfirstlane(flag_get(L_T0));
        if (t0) break;
        __builtin_amdgcn_s_sleep(4);
      }
      st64(v2u{t0, tag}, rws, ck_st);
    }

    v8u Bf[2][8];
    {
      const rsrc_t ro = mk_rsrc(p.wo + (size_t)h * DH * DM + a * 128, DH * DM * 2);
      const unsigned vo = r * 8 * 1024 + c * 16;
      v4u T[32]; v2u ck;
      static_for<32 + DO>([&](auto N) { constexpr int n = N;
        if constexpr (n >= DO) TOUCH(T[n - DO]);
        FENCE();
        if constexpr (n == 10) ck = ld64<SC1>(rws, a == 3 ? lane * 8 : OOB, WS_CK);
        if constexpr (n == 10 + DO) { TOUCH(ck);
          if (a == 3) {
            const unsigned tn = clock10ns();
            const float dt = wave_sum((float)(int)((ck[1] == tag ? ck[0] : tn) - t0)) * (1.0f / 64);
            flag_set(L_REF, (t0 + (int)dt + (128 + LD - NA) * TAU_K / 16 - LEAD0) | 1u); } }
        FENCE();
        if constexpr (n < 32) T[n] = ld128<0>(ro, vo, (((n >> 3) * 32 + (n & 7)) ^ rot) * 1024);
        FENCE();
        if constexpr (n >= DO && ((n - DO) & 7) == 7) { constexpr int s = (n - DO) >> 3;
          static_for<8>([&](auto J) { constexpr int j = J; const v4u f = bfrag<j>(T + 8 * s);
            if constexpr (s & 1) Bf[s >> 1][j].hi = f; else Bf[s >> 1][j].lo = f;
            PIN(Bf[s >> 1][j]); }); }
        FENCE(); });
    }

    const int cons = (blk & 7) * 8 + (idx & 7), col0 = 32 * (cons >> 2) + 4 * (cons & 1) + (cons & 2);
    const unsigned co = (lane & 1) * 32 + (r >> 1) * 16 + (r & 1) * 2;
    if (a == 3) {
      unsigned mb;
#pragma unroll 1
      for (unsigned spin = 0; spin < CAP_LDS; ++spin) {
        mb = flag_get(L_M + 4 * (lane & 3));
        if (__all(mb != SENT)) break;
        __builtin_amdgcn_s_sleep(4);
      }
      ORDER();
      const float m4 = __uint_as_float(mb);
      float M = fmaxf(m4, dpp<XOR1>(m4)); M = fmaxf(M, dpp<XOR2>(M));
      float L = 0.f;
#pragma unroll
      for (int i = 0; i < 8; ++i) { const f32x4 s4 = *(const f32x4*)(lds + L_S + i * 1024 + l16);
#pragma unroll
        for (int j = 0; j < 4; ++j) L += exp2_(s4[j] - M); }
      L = wave_sum(L);
      const float lse = M + __builtin_amdgcn_logf(L);
      const unsigned tk = flag_get(L_T);
      st128(v4u{__float_as_uint(lse), tag, tk, tag}, rws, lane == 0 ? WS_LS + (b * 64 + prod) * 16 : OOB);
      const unsigned long long need = blk == 0 ? ~0ull : 1ull << (prod ^ 1);
      float other; unsigned mean;
#pragma unroll 1
      for (unsigned spin = 0; spin < CAP_SLOW; ++spin) {
        __builtin_amdgcn_s_sleep(80);
        const v4u m = ld128<SC1>(rws, l16, WS_LS + b * 1024);
        const bool ok = ((m[1] ^ tag) | (m[3] ^ tag)) == 0u;
        const unsigned tn = clock10ns();
        const float dt = __builtin_amdgcn_fmed3f(wave_sum((float)(int)((ok ? m[2] : tn) - tk)) * (1.0f / 64), -1000.f, 1000.f);
        mean = __builtin_amdgcn_readfirstlane(tk + (int)dt);
        flag_set(L_REF, (mean - LEAD1) | 1u);
        other = __uint_as_float(__builtin_amdgcn_readlane(m[0], prod ^ 1));
        if ((__ballot(ok) & need) == need) break;
        ORDER();
      }
      *(float*)(lds + L_F + 4 * (lane & 3)) = exp2_(m4 - M) * rcp_(L) * rcp_(1.0f + exp2_(other - lse));
      __builtin_amdgcn_raw_buffer_store_b32(tag, rws, (blk == 0 && lane == 0) ? WS_E : OOB, 0, SC1);
      while ((int)(clock10ns() - mean) < T_LAST) __builtin_amdgcn_s_sleep(2);
      st64(v2u{flag_get(L_T), tag}, rws, ck_st + 512);
      __builtin_amdgcn_s_sleep(36);
      const v2u ck = ld64<SC1>(rws, lane * 8, WS_CK + 512);
      const bool ok = ck[1] == tag;
      const int dt = (int)(wave_sum(ok ? (float)(int)(ck[0] - mean) : 0.f) * rcp_(fmaxf(wave_sum(ok ? 1.f : 0.f), 1.f)));
      flag_set(L_REF, (mean + dt - LEAD2) | 1u);
    } else if (a == 2) {
      const unsigned b2v = __builtin_amdgcn_raw_buffer_load_b16(mk_rsrc(p.b2, DM * 2), co, col0 * 2, 0);
      const unsigned xv = __builtin_amdgcn_raw_buffer_load_b16(rx, co, (b * DM + col0) * 2, 0);
      const unsigned mo = lane < 28 ? (unsigned)((lane >> 1) * 8192 + (lane & 1) * 64) : OOB, ms = WS_MP + (col0 >> 6) * 1024 + b * 256 + (col0 & 63) * 4;
      f32x4 m4;
#pragma unroll 1
      for (unsigned spin = 0; spin < CAP_SLOW; ++spin) {
        __builtin_amdgcn_s_sleep(64);
        const v2u G0 = ld64<SC1>(rws, mo, ms), G1 = ld64<SC1>(rws, mo, ms + 32);
        unsigned bad = 0;
        m4 = untag4(v4u{G0[0], G0[1], G1[0], G1[1]}, lane < 28 ? tag & 0xfffu : 0u, bad);
        if (all_fresh(bad)) break;
        ORDER();
      }
      const float m = rbf(rbf(sum4_parity(m4)) + bflo(b2v));
      *(f32x2*)(lds + L_GX + (r * 2 + (lane & 1)) * 8) = f32x2{rbf(m * rcp_(1.0f + exp2_(-LOG2E * m))), bflo(xv)};
    }

    unsigned a_off = L_O + c * O_ROW + r * 16, sidx = (lane & 2) ? 0xeeeeu : 0x4444u;
    unsigned tb_off = WS_TB + (b * 64 + prod) * 2048 + a * 512 + (c >> 2) * 128 + r * 32 + (c & 3) * 8;
    unsigned go = (lane >> 1) * 2048 + (lane & 1) * 16, out_off = (lane & 14) == 0 ? (b * DM + col0) * 2 + co : OOB;
    f32x4 d[8];
#pragma unroll
    for (int j = 0; j < 8; ++j) { d[j] = f32x4{0.f, 0.f, 0.f, 0.f}; PIN(d[j]); }
    PIN(a_off); PIN(sidx); PIN(tb_off); PIN(go); PIN(out_off);
    asm volatile("s_branch 1f\n\t.p2align 11\n1:");
    WG_BARRIER();
    const v4u A0 = *(const v4u*)(lds + a_off), A1 = *(const v4u*)(lds + a_off + 64);
    const float f = *(const float*)(lds + L_F + r * 4);
    FENCE();
    float t[8];
    auto mul = [&](int j) { d[j] = smfma(A0, Bf[0][j], d[j], sidx); d[j + 4] = smfma(A0, Bf[0][j + 4], d[j + 4], sidx);
      d[j] = smfma(A1, Bf[1][j], d[j], sidx); d[j + 4] = smfma(A1, Bf[1][j + 4], d[j + 4], sidx); };
    auto sum = [&](int j) { const f32x2 u2 = d[j].lo + d[j].hi; t[j] = (u2[0] + u2[1]) * f; PIN(t[j]); };
    mul(0); mul(2); mul(1); mul(3);
    sum(0); sum(4); sum(2); sum(6);
    unsigned w0 = tag1(tr32(tr16(t[0], t[4]), tr16(t[2], t[6])), tag); PIN(w0);
    sum(1); sum(5);
    float x1 = tr16(t[1], t[5]); PIN(x1);
    sum(3); sum(7);
    const unsigned w1 = tag1(tr32(x1, tr16(t[3], t[7])), tag);
    __builtin_amdgcn_sched_group_barrier(0x8, 7, 0);
#pragma unroll
    for (int k = 0; k < 9; ++k) { __builtin_amdgcn_sched_group_barrier(0x2, 3, 0); __builtin_amdgcn_sched_group_barrier(0x8, 1, 0); }
    FENCE();
    st64(v2u{w0, w1}, rws, tb_off);
    if (a == 0) {
      const f32x2 gx = *(const f32x2*)(lds + L_GX + (r * 2 + (lane & 1)) * 8);
      const unsigned t1 = clock10ns();
      const int wait = (int)(__builtin_amdgcn_readfirstlane(flag_get(L_REF)) + LEAD2 + 8 * TAU_V + T_LOOK - t1);
      __builtin_amdgcn_s_sleep(4);
      while ((int)(clock10ns() - t1) < (wait < 120 ? wait : 120)) {}
      const rsrc_t rout = mk_rsrc(p.out, NB * DM * 2);
      unsigned o;
#pragma unroll 1
      for (unsigned spin = 0; spin < CAP_FAST; ++spin) {
        const v4u G0 = ld128<SC1>(rws, go, WS_TB + b * 131072 + cons * 32), G1 = ld128<SC1>(rws, go, WS_TB + b * 131072 + 65536 + cons * 32);
        unsigned bad = 0;
        f32x4 s4 = untag4(G0, tag & 0xfffu, bad); s4 += untag4(G1, tag & 0xfffu, bad);
        o = bf_bits(gx[1] + rbf(rbf(sum4_parity(s4)) * gx[0]));
        PIN(o);
        if (all_fresh(bad)) break;
        ORDER();
      }
      __builtin_amdgcn_raw_buffer_store_b16((unsigned short)o, rout, out_off, 0, 0);
    }
    return;
  }

  constexpr unsigned KVLEN = 2048 * DH * 2;
  const char* kp = (const char*)p.k + kvoff; const char* vp = (const char*)p.v + kvoff;
  const rsrc_t rk = mk_rsrc(kp, KVLEN);
  auto kvload = [&](rsrc_t rs, unsigned o, int i) { return ld128<NT>(rs, l16 + (i & 3) * 1024, o ^ (((i >> 2) & 1) * 4096 + (i >> 3) * 32768)); };
  float* const scs = (float*)(lds + L_S + w * 2048) + lane;
  v4u S[NU + LD], q;
  unsigned now, refv = 0;
  auto pace = [&](int sched) {
    const unsigned rf = __builtin_amdgcn_readfirstlane(refv);
    int lv = ((int)(now - rf) - sched) >> 5;
    asm volatile("" : "+s"(lv));
    lv = rf ? lv : 3;
    unsigned m = 0xffffu; m = lv > 0 ? 0x5555u : m; m = lv > 1 ? 0x1111u : m; m = lv > 2 ? 0x1249u : m;
    m = lv > 3 ? 0x0421u : m; m = lv > 4 ? 0x0101u : m; m = lv > 5 ? 0u : m;
    return (unsigned)__builtin_amdgcn_readfirstlane(m); };
  float ka[8], mw = -INFINITY;
  auto kdot = [&](v4u t, int i) {
    float d = dot2(t[0], q[0], 0.f); d = dot2(t[1], q[1], d); d = dot2(t[2], q[2], d); d = dot2(t[3], q[3], d);
    if (i >= 8) { i -= 8; d = trd<XOR8>(ka[i], d, lane & 8);
      if (i >= 4) { i -= 4; d = trd<XOR7>(ka[i], d, lane & 4);
        if (i >= 2) { i -= 2; d = trd<XOR2>(ka[i], d, lane & 2);
          if (i >= 1) { i -= 1; d = trd<XOR1>(ka[i], d, lane & 1); } } } }
    PIN(d); ka[i] = d; };
  auto kdone = [&](int g) { const float s = ka[0] * C2; scs[g * 64] = s; mw = fmaxf(mw, s); PIN(mw); };

  static_for<NU + LD>([&](auto N) {
    constexpr int n = N, dn = n < N0 ? D0 : D1;
    if constexpr (n >= dn) TOUCH(S[n - dn]);
    FENCE();
    S[n] = kvload(rk, kx, n);
    if constexpr (n == 0) __builtin_amdgcn_s_setprio(2);
    if constexpr (n == NS0) { FENCE(); WG_BARRIER(); }
    FENCE();
    if constexpr (n == NA - 1) {
#pragma unroll 1
      for (unsigned spin = 0; spin < CAP_LDS; ++spin) {
        const unsigned rdy = flag_get(L_RDY);
        ORDER();
        q = *(const v4u*)(lds + L_Q + c * 16);
        if (__builtin_amdgcn_readfirstlane(rdy)) break;
        __builtin_amdgcn_s_sleep(1);
      }
      now = clock10ns();
    }
    if constexpr (n == NA) flag_set(L_T0, now | 1u);
    if constexpr (n >= NA) {
      constexpr int hi = (n - NA + 1) * 4 < n - LD + 1 ? (n - NA + 1) * 4 : n - LD + 1;
      constexpr int lo = (n - NA) * 4 < n - LD ? (n - NA) * 4 : n - LD;
      static_for<hi - lo>([&](auto J) { constexpr int i = lo + J; kdot(S[i], i & 15); if constexpr ((i & 15) == 15) kdone(i >> 4); });
    }
    FENCE();
  });
  v4u R[16];
#pragma unroll
  for (int i = 0; i < LD; ++i) R[i] = S[NU + i];
  float snext = scs[0];
  v4u sel;
#pragma unroll
  for (int j = 0; j < 4; ++j) { const unsigned pick = (c & 8) ? 0x0302u : 0x0100u;
    sel[j] = ((c >> 1) & 3) != j ? 0x0c0c0c0cu : (c & 1) ? (pick << 16) | 0x0c0cu : 0x0c0c0000u | pick; }
  unsigned o_off = L_O + (4 * w + (r & 2)) * O_ROW + ((c * 8 + (r & 1) * 4) ^ rot);
  PIN(o_off); PIN(sel);
#pragma unroll 1
  for (int g = NU / 16; g < 8; ++g) {
    const rsrc_t rn = mk_rsrc(g == 7 ? vp : kp, KVLEN);
    const unsigned o0 = (g * 65536) ^ kx, o1 = (((g + 1) & 7) * 65536) ^ kx;
    const unsigned mask = pace((g - 8) * TAU_K);
    static_for<16>([&](auto I) { constexpr int i = I;
      TOUCH(R[i]); THROTTLE(mask, i); FENCE();
      R[(i + LD) & 15] = i + LD < 16 ? kvload(rk, o0, i + LD) : kvload(rn, o1, i + LD - 16);
      if constexpr (i == 15) { now = clock10ns(); refv = flag_get(L_REF); }
      FENCE();
      kdot(R[i], i);
      FENCE(); });
    kdone(g);
  }
  mw = wave_max(mw);
  flag_set(L_T + 4 * w, now);
  ORDER();
  flag_set(L_M + 4 * w, min(__float_as_uint(mw), SENT - 1u));
  f32x4 acc = {0.f, 0.f, 0.f, 0.f};
  const rsrc_t rv = mk_rsrc(vp, KVLEN);
#pragma unroll 1
  for (int g = 0; g < 8; ++g) {
    const rsrc_t rn = mk_rsrc(vp, g == 7 ? 0u : KVLEN);
    const unsigned o0 = (g * 65536) ^ kx, o1 = (((g + 1) & 7) * 65536) ^ kx;
    const float pg = exp2_(snext - mw), ph = rbf(pg);
    const unsigned pp = pk_bf16(ph, pg - ph);
    snext = scs[((g + 1) & 7) * 64];
    flag_set(L_T + 4 * w, now - g * TAU_V);
    const unsigned mask = pace(g * TAU_V) & (g == 7 ? 0xffu : 0xffffu);
    static_for<16>([&](auto I) { constexpr int i = I;
      const unsigned pb = __builtin_amdgcn_mov_dpp(pp, BCAST + i, 0xf, 0xf, true);
      v4u A;
#pragma unroll
      for (int j = 0; j < 4; ++j) A[j] = __builtin_amdgcn_perm(pb, pb, sel[j]);
      PIN(A);
      FENCE();
      TOUCH(R[i]); THROTTLE(mask, i); FENCE();
      R[(i + LD) & 15] = i + LD < 16 ? kvload(rv, o0, i + LD) : kvload(rn, o1, i + LD - 16);
      if constexpr (i == 15) { now = clock10ns(); refv = flag_get(L_REF); }
      FENCE();
      acc = mfma(A, R[i], acc);
      FENCE(); });
    const float e0 = tr32(acc[0], acc[2]), e1 = tr32(acc[1], acc[3]);
    const unsigned hi = pk_bf16(e0, e1), lo = pk_bf16(e0 - bflo(hi), e1 - bfhi(hi));
    *(unsigned*)(lds + o_off) = hi;
    *(unsigned*)(lds + o_off + O_ROW) = lo;
    ORDER();
  }
  WG_BARRIER();
}

extern "C" void kernel_launch(void* const* d_in, const int* in_sizes, int n_in, void* d_out, int out_size, void* d_ws, size_t ws_size,
                              hipStream_t stream) {
  Params p;
  p.x = (const unsigned short*)d_in[0]; p.k = (const unsigned short*)d_in[1]; p.v = (const unsigned short*)d_in[2];
  p.wq = (const unsigned short*)d_in[3]; p.wo = (const unsigned short*)d_in[4];
  p.w1 = (const unsigned short*)d_in[6]; p.b1 = (const unsigned short*)d_in[7]; p.w2 = (const unsigned short*)d_in[8]; p.b2 = (const unsigned short*)d_in[9];
  p.out = (unsigned short*)d_out;
  p.ws = (unsigned char*)d_ws;
  k_fused<<<256, 512, 0, stream>>>(p);
}
```

```cpp
#include <hip/hip_runtime.h>
#include <hip/hip_bf16.h>
#include <stdint.h>
#include <utility>

typedef unsigned v2u __attribute__((ext_vector_type(2)));
typedef unsigned v4u __attribute__((ext_vector_type(4)));
typedef unsigned v8u __attribute__((ext_vector_type(8)));
typedef float f32x2 __attribute__((ext_vector_type(2)));
typedef float f32x4 __attribute__((ext_vector_type(4)));
typedef __bf16 bf16x2 __attribute__((ext_vector_type(2)));
typedef __bf16 bf16x8 __attribute__((ext_vector_type(8)));
typedef __bf16 bf16x16 __attribute__((ext_vector_type(16)));
typedef __amdgpu_buffer_rsrc_t rsrc_t;
typedef __attribute__((address_space(3))) unsigned lds_u32;

constexpr int NB = 4, DM = 512, NH = 32, SK = 4096, DH = 128, FF = 1792;
constexpr int NT = 2, SC1 = 16;
constexpr unsigned SENT = 0xFFFFFFFFu, OOB = 0x80000000u;
constexpr unsigned SPIN_CAP = 1u << 14, LDS_CAP = 1u << 20;
constexpr float LOG2E = 1.4426950408889634f, C2 = 0.088388347648318440f * LOG2E;

#define P_NA 40
constexpr int D0 = 7, N0 = 10, D1 = 5;
constexpr int NS0 = D0 + 7;
constexpr int NA = P_NA;
constexpr int NU = 48;
constexpr int LD = 8;
constexpr int DO = 3;
static_assert((NU + LD - NA) * 4 >= NU, "the backlog is gone before the loop starts");
#define P_TAUK 228
#define P_TAUV 215
#define P_OFF0 70
#define P_OFF1 120
#define P_OFF2 135
#define P_C1 1100
#define P_TP 82
#define P_EX 0

template <class F, int... I> __device__ __forceinline__ void static_for_impl(F&& f, std::integer_sequence<int, I...>) { (f(std::integral_constant<int, I>{}), ...); }
template <int N, class F> __device__ __forceinline__ void static_for(F&& f) { static_for_impl(f, std::make_integer_sequence<int, N>{}); }

#define TOUCH(x) asm volatile("" :: "v"(x))
#define PIN(x) asm volatile("" : "+v"(x))
#define ORDER() asm volatile("" ::: "memory")
#define FENCE() __builtin_amdgcn_sched_barrier(0)
#define WG_BARRIER() asm volatile("s_waitcnt lgkmcnt(0)\n\ts_barrier" ::: "memory")
#define THROTTLE(mask, i) asm volatile("s_bitcmp1_b32 %0, %1\n\ts_cbranch_scc0 1f\n\ts_waitcnt vmcnt(3)\n1:" :: "s"(mask), "n"(i) : "scc")

__device__ __forceinline__ unsigned clock10ns() { return (unsigned)__builtin_amdgcn_s_memrealtime(); }
__device__ __forceinline__ float dot2(unsigned a, unsigned b, float acc) {
  return __builtin_amdgcn_fdot2_f32_bf16(__builtin_bit_cast(bf16x2, a), __builtin_bit_cast(bf16x2, b), acc, false);
}
__device__ __forceinline__ f32x4 mfma(v4u a, v4u b, f32x4 c) {
  return __builtin_amdgcn_mfma_f32_16x16x32_bf16(__builtin_bit_cast(bf16x8, a), __builtin_bit_cast(bf16x8, b), c, 0, 0, 0);
}
__device__ __forceinline__ f32x4 smfma(v4u a, v8u b, f32x4 c, unsigned idx) {
  return __builtin_amdgcn_smfmac_f32_16x16x64_bf16(__builtin_bit_cast(bf16x8, a), __builtin_bit_cast(bf16x16, b), c, (int)idx, 0, 0);
}
__device__ __forceinline__ float bflo(unsigned u) { return __uint_as_float(u << 16); }
__device__ __forceinline__ float bfhi(unsigned u) { return __uint_as_float(u & 0xffff0000u); }
__device__ __forceinline__ unsigned pk_bf16(float lo, float hi) { bf16x2 r; r.x = (__bf16)lo; r.y = (__bf16)hi; return __builtin_bit_cast(unsigned, r); }
__device__ __forceinline__ unsigned bf_bits(float x) { return (unsigned)__builtin_bit_cast(unsigned short, (__bf16)x); }
__device__ __forceinline__ float rbf(float x) { return bflo(bf_bits(x)); }
__device__ __forceinline__ float exp2_(float x) { return __builtin_amdgcn_exp2f(x); }
__device__ __forceinline__ float rcp_(float x) { return __builtin_amdgcn_rcpf(x); }

template <int CTRL> __device__ __forceinline__ float dpp(float x) { return __int_as_float(__builtin_amdgcn_mov_dpp(__float_as_int(x), CTRL, 0xf, 0xf, true)); }
constexpr int XOR1 = 0xB1, XOR2 = 0x4E, XOR7 = 0x141, XOR8 = 0x128, ROR4 = 0x124, BCAST = 0x150;
__device__ __forceinline__ float tr32(float a, float b) {
  auto s = __builtin_amdgcn_permlane32_swap(__float_as_uint(a), __float_as_uint(b), false, false);
  return __uint_as_float(s[0]) + __uint_as_float(s[1]);
}
__device__ __forceinline__ float tr16(float a, float b) {
  auto s = __builtin_amdgcn_permlane16_swap(__float_as_uint(a), __float_as_uint(b), false, false);
  return __uint_as_float(s[0]) + __uint_as_float(s[1]);
}
template <int CTRL> __device__ __forceinline__ float trd(float lo, float hi, bool up) {
  const float t1 = lo + dpp<CTRL>(lo), t2 = hi + dpp<CTRL>(hi);
  return up ? t2 : t1;
}
__device__ __forceinline__ float wave_sum(float x) {
  x += dpp<XOR1>(x); x += dpp<XOR2>(x); x += dpp<XOR7>(x); x += dpp<XOR8>(x);
  x = tr16(x, x);
  return tr32(x, x);
}
__device__ __forceinline__ float wave_max(float x) {
  x = fmaxf(x, dpp<XOR1>(x)); x = fmaxf(x, dpp<XOR2>(x)); x = fmaxf(x, dpp<XOR7>(x)); x = fmaxf(x, dpp<XOR8>(x));
  auto s = __builtin_amdgcn_permlane16_swap(__float_as_uint(x), __float_as_uint(x), false, false);
  x = fmaxf(__uint_as_float(s[0]), __uint_as_float(s[1]));
  auto t = __builtin_amdgcn_permlane32_swap(__float_as_uint(x), __float_as_uint(x), false, false);
  return fmaxf(__uint_as_float(t[0]), __uint_as_float(t[1]));
}
__device__ __forceinline__ float sum4_parity(f32x4 v) {
  float s = tr16(tr32(v[0], v[2]), tr32(v[1], v[3]));
  s += dpp<XOR8>(s); s += dpp<ROR4>(s); s += dpp<XOR2>(s);
  return s;
}

__device__ __forceinline__ rsrc_t mk_rsrc(const void* p, unsigned bytes) { return __builtin_amdgcn_make_buffer_rsrc(const_cast<void*>(p), 0, bytes, 0x00020000); }
template <int AUX> __device__ __forceinline__ v4u ld128(rsrc_t r, unsigned v, unsigned s) { return __builtin_amdgcn_raw_buffer_load_b128(r, v, s, AUX); }
template <int AUX> __device__ __forceinline__ v2u ld64(rsrc_t r, unsigned v, unsigned s) { return __builtin_amdgcn_raw_buffer_load_b64(r, v, s, AUX); }
__device__ __forceinline__ void st128(v4u d, rsrc_t r, unsigned off) { __builtin_amdgcn_raw_buffer_store_b128(d, r, off, 0, SC1); }
__device__ __forceinline__ void st64(v2u d, rsrc_t r, unsigned off) { __builtin_amdgcn_raw_buffer_store_b64(d, r, off, 0, SC1); }
__device__ __forceinline__ unsigned tag1(float v, unsigned tag) { return ((__float_as_uint(v) + 0x800u) & 0xfffff000u) | (tag & 0xfffu); }
__device__ __forceinline__ v4u tag4(f32x4 v, unsigned tag) { return v4u{tag1(v[0], tag), tag1(v[1], tag), tag1(v[2], tag), tag1(v[3], tag)}; }
__device__ __forceinline__ f32x4 untag4(v4u g, unsigned tag12, unsigned& bad) {
  g ^= tag12;
  bad |= (g[0] | g[1]) | (g[2] | g[3]);
  return __builtin_bit_cast(f32x4, g);
}
__device__ __forceinline__ bool all_fresh(unsigned bad) { return !__any((bad & 0xfffu) != 0u); }

__device__ __forceinline__ float row_sum(float x) { x += dpp<XOR1>(x); x += dpp<XOR2>(x); x = tr16(x, x); return tr32(x, x); }
__device__ __forceinline__ void ln_stats(const v4u (&x)[4], float& mean, float& inv) {
  float s = 0.f;
#pragma unroll
  for (int t = 0; t < 4; ++t)
#pragma unroll
    for (int j = 0; j < 4; ++j) s += bflo(x[t][j]) + bfhi(x[t][j]);
  const float mu = row_sum(s) * (1.0f / DM);
  float q = 0.f;
#pragma unroll
  for (int t = 0; t < 4; ++t)
#pragma unroll
    for (int j = 0; j < 4; ++j) { const float d0 = bflo(x[t][j]) - mu, d1 = bfhi(x[t][j]) - mu; q = fmaf(d0, d0, q); q = fmaf(d1, d1, q); }
  const float var = row_sum(q) * (1.0f / DM);
  mean = rbf(mu);
  inv = rcp_(rbf(__builtin_amdgcn_sqrtf(rbf(rbf(var) + 1e-5f))));
}
__device__ __forceinline__ v4u ln_apply(v4u x, float mean, float inv) {
  v4u o;
#pragma unroll
  for (int j = 0; j < 4; ++j) { const unsigned d = pk_bf16(bflo(x[j]) - mean, bfhi(x[j]) - mean); o[j] = pk_bf16(bflo(d) * inv, bfhi(d) * inv); }
  return o;
}

template <int J, class T> __device__ __forceinline__ v4u bfrag(const T* t) {
  v4u f;
#pragma unroll
  for (int d = 0; d < 4; ++d) f[d] = __builtin_amdgcn_perm(t[2 * d + 1][J >> 1], t[2 * d][J >> 1], (J & 1) ? 0x07060302u : 0x05040100u);
  return f;
}
template <int LO, int HI> __device__ __forceinline__ void tile_load(v2u (&u)[32], rsrc_t r, unsigned voff, unsigned stride) {
#pragma unroll
  for (int n = LO; n < HI; ++n) u[n] = ld64<NT>(r, voff, n * stride);
  FENCE();
}
__device__ __forceinline__ f32x4 tile_mul(v4u a, const v2u (&u)[32]) {
  f32x4 o = {0.f, 0.f, 0.f, 0.f};
  static_for<16>([&](auto N) { constexpr int s = N >> 2, j = N & 3; o[j] += mfma(a, bfrag<j>(u + 8 * s), f32x4{0.f, 0.f, 0.f, 0.f})[s]; });
  return o;
}

constexpr unsigned WS_E = 0;
constexpr unsigned WS_LS = 4096;
constexpr unsigned WS_CK = 32768;
constexpr unsigned WS_X1 = 65536;
constexpr unsigned WS_MP = WS_X1 + 32 * 4096;
constexpr unsigned WS_TB = WS_MP + 16 * 8192;
constexpr unsigned WS_QX = WS_TB + 4 * 64 * 2048;
constexpr unsigned WS_BYTES = WS_QX + 32 * 8192;
constexpr int L_Q = 0, L_M = 256, L_F = 272, L_TAG = 288, L_REF = 292, L_RDY = 296, L_T0 = 300, L_T = 304, L_GX = 320, L_END_T = 384,
              L_H = 1024, L_O = 2048, O_ROW = 144, L_S = L_O + 16 * O_ROW, L_BYTES = L_S + 8192;

struct Params {
  const unsigned short* x; const unsigned short* k; const unsigned short* v; const unsigned short* wq; const unsigned short* wo;
  const unsigned short* w1; const unsigned short* b1; const unsigned short* w2; const unsigned short* b2;
  unsigned short* out; unsigned char* ws;
};

#define DIAG 1
__global__ void __launch_bounds__(512, 2) k_fused(Params p_arg) {
  unsigned long long tmark = 0;
  const __attribute__((address_space(4))) Params* kap = (const __attribute__((address_space(4))) Params*)__builtin_amdgcn_kernarg_segment_ptr();
  if (DIAG == 1) asm volatile("s_memrealtime %0\n\ts_waitcnt lgkmcnt(0)" : "=s"(tmark), "+s"(kap));
  Params p; p.x = kap->x; p.k = kap->k; p.v = kap->v; p.wq = kap->wq; p.wo = kap->wo; p.w1 = kap->w1; p.b1 = kap->b1; p.w2 = kap->w2; p.b2 = kap->b2; p.out = kap->out; p.ws = kap->ws;
  if (DIAG == 2) asm volatile("s_waitcnt lgkmcnt(0)\n\ts_memrealtime %0\n\ts_waitcnt lgkmcnt(0)" : "=s"(tmark), "+s"(p.x), "+s"(p.ws), "+s"(p.b2));
  __shared__ __attribute__((aligned(16))) unsigned char lds[L_BYTES];
  auto flag_get = [&](int off) { return __hip_atomic_load((lds_u32*)lds + off / 4, __ATOMIC_RELAXED, __HIP_MEMORY_SCOPE_WORKGROUP); };
  auto flag_set = [&](int off, unsigned v) { __hip_atomic_store((lds_u32*)lds + off / 4, v, __ATOMIC_RELAXED, __HIP_MEMORY_SCOPE_WORKGROUP); };
  const int blk = blockIdx.x, lane = threadIdx.x & 63, w = __builtin_amdgcn_readfirstlane(threadIdx.x >> 6);
  const int idx = blk >> 3, h = (blk & 7) * 4 + (idx & 3), bs = idx >> 2, b = bs >> 1, sp = bs & 1;
  const int r = lane >> 4, c = lane & 15;
  const unsigned l16 = lane * 16;
  unsigned kvoff = ((b * NH + h) * SK + sp * 2048) * (DH * 2), kx = (w & 3) * 8192 + bs * 65536;
  asm volatile("" : "+s"(kvoff), "+s"(kx));
  asm volatile("" :: "s"(p.x), "s"(p.k), "s"(p.v), "s"(p.wq), "s"(p.wo), "s"(p.w1), "s"(p.b1), "s"(p.w2), "s"(p.b2), "s"(p.out), "s"(p.ws));
  const unsigned rot = b * 32 + sp * 4;

  if (w >= 4) {
    const int a = w - 4, prod = 2 * h + sp;
    *(v4u*)(lds + L_M) = v4u{SENT, SENT, SENT, SENT}; *(v4u*)(lds + L_TAG) = v4u{0u, 0u, 0u, 0u};
    if (a != 0) WG_BARRIER();
    FENCE();
    const rsrc_t rws = mk_rsrc(p.ws, WS_BYTES), rx = mk_rsrc(p.x, NB * DM * 2);
    const unsigned xoff = ((lane >> 2) & 3) * (DM * 2) + ((lane & 3) + 4 * r) * 16;
    if (a == 0) {
      v4u xs[4]; v2u U[32];
#pragma unroll
      for (int t = 0; t < 4; ++t) xs[t] = ld128<0>(rx, xoff, ((t + b) & 3) * 256);
      FENCE();
      tile_load<0, 32>(U, mk_rsrc(p.wq + (size_t)b * 128 * (NH * DH) + h * DH + sp * 64, 128 * NH * DH * 2), r * 32 * 8192 + c * 8, 8192);
      const unsigned ep = __builtin_amdgcn_raw_buffer_load_b32(rws, 0, WS_E, SC1);
      FENCE();
      WG_BARRIER();
      if (DIAG == 3) { TOUCH(U[31]); FENCE(); tmark = __builtin_amdgcn_s_memrealtime(); FENCE(); }
      float mean, inv;
      ln_stats(xs, mean, inv);
      const f32x4 g = tile_mul(ln_apply(xs[0], mean, inv), U);
      FENCE();
      const unsigned tg = __builtin_amdgcn_readfirstlane(ep) + 1u;
      st128(tag4(g, tg), rws, WS_QX + h * 8192 + bs * 1024 + l16);
      asm volatile("s_waitcnt vmcnt(0)" ::: "memory");
      __builtin_amdgcn_s_sleep(16);
      f32x4 q4;
#pragma unroll 1
      for (unsigned spin = 0; spin < SPIN_CAP; ++spin) {
        const v4u G0 = ld128<SC1>(rws, r * 1024 + c * 16, WS_QX + h * 8192 + b * 256), G1 = ld128<SC1>(rws, r * 1024 + c * 16, WS_QX + h * 8192 + b * 256 + 4096);
        unsigned bad = 0;
        q4 = untag4(G0, tg & 0xfffu, bad); q4 += untag4(G1, tg & 0xfffu, bad);
        if (all_fresh(bad)) break;
        ORDER();
      }
      *(unsigned*)(lds + L_Q + (lane & 31) * 8 + (lane >> 5) * 4) = pk_bf16(tr32(q4[0], q4[2]), tr32(q4[1], q4[3]));
      *(unsigned*)(lds + L_TAG) = tg;
      ORDER();
      flag_set(L_RDY, 1u);
    }
#pragma unroll 1
    for (unsigned spin = 0; spin < LDS_CAP; ++spin) {
      if (__builtin_amdgcn_readfirstlane(flag_get(L_RDY))) break;
      __builtin_amdgcn_s_sleep(4);
    }
    ORDER();
    const unsigned tag = __builtin_amdgcn_readfirstlane(*(const unsigned*)(lds + L_TAG));

    if (a == 2 && h < 28) {
      const int cg = (h & 1) * 4 + b;
      const unsigned stride = sp ? DM * 2 : FF * 2;
      const rsrc_t rt = mk_rsrc(sp ? p.w2 + (size_t)(h >> 1) * 128 * DM + cg * 64 : p.w1 + (size_t)b * 128 * FF + h * 64, 128 * stride);
      v2u U[32]; v4u A;
      tile_load<0, 16>(U, rt, r * 32 * stride + c * 8, stride);
      TOUCH(U[15]); FENCE();
      tile_load<16, 32>(U, rt, r * 32 * stride + c * 8, stride);
      if (sp == 0) {
        v4u xs[4];
#pragma unroll
        for (int t = 0; t < 4; ++t) xs[t] = ld128<0>(rx, xoff, ((t + b) & 3) * 256);
#pragma unroll 1
        for (int it = 0; it < 2; ++it) {
          float mean, inv;
          ln_stats(xs, mean, inv);
#pragma unroll
          for (int t = 0; t < 4; ++t) xs[t] = ln_apply(xs[t], mean, inv);
        }
        A = xs[0];
      } else {
        v2u b1v[2];
#pragma unroll
        for (int k = 0; k < 2; ++k) b1v[k] = ld64<0>(mk_rsrc(p.b1 + (h >> 1) * 128, 256), c * 8, k * 128);
        f32x4 z4[2];
#pragma unroll 1
        for (unsigned spin = 0; spin < SPIN_CAP; ++spin) {
          __builtin_amdgcn_s_sleep(112); __builtin_amdgcn_s_sleep(112);
          v4u G[8];
#pragma unroll
          for (int j = 0; j < 8; ++j) G[j] = ld128<SC1>(rws, l16, WS_X1 + (h >> 1) * 8192 + j * 1024);
          unsigned bad = 0;
#pragma unroll
          for (int j = 0; j < 8; ++j) { const f32x4 v = untag4(G[j], tag & 0xfffu, bad); z4[j >> 2] = (j & 3) ? z4[j >> 2] + v : v; }
          if (all_fresh(bad)) break;
          ORDER();
        }
#pragma unroll
        for (int k = 0; k < 2; ++k) {
          float hv[4];
#pragma unroll
          for (int j = 0; j < 4; ++j) {
            const float z = rbf(rbf(z4[k][j]) + ((j & 1) ? bfhi(b1v[k][j >> 1]) : bflo(b1v[k][j >> 1])));
            hv[j] = z * rcp_(1.0f + exp2_((-2.0f * 0.7978845608028654f * LOG2E) * (z + 0.044715f * z * z * z)));
          }
          *(v2u*)(lds + L_H + r * 256 + k * 128 + c * 8) = v2u{pk_bf16(hv[0], hv[1]), pk_bf16(hv[2], hv[3])};
        }
        asm volatile("s_waitcnt lgkmcnt(0)" ::: "memory");
        A = *(const v4u*)(lds + L_H + ((lane >> 2) & 3) * 256 + ((lane & 3) + 4 * r) * 16);
      }
      st128(tag4(tile_mul(A, U), tag), rws, (sp ? WS_MP + (h >> 1) * 8192 + cg * 1024 : WS_X1 + h * 4096 + b * 1024) + l16);
    }

    const unsigned ck_st = (lane == 0 && (idx & 3) == 0) ? WS_CK + (bs * 8 + (blk & 7)) * 8 : OOB;
    unsigned t0 = 0;
    if (a == 3) {
#pragma unroll 1
      for (unsigned spin = 0; spin < LDS_CAP; ++spin) {
        t0 = __builtin_amdgcn_readfirstlane(flag_get(L_T0));
        if (t0) break;
        __builtin_amdgcn_s_sleep(4);
      }
      st64(v2u{t0, tag}, rws, ck_st);
    }

    v8u Bf[2][8];
    {
      const rsrc_t ro = mk_rsrc(p.wo + (size_t)h * DH * DM + a * 128, DH * DM * 2);
      const unsigned vo = r * 8 * 1024 + c * 16;
      v4u T[32]; v2u ck;
      static_for<32 + DO>([&](auto N) { constexpr int n = N;
        if constexpr (n >= DO) TOUCH(T[n - DO]);
        FENCE();
        if constexpr (n == 10) ck = ld64<SC1>(rws, a == 3 ? lane * 8 : OOB, WS_CK);
        if constexpr (n == 10 + DO) { TOUCH(ck);
          if (a == 3) {
            const unsigned tn = clock10ns();
            const float dt = wave_sum((float)(int)((ck[1] == tag ? ck[0] : tn) - t0)) * (1.0f / 64);
            flag_set(L_REF, (t0 + (int)dt + (128 + LD - NA) * P_TAUK / 16 - P_OFF0) | 1u); } }
        FENCE();
        if constexpr (n < 32) T[n] = ld128<0>(ro, vo, (((n >> 3) * 32 + (n & 7)) ^ rot) * 1024);
        FENCE();
        if constexpr (n >= DO && ((n - DO) & 7) == 7) { constexpr int s = (n - DO) >> 3;
          static_for<8>([&](auto J) { constexpr int j = J; const v4u f = bfrag<j>(T + 8 * s);
            if constexpr (s & 1) Bf[s >> 1][j].hi = f; else Bf[s >> 1][j].lo = f;
            PIN(Bf[s >> 1][j]); }); }
        FENCE(); });
    }

    const int cons = (blk & 7) * 8 + (idx & 7), col0 = 32 * (cons >> 2) + 4 * (cons & 1) + (cons & 2);
    const unsigned co = (lane & 1) * 32 + (r >> 1) * 16 + (r & 1) * 2;
    if (a == 3) {
      unsigned mb;
#pragma unroll 1
      for (unsigned spin = 0; spin < LDS_CAP; ++spin) {
        mb = flag_get(L_M + 4 * (lane & 3));
        if (__all(mb != SENT)) break;
        __builtin_amdgcn_s_sleep(4);
      }
      ORDER();
      const float m4 = __uint_as_float(mb);
      float M = fmaxf(m4, dpp<XOR1>(m4)); M = fmaxf(M, dpp<XOR2>(M));
      float L = 0.f;
#pragma unroll
      for (int i = 0; i < 8; ++i) { const f32x4 s4 = *(const f32x4*)(lds + L_S + i * 1024 + l16);
#pragma unroll
        for (int j = 0; j < 4; ++j) L += exp2_(s4[j] - M); }
      L = wave_sum(L);
      const float lse = M + __builtin_amdgcn_logf(L);
      const unsigned tk = flag_get(L_T);
      st128(v4u{__float_as_uint(lse), tag, tk, tag}, rws, lane == 0 ? WS_LS + (b * 64 + prod) * 16 : OOB);
      const unsigned long long need = blk == 0 ? ~0ull : 1ull << (prod ^ 1);
      float other; unsigned mean;
#pragma unroll 1
      for (unsigned spin = 0; spin < SPIN_CAP; ++spin) {
        __builtin_amdgcn_s_sleep(80);
        const v4u m = ld128<SC1>(rws, l16, WS_LS + b * 1024);
        const bool ok = ((m[1] ^ tag) | (m[3] ^ tag)) == 0u;
        const unsigned tn = clock10ns();
        const float dt = __builtin_amdgcn_fmed3f(wave_sum((float)(int)((ok ? m[2] : tn) - tk)) * (1.0f / 64), -1000.f, 1000.f);
        mean = __builtin_amdgcn_readfirstlane(tk + (int)dt);
        flag_set(L_REF, (mean - P_OFF1) | 1u);
        other = __uint_as_float(__builtin_amdgcn_readlane(m[0], prod ^ 1));
        if ((__ballot(ok) & need) == need) break;
        ORDER();
      }
      *(float*)(lds + L_F + 4 * (lane & 3)) = exp2_(m4 - M) * rcp_(L) * rcp_(1.0f + exp2_(other - lse));
      __builtin_amdgcn_raw_buffer_store_b32(tag, rws, (blk == 0 && lane == 0) ? WS_E : OOB, 0, SC1);
      while ((int)(clock10ns() - mean) < P_C1) __builtin_amdgcn_s_sleep(2);
      st64(v2u{flag_get(L_T), tag}, rws, ck_st + 512);
      __builtin_amdgcn_s_sleep(36);
      const v2u ck = ld64<SC1>(rws, lane * 8, WS_CK + 512);
      const bool ok = ck[1] == tag;
      const int dt = (int)(wave_sum(ok ? (float)(int)(ck[0] - mean) : 0.f) * rcp_(fmaxf(wave_sum(ok ? 1.f : 0.f), 1.f)));
      flag_set(L_REF, (mean + dt - P_OFF2) | 1u);
      flag_set(L_END_T, mean + dt + dt * P_EX / 4 + 8 * P_TAUV + P_TP);
    } else if (a == 2) {
      const unsigned b2v = __builtin_amdgcn_raw_buffer_load_b16(mk_rsrc(p.b2, DM * 2), co, col0 * 2, 0);
      const unsigned xv = __builtin_amdgcn_raw_buffer_load_b16(rx, co, (b * DM + col0) * 2, 0);
      const unsigned mo = lane < 28 ? (unsigned)((lane >> 1) * 8192 + (lane & 1) * 64) : OOB, ms = WS_MP + (col0 >> 6) * 1024 + b * 256 + (col0 & 63) * 4;
      f32x4 m4;
#pragma unroll 1
      for (unsigned spin = 0; spin < SPIN_CAP; ++spin) {
        __builtin_amdgcn_s_sleep(64);
        const v2u G0 = ld64<SC1>(rws, mo, ms), G1 = ld64<SC1>(rws, mo, ms + 32);
        unsigned bad = 0;
        m4 = untag4(v4u{G0[0], G0[1], G1[0], G1[1]}, lane < 28 ? tag & 0xfffu : 0u, bad);
        if (all_fresh(bad)) break;
        ORDER();
      }
      const float m = rbf(rbf(sum4_parity(m4)) + bflo(b2v));
      *(f32x2*)(lds + L_GX + (r * 2 + (lane & 1)) * 8) = f32x2{rbf(m * rcp_(1.0f + exp2_(-LOG2E * m))), bflo(xv)};
    }

    unsigned a_off = L_O + c * O_ROW + r * 16, sidx = (lane & 2) ? 0xeeeeu : 0x4444u;
    unsigned tb_off = WS_TB + (b * 64 + prod) * 2048 + a * 512 + (c >> 2) * 128 + r * 32 + (c & 3) * 8;
    unsigned go = (lane >> 1) * 2048 + (lane & 1) * 16, out_off = (lane & 14) == 0 ? (b * DM + col0) * 2 + co : OOB;
    f32x4 d[8];
#pragma unroll
    for (int j = 0; j < 8; ++j) { d[j] = f32x4{0.f, 0.f, 0.f, 0.f}; PIN(d[j]); }
    PIN(a_off); PIN(sidx); PIN(tb_off); PIN(go); PIN(out_off);
    asm volatile("s_branch 1f\n\t.p2align 11\n1:");
    WG_BARRIER();
    const v4u A0 = *(const v4u*)(lds + a_off), A1 = *(const v4u*)(lds + a_off + 64);
    const float f = *(const float*)(lds + L_F + r * 4);
    FENCE();
    float t[8];
    auto mul = [&](int j) { d[j] = smfma(A0, Bf[0][j], d[j], sidx); d[j + 4] = smfma(A0, Bf[0][j + 4], d[j + 4], sidx);
      d[j] = smfma(A1, Bf[1][j], d[j], sidx); d[j + 4] = smfma(A1, Bf[1][j + 4], d[j + 4], sidx); };
    auto sum = [&](int j) { const f32x2 u2 = d[j].lo + d[j].hi; t[j] = (u2[0] + u2[1]) * f; PIN(t[j]); };
    mul(0); mul(2); mul(1); mul(3);
    sum(0); sum(4); sum(2); sum(6);
    unsigned w0 = tag1(tr32(tr16(t[0], t[4]), tr16(t[2], t[6])), tag); PIN(w0);
    sum(1); sum(5);
    float x1 = tr16(t[1], t[5]); PIN(x1);
    sum(3); sum(7);
    const unsigned w1 = tag1(tr32(x1, tr16(t[3], t[7])), tag);
    __builtin_amdgcn_sched_group_barrier(0x8, 7, 0);
#pragma unroll
    for (int k = 0; k < 9; ++k) { __builtin_amdgcn_sched_group_barrier(0x2, 3, 0); __builtin_amdgcn_sched_group_barrier(0x8, 1, 0); }
    FENCE();
    st64(v2u{w0, w1}, rws, tb_off);
    if (a == 0) {
      const f32x2 gx = *(const f32x2*)(lds + L_GX + (r * 2 + (lane & 1)) * 8);
      const unsigned t1 = clock10ns();
      const int wait = (int)(__builtin_amdgcn_readfirstlane(flag_get(L_END_T)) - t1);
      __builtin_amdgcn_s_sleep(4);
      while ((int)(clock10ns() - t1) < (wait < 120 ? wait : 120)) {}
      f32x4 s4;
#pragma unroll 1
      for (unsigned spin = 0; spin < SPIN_CAP; ++spin) {
        const v4u G0 = ld128<SC1>(rws, go, WS_TB + b * 131072 + cons * 32), G1 = ld128<SC1>(rws, go, WS_TB + b * 131072 + 65536 + cons * 32);
        unsigned bad = 0;
        s4 = untag4(G0, tag & 0xfffu, bad); s4 += untag4(G1, tag & 0xfffu, bad);
        if (all_fresh(bad)) break;
        __builtin_amdgcn_s_sleep(1);
        ORDER();
      }
      const float o = gx[1] + rbf(rbf(sum4_parity(s4)) * gx[0]);
      __builtin_amdgcn_raw_buffer_store_b16((unsigned short)bf_bits(o), mk_rsrc(p.out, NB * DM * 2), out_off, 0, 0);
      if (blk == 0) { while (clock10ns() - (unsigned)tmark < 10000 + 400 * (unsigned)__builtin_ctzll((unsigned long long)p.v)) __builtin_amdgcn_s_sleep(1); }
    }
    return;
  }

  constexpr unsigned KVLEN = 2048 * DH * 2;
  const char* kp = (const char*)p.k + kvoff; const char* vp = (const char*)p.v + kvoff;
  const rsrc_t rk = mk_rsrc(kp, KVLEN);
  auto kvload = [&](rsrc_t rs, unsigned o, int i) { return ld128<NT>(rs, l16 + (i & 3) * 1024, o ^ (((i >> 2) & 1) * 4096 + (i >> 3) * 32768)); };
  float* const scs = (float*)(lds + L_S + w * 2048) + lane;
  v4u S[NU + LD], q;
  unsigned now, refv = 0;
  auto pace = [&](int sched) {
    const unsigned rf = __builtin_amdgcn_readfirstlane(refv);
    int lv = ((int)(now - rf) - sched) >> 5;
    asm volatile("" : "+s"(lv));
    lv = rf ? lv : 3;
    unsigned m = 0xffffu; m = lv > 0 ? 0x5555u : m; m = lv > 1 ? 0x1111u : m; m = lv > 2 ? 0x1249u : m;
    m = lv > 3 ? 0x0421u : m; m = lv > 4 ? 0x0101u : m; m = lv > 5 ? 0u : m;
    return (unsigned)__builtin_amdgcn_readfirstlane(m); };
  float ka[8], mw = -INFINITY;
  auto kdot = [&](v4u t, int i) {
    float d = dot2(t[0], q[0], 0.f); d = dot2(t[1], q[1], d); d = dot2(t[2], q[2], d); d = dot2(t[3], q[3], d);
    if (i >= 8) { i -= 8; d = trd<XOR8>(ka[i], d, lane & 8);
      if (i >= 4) { i -= 4; d = trd<XOR7>(ka[i], d, lane & 4);
        if (i >= 2) { i -= 2; d = trd<XOR2>(ka[i], d, lane & 2);
          if (i >= 1) { i -= 1; d = trd<XOR1>(ka[i], d, lane & 1); } } } }
    PIN(d); ka[i] = d; };
  auto kdone = [&](int g) { const float s = ka[0] * C2; scs[g * 64] = s; mw = fmaxf(mw, s); PIN(mw); };

  static_for<NU + LD>([&](auto N) {
    constexpr int n = N, dn = n < N0 ? D0 : D1;
    if constexpr (n >= dn) TOUCH(S[n - dn]);
    FENCE();
    S[n] = kvload(rk, kx, n);
    if constexpr (n == 0) __builtin_amdgcn_s_setprio(2);
    if constexpr (n == NS0) { FENCE(); WG_BARRIER(); }
    FENCE();
    if constexpr (n == NA - 1) {
#pragma unroll 1
      for (unsigned spin = 0; spin < LDS_CAP; ++spin) {
        const unsigned rdy = flag_get(L_RDY);
        ORDER();
        q = *(const v4u*)(lds + L_Q + c * 16);
        if (__builtin_amdgcn_readfirstlane(rdy)) break;
        __builtin_amdgcn_s_sleep(1);
      }
      now = clock10ns();
    }
    if constexpr (n == NA) flag_set(L_T0, now | 1u);
    if constexpr (n >= NA) {
      constexpr int hi = (n - NA + 1) * 4 < n - LD + 1 ? (n - NA + 1) * 4 : n - LD + 1;
      constexpr int lo = (n - NA) * 4 < n - LD ? (n - NA) * 4 : n - LD;
      static_for<hi - lo>([&](auto J) { constexpr int i = lo + J; kdot(S[i], i & 15); if constexpr ((i & 15) == 15) kdone(i >> 4); });
    }
    FENCE();
  });
  v4u R[16];
#pragma unroll
  for (int i = 0; i < LD; ++i) R[i] = S[NU + i];
  float snext = scs[0];
  v4u sel;
#pragma unroll
  for (int j = 0; j < 4; ++j) { const unsigned pick = (c & 8) ? 0x0302u : 0x0100u;
    sel[j] = ((c >> 1) & 3) != j ? 0x0c0c0c0cu : (c & 1) ? (pick << 16) | 0x0c0cu : 0x0c0c0000u | pick; }
  unsigned o_off = L_O + (4 * w + (r & 2)) * O_ROW + ((c * 8 + (r & 1) * 4) ^ rot);
  PIN(o_off); PIN(sel);
#pragma unroll 1
  for (int g = NU / 16; g < 8; ++g) {
    const rsrc_t rn = mk_rsrc(g == 7 ? vp : kp, KVLEN);
    const unsigned o0 = (g * 65536) ^ kx, o1 = (((g + 1) & 7) * 65536) ^ kx;
    const unsigned mask = pace((g - 8) * P_TAUK);
    static_for<16>([&](auto I) { constexpr int i = I;
      TOUCH(R[i]); THROTTLE(mask, i); FENCE();
      R[(i + LD) & 15] = i + LD < 16 ? kvload(rk, o0, i + LD) : kvload(rn, o1, i + LD - 16);
      if constexpr (i == 15) { now = clock10ns(); refv = flag_get(L_REF); }
      FENCE();
      kdot(R[i], i);
      FENCE(); });
    kdone(g);
  }
  mw = wave_max(mw);
  flag_set(L_T + 4 * w, now);
  ORDER();
  flag_set(L_M + 4 * w, min(__float_as_uint(mw), SENT - 1u));
  f32x4 acc = {0.f, 0.f, 0.f, 0.f};
  const rsrc_t rv = mk_rsrc(vp, KVLEN);
#pragma unroll 1
  for (int g = 0; g < 8; ++g) {
    const rsrc_t rn = mk_rsrc(vp, g == 7 ? 0u : KVLEN);
    const unsigned o0 = (g * 65536) ^ kx, o1 = (((g + 1) & 7) * 65536) ^ kx;
    const float pg = exp2_(snext - mw), ph = rbf(pg);
    const unsigned pp = pk_bf16(ph, pg - ph);
    snext = scs[((g + 1) & 7) * 64];
    flag_set(L_T + 4 * w, now - g * P_TAUV);
    const unsigned mask = pace(g * P_TAUV) & (g == 7 ? 0xffu : 0xffffu);
    static_for<16>([&](auto I) { constexpr int i = I;
      const unsigned pb = __builtin_amdgcn_mov_dpp(pp, BCAST + i, 0xf, 0xf, true);
      v4u A;
#pragma unroll
      for (int j = 0; j < 4; ++j) A[j] = __builtin_amdgcn_perm(pb, pb, sel[j]);
      PIN(A);
      FENCE();
      TOUCH(R[i]); THROTTLE(mask, i); FENCE();
      R[(i + LD) & 15] = i + LD < 16 ? kvload(rv, o0, i + LD) : kvload(rn, o1, i + LD - 16);
      if constexpr (i == 15) { now = clock10ns(); refv = flag_get(L_REF); }
      FENCE();
      acc = mfma(A, R[i], acc);
      FENCE(); });
    const float e0 = tr32(acc[0], acc[2]), e1 = tr32(acc[1], acc[3]);
    const unsigned hi = pk_bf16(e0, e1), lo = pk_bf16(e0 - bflo(hi), e1 - bfhi(hi));
    *(unsigned*)(lds + o_off) = hi;
    *(unsigned*)(lds + o_off + O_ROW) = lo;
    ORDER();
  }
  WG_BARRIER();
}

extern "C" void kernel_launch(void* const* d_in, const int* in_sizes, int n_in, void* d_out, int out_size, void* d_ws, size_t ws_size,
                              hipStream_t stream) {
  Params p;
  p.x = (const unsigned short*)d_in[0]; p.k = (const unsigned short*)d_in[1]; p.v = (const unsigned short*)d_in[2];
  p.wq = (const unsigned short*)d_in[3]; p.wo = (const unsigned short*)d_in[4];
  p.w1 = (const unsigned short*)d_in[6]; p.b1 = (const unsigned short*)d_in[7]; p.w2 = (const unsigned short*)d_in[8]; p.b2 = (const unsigned short*)d_in[9];
  p.out = (unsigned short*)d_out;
  p.ws = (unsigned char*)d_ws;
  k_fused<<<256, 512, 0, stream>>>(p);
}
```

```cpp
#include <hip/hip_runtime.h>
#include <hip/hip_bf16.h>
#include <stdint.h>
#include <utility>

typedef unsigned v2u __attribute__((ext_vector_type(2)));
typedef unsigned v4u __attribute__((ext_vector_type(4)));
typedef unsigned v8u __attribute__((ext_vector_type(8)));
typedef float f32x2 __attribute__((ext_vector_type(2)));
typedef float f32x4 __attribute__((ext_vector_type(4)));
typedef __bf16 bf16x2 __attribute__((ext_vector_type(2)));
typedef __bf16 bf16x8 __attribute__((ext_vector_type(8)));
typedef __bf16 bf16x16 __attribute__((ext_vector_type(16)));
typedef __amdgpu_buffer_rsrc_t rsrc_t;
typedef __attribute__((address_space(3))) unsigned lds_u32;

constexpr int NB = 4, DM = 512, NH = 32, SK = 4096, DH = 128, FF = 1792;
constexpr int NT = 2, SC1 = 16;
constexpr unsigned SENT = 0xFFFFFFFFu, OOB = 0x80000000u;
constexpr unsigned CAP_FAST = 1u << 19, CAP_SLOW = 1u << 16, CAP_LDS = 1u << 22;
constexpr float LOG2E = 1.4426950408889634f, C2 = 0.088388347648318440f * LOG2E;

constexpr int D0 = 7, N0 = 10, D1 = 5;
constexpr int NS0 = 18;
constexpr int NA = 40;
constexpr int NU = 48;
constexpr int LD = 8;
constexpr int DO = 3;
static_assert((NU + LD - NA) * 4 >= NU, "the backlog is gone before the loop starts");
constexpr int TAU_K = 228, TAU_V = 215;
constexpr int LEAD0 = 15, LEAD1 = 110, LEAD2 = 125;
constexpr int T_LAST = 1100;
constexpr int T_LOOK = 20;

template <class F, int... I> __device__ __forceinline__ void static_for_impl(F&& f, std::integer_sequence<int, I...>) { (f(std::integral_constant<int, I>{}), ...); }
template <int N, class F> __device__ __forceinline__ void static_for(F&& f) { static_for_impl(f, std::make_integer_sequence<int, N>{}); }

#define TOUCH(x) asm volatile("" :: "v"(x))
#define PIN(x) asm volatile("" : "+v"(x))
#define ORDER() asm volatile("" ::: "memory")
#define FENCE() __builtin_amdgcn_sched_barrier(0)
#define WG_BARRIER() asm volatile("s_waitcnt lgkmcnt(0)\n\ts_barrier" ::: "memory")
#define THROTTLE(mask, i) asm volatile("s_bitcmp1_b32 %0, %1\n\ts_cbranch_scc0 1f\n\ts_waitcnt vmcnt(3)\n1:" :: "s"(mask), "n"(i) : "scc")

__device__ __forceinline__ unsigned clock10ns() { return (unsigned)__builtin_amdgcn_s_memrealtime(); }
__device__ __forceinline__ float dot2(unsigned a, unsigned b, float acc) {
  return __builtin_amdgcn_fdot2_f32_bf16(__builtin_bit_cast(bf16x2, a), __builtin_bit_cast(bf16x2, b), acc, false);
}
__device__ __forceinline__ f32x4 mfma(v4u a, v4u b, f32x4 c) {
  return __builtin_amdgcn_mfma_f32_16x16x32_bf16(__builtin_bit_cast(bf16x8, a), __builtin_bit_cast(bf16x8, b), c, 0, 0, 0);
}
__device__ __forceinline__ f32x4 smfma(v4u a, v8u b, f32x4 c, unsigned idx) {
  return __builtin_amdgcn_smfmac_f32_16x16x64_bf16(__builtin_bit_cast(bf16x8, a), __builtin_bit_cast(bf16x16, b), c, (int)idx, 0, 0);
}
__device__ __forceinline__ float bflo(unsigned u) { return __uint_as_float(u << 16); }
__device__ __forceinline__ float bfhi(unsigned u) { return __uint_as_float(u & 0xffff0000u); }
__device__ __forceinline__ unsigned pk_bf16(float lo, float hi) { bf16x2 r; r.x = (__bf16)lo; r.y = (__bf16)hi; return __builtin_bit_cast(unsigned, r); }
__device__ __forceinline__ unsigned bf_bits(float x) { return (unsigned)__builtin_bit_cast(unsigned short, (__bf16)x); }
__device__ __forceinline__ float rbf(float x) { return bflo(bf_bits(x)); }
__device__ __forceinline__ float exp2_(float x) { return __builtin_amdgcn_exp2f(x); }
__device__ __forceinline__ float rcp_(float x) { return __builtin_amdgcn_rcpf(x); }

template <int CTRL> __device__ __forceinline__ float dpp(float x) { return __int_as_float(__builtin_amdgcn_mov_dpp(__float_as_int(x), CTRL, 0xf, 0xf, true)); }
constexpr int XOR1 = 0xB1, XOR2 = 0x4E, XOR7 = 0x141, XOR8 = 0x128, ROR4 = 0x124, BCAST = 0x150;
__device__ __forceinline__ float tr32(float a, float b) {
  auto s = __builtin_amdgcn_permlane32_swap(__float_as_uint(a), __float_as_uint(b), false, false);
  return __uint_as_float(s[0]) + __uint_as_float(s[1]);
}
__device__ __forceinline__ float tr16(float a, float b) {
  auto s = __builtin_amdgcn_permlane16_swap(__float_as_uint(a), __float_as_uint(b), false, false);
  return __uint_as_float(s[0]) + __uint_as_float(s[1]);
}
template <int CTRL> __device__ __forceinline__ float trd(float lo, float hi, bool up) {
  const float t1 = lo + dpp<CTRL>(lo), t2 = hi + dpp<CTRL>(hi);
  return up ? t2 : t1;
}
__device__ __forceinline__ float wave_sum(float x) {
  x += dpp<XOR1>(x); x += dpp<XOR2>(x); x += dpp<XOR7>(x); x += dpp<XOR8>(x);
  x = tr16(x, x);
  return tr32(x, x);
}
__device__ __forceinline__ float wave_max(float x) {
  x = fmaxf(x, dpp<XOR1>(x)); x = fmaxf(x, dpp<XOR2>(x)); x = fmaxf(x, dpp<XOR7>(x)); x = fmaxf(x, dpp<XOR8>(x));
  auto s = __builtin_amdgcn_permlane16_swap(__float_as_uint(x), __float_as_uint(x), false, false);
  x = fmaxf(__uint_as_float(s[0]), __uint_as_float(s[1]));
  auto t = __builtin_amdgcn_permlane32_swap(__float_as_uint(x), __float_as_uint(x), false, false);
  return fmaxf(__uint_as_float(t[0]), __uint_as_float(t[1]));
}
__device__ __forceinline__ float sum4_parity(f32x4 v) {
  float s = tr16(tr32(v[0], v[2]), tr32(v[1], v[3]));
  s += dpp<XOR8>(s); s += dpp<ROR4>(s); s += dpp<XOR2>(s);
  return s;
}

__device__ __forceinline__ rsrc_t mk_rsrc(const void* p, unsigned bytes) { return __builtin_amdgcn_make_buffer_rsrc(const_cast<void*>(p), 0, bytes, 0x00020000); }
template <int AUX> __device__ __forceinline__ v4u ld128(rsrc_t r, unsigned v, unsigned s) { return __builtin_amdgcn_raw_buffer_load_b128(r, v, s, AUX); }
template <int AUX> __device__ __forceinline__ v2u ld64(rsrc_t r, unsigned v, unsigned s) { return __builtin_amdgcn_raw_buffer_load_b64(r, v, s, AUX); }
__device__ __forceinline__ void st128(v4u d, rsrc_t r, unsigned off) { __builtin_amdgcn_raw_buffer_store_b128(d, r, off, 0, SC1); }
__device__ __forceinline__ void st64(v2u d, rsrc_t r, unsigned off) { __builtin_amdgcn_raw_buffer_store_b64(d, r, off, 0, SC1); }
__device__ __forceinline__ unsigned tag1(float v, unsigned tag) { return ((__float_as_uint(v) + 0x800u) & 0xfffff000u) | (tag & 0xfffu); }
__device__ __forceinline__ v4u tag4(f32x4 v, unsigned tag) { return v4u{tag1(v[0], tag), tag1(v[1], tag >> 12), tag1(v[2], tag), tag1(v[3], tag >> 12)}; }
__device__ __forceinline__ f32x4 untag4(v4u g, unsigned tag, unsigned& bad) {
  const unsigned lo = tag & 0xfffu, hi = (tag >> 12) & 0xfffu;
  g ^= v4u{lo, hi, lo, hi};
  bad |= (g[0] | g[1]) | (g[2] | g[3]);
  return __builtin_bit_cast(f32x4, g);
}
__device__ __forceinline__ bool all_fresh(unsigned bad) { return !__any((bad & 0xfffu) != 0u); }

__device__ __forceinline__ float row_sum(float x) { x += dpp<XOR1>(x); x += dpp<XOR2>(x); x = tr16(x, x); return tr32(x, x); }
__device__ __forceinline__ void ln_stats(const v4u (&x)[4], float& mean, float& inv) {
  float s = 0.f;
#pragma unroll
  for (int t = 0; t < 4; ++t)
#pragma unroll
    for (int j = 0; j < 4; ++j) s += bflo(x[t][j]) + bfhi(x[t][j]);
  const float mu = row_sum(s) * (1.0f / DM);
  float q = 0.f;
#pragma unroll
  for (int t = 0; t < 4; ++t)
#pragma unroll
    for (int j = 0; j < 4; ++j) { const float d0 = bflo(x[t][j]) - mu, d1 = bfhi(x[t][j]) - mu; q = fmaf(d0, d0, q); q = fmaf(d1, d1, q); }
  const float var = row_sum(q) * (1.0f / DM);
  mean = rbf(mu);
  inv = rcp_(rbf(__builtin_amdgcn_sqrtf(rbf(rbf(var) + 1e-5f))));
}
__device__ __forceinline__ v4u ln_apply(v4u x, float mean, float inv) {
  v4u o;
#pragma unroll
  for (int j = 0; j < 4; ++j) { const unsigned d = pk_bf16(bflo(x[j]) - mean, bfhi(x[j]) - mean); o[j] = pk_bf16(bflo(d) * inv, bfhi(d) * inv); }
  return o;
}

template <int J, class T> __device__ __forceinline__ v4u bfrag(const T* t) {
  v4u f;
#pragma unroll
  for (int d = 0; d < 4; ++d) f[d] = __builtin_amdgcn_perm(t[2 * d + 1][J >> 1], t[2 * d][J >> 1], (J & 1) ? 0x07060302u : 0x05040100u);
  return f;
}
template <int LO, int HI> __device__ __forceinline__ void tile_load(v2u (&u)[32], rsrc_t r, unsigned voff, unsigned stride) {
#pragma unroll
  for (int n = LO; n < HI; ++n) u[n] = ld64<NT>(r, voff, n * stride);
  FENCE();
}
__device__ __forceinline__ f32x4 tile_mul(v4u a, const v2u (&u)[32]) {
  f32x4 o = {0.f, 0.f, 0.f, 0.f};
  static_for<16>([&](auto N) { constexpr int s = N >> 2, j = N & 3; o[j] += mfma(a, bfrag<j>(u + 8 * s), f32x4{0.f, 0.f, 0.f, 0.f})[s]; });
  return o;
}

constexpr unsigned WS_E = 0;
constexpr unsigned WS_LS = 4096;
constexpr unsigned WS_CK = 32768;
constexpr unsigned WS_X1 = 65536;
constexpr unsigned WS_MP = WS_X1 + 32 * 4096;
constexpr unsigned WS_TB = WS_MP + 16 * 8192;
constexpr unsigned WS_QX = WS_TB + 4 * 64 * 2048;
constexpr unsigned WS_BYTES = WS_QX + 32 * 8192;
constexpr int L_Q = 0, L_M = 256, L_F = 272, L_TAG = 288, L_REF = 292, L_RDY = 296, L_T0 = 300, L_T = 304, L_GX = 320,
              L_H = 1024, L_O = 2048, O_ROW = 144, L_S = L_O + 16 * O_ROW, L_BYTES = L_S + 8192;

struct Params {
  const unsigned short* x; const unsigned short* k; const unsigned short* v; const unsigned short* wq; const unsigned short* wo;
  const unsigned short* w1; const unsigned short* b1; const unsigned short* w2; const unsigned short* b2;
  unsigned short* out; unsigned char* ws;
};

__global__ void __launch_bounds__(512, 2) k_fused(Params p) {
  __shared__ __attribute__((aligned(16))) unsigned char lds[L_BYTES];
  auto flag_get = [&](int off) { return __hip_atomic_load((lds_u32*)lds + off / 4, __ATOMIC_RELAXED, __HIP_MEMORY_SCOPE_WORKGROUP); };
  auto flag_set = [&](int off, unsigned v) { __hip_atomic_store((lds_u32*)lds + off / 4, v, __ATOMIC_RELAXED, __HIP_MEMORY_SCOPE_WORKGROUP); };
  const int blk = blockIdx.x, lane = threadIdx.x & 63, w = __builtin_amdgcn_readfirstlane(threadIdx.x >> 6);
  const int idx = blk >> 3, h = (blk & 7) * 4 + (idx & 3), bs = idx >> 2, b = bs >> 1, sp = bs & 1;
  const int r = lane >> 4, c = lane & 15;
  const unsigned l16 = lane * 16;
  unsigned kvoff = ((b * NH + h) * SK + sp * 2048) * (DH * 2), kx = (w & 3) * 8192 + bs * 65536;
  asm volatile("" : "+s"(kvoff), "+s"(kx));
  asm volatile("" :: "s"(p.x), "s"(p.k), "s"(p.v), "s"(p.wq), "s"(p.wo), "s"(p.w1), "s"(p.b1), "s"(p.w2), "s"(p.b2), "s"(p.out), "s"(p.ws));
  const unsigned rot = b * 32 + sp * 4;

  if (w >= 4) {
    const int a = w - 4, prod = 2 * h + sp;
    *(v4u*)(lds + L_M) = v4u{SENT, SENT, SENT, SENT}; *(v4u*)(lds + L_TAG) = v4u{0u, 0u, 0u, 0u};
    if (a != 0) WG_BARRIER();
    FENCE();
    const rsrc_t rws = mk_rsrc(p.ws, WS_BYTES), rx = mk_rsrc(p.x, NB * DM * 2);
    const unsigned xoff = ((lane >> 2) & 3) * (DM * 2) + ((lane & 3) + 4 * r) * 16;
    if (a == 0) {
      v4u xs[4]; v2u U[32];
#pragma unroll
      for (int t = 0; t < 4; ++t) xs[t] = ld128<0>(rx, xoff, ((t + b) & 3) * 256);
      FENCE();
      tile_load<0, 32>(U, mk_rsrc(p.wq + (size_t)b * 128 * (NH * DH) + h * DH + sp * 64, 128 * NH * DH * 2), r * 32 * 8192 + c * 8, 8192);
      const unsigned ep = __builtin_amdgcn_raw_buffer_load_b32(rws, 0, WS_E, SC1);
      FENCE();
      WG_BARRIER();
      float mean, inv;
      ln_stats(xs, mean, inv);
      const f32x4 g = tile_mul(ln_apply(xs[0], mean, inv), U);
      FENCE();
      const unsigned tg = __builtin_amdgcn_readfirstlane(ep) * 0x9E3779B1u + 0x7F4A7C15u;
      st128(tag4(g, tg), rws, WS_QX + h * 8192 + bs * 1024 + l16);
      asm volatile("s_waitcnt vmcnt(0)" ::: "memory");
      __builtin_amdgcn_s_sleep(16);
      f32x4 q4;
#pragma unroll 1
      for (unsigned spin = 0; spin < CAP_FAST; ++spin) {
        const v4u G0 = ld128<SC1>(rws, r * 1024 + c * 16, WS_QX + h * 8192 + b * 256), G1 = ld128<SC1>(rws, r * 1024 + c * 16, WS_QX + h * 8192 + b * 256 + 4096);
        unsigned bad = 0;
        q4 = untag4(G0, tg, bad); q4 += untag4(G1, tg, bad);
        if (all_fresh(bad)) break;
        ORDER();
      }
      *(unsigned*)(lds + L_Q + (lane & 31) * 8 + (lane >> 5) * 4) = pk_bf16(tr32(q4[0], q4[2]), tr32(q4[1], q4[3]));
      *(unsigned*)(lds + L_TAG) = tg;
      ORDER();
      flag_set(L_RDY, 1u);
    }
#pragma unroll 1
    for (unsigned spin = 0; spin < CAP_LDS; ++spin) {
      if (__builtin_amdgcn_readfirstlane(flag_get(L_RDY))) break;
      __builtin_amdgcn_s_sleep(4);
    }
    ORDER();
    const unsigned tag = __builtin_amdgcn_readfirstlane(*(const unsigned*)(lds + L_TAG));

    if (a == 2 && h < 28) {
      const int cg = (h & 1) * 4 + b;
      const unsigned stride = sp ? DM * 2 : FF * 2;
      const rsrc_t rt = mk_rsrc(sp ? p.w2 + (size_t)(h >> 1) * 128 * DM + cg * 64 : p.w1 + (size_t)b * 128 * FF + h * 64, 128 * stride);
      v2u U[32]; v4u A;
      tile_load<0, 16>(U, rt, r * 32 * stride + c * 8, stride);
      TOUCH(U[15]); FENCE();
      tile_load<16, 32>(U, rt, r * 32 * stride + c * 8, stride);
      if (sp == 0) {
        v4u xs[4];
#pragma unroll
        for (int t = 0; t < 4; ++t) xs[t] = ld128<0>(rx, xoff, ((t + b) & 3) * 256);
#pragma unroll 1
        for (int it = 0; it < 2; ++it) {
          float mean, inv;
          ln_stats(xs, mean, inv);
#pragma unroll
          for (int t = 0; t < 4; ++t) xs[t] = ln_apply(xs[t], mean, inv);
        }
        A = xs[0];
      } else {
        v2u b1v[2];
#pragma unroll
        for (int k = 0; k < 2; ++k) b1v[k] = ld64<0>(mk_rsrc(p.b1 + (h >> 1) * 128, 256), c * 8, k * 128);
        f32x4 z4[2];
#pragma unroll 1
        for (unsigned spin = 0; spin < CAP_SLOW; ++spin) {
          __builtin_amdgcn_s_sleep(112); __builtin_amdgcn_s_sleep(112);
          v4u G[8];
#pragma unroll
          for (int j = 0; j < 8; ++j) G[j] = ld128<SC1>(rws, l16, WS_X1 + (h >> 1) * 8192 + j * 1024);
          unsigned bad = 0;
#pragma unroll
          for (int j = 0; j < 8; ++j) { const f32x4 v = untag4(G[j], tag, bad); z4[j >> 2] = (j & 3) ? z4[j >> 2] + v : v; }
          if (all_fresh(bad)) break;
          ORDER();
        }
#pragma unroll
        for (int k = 0; k < 2; ++k) {
          float hv[4];
#pragma unroll
          for (int j = 0; j < 4; ++j) {
            const float z = rbf(rbf(z4[k][j]) + ((j & 1) ? bfhi(b1v[k][j >> 1]) : bflo(b1v[k][j >> 1])));
            hv[j] = z * rcp_(1.0f + exp2_((-2.0f * 0.7978845608028654f * LOG2E) * (z + 0.044715f * z * z * z)));
          }
          *(v2u*)(lds + L_H + r * 256 + k * 128 + c * 8) = v2u{pk_bf16(hv[0], hv[1]), pk_bf16(hv[2], hv[3])};
        }
        asm volatile("s_waitcnt lgkmcnt(0)" ::: "memory");
        A = *(const v4u*)(lds + L_H + ((lane >> 2) & 3) * 256 + ((lane & 3) + 4 * r) * 16);
      }
      st128(tag4(tile_mul(A, U), tag), rws, (sp ? WS_MP + (h >> 1) * 8192 + cg * 1024 : WS_X1 + h * 4096 + b * 1024) + l16);
    }

    const unsigned ck_st = (lane == 0 && (idx & 3) == 0) ? WS_CK + (bs * 8 + (blk & 7)) * 8 : OOB;
    unsigned t0 = 0;
    if (a == 3) {
#pragma unroll 1
      for (unsigned spin = 0; spin < CAP_LDS; ++spin) {
        t0 = __builtin_amdgcn_readfirstlane(flag_get(L_T0));
        if (t0) break;
        __builtin_amdgcn_s_sleep(4);
      }
      st64(v2u{t0, tag}, rws, ck_st);
    }

    v8u Bf[2][8];
    {
      const rsrc_t ro = mk_rsrc(p.wo + (size_t)h * DH * DM + a * 128, DH * DM * 2);
      const unsigned vo = r * 8 * 1024 + c * 16;
      v4u T[32]; v2u ck;
      static_for<32 + DO>([&](auto N) { constexpr int n = N;
        if constexpr (n >= DO) TOUCH(T[n - DO]);
        FENCE();
        if constexpr (n == 10) ck = ld64<SC1>(rws, a == 3 ? lane * 8 : OOB, WS_CK);
        if constexpr (n == 10 + DO) { TOUCH(ck);
          if (a == 3) {
            const unsigned tn = clock10ns();
            const float dt = wave_sum((float)(int)((ck[1] == tag ? ck[0] : tn) - t0)) * (1.0f / 64);
            flag_set(L_REF, (t0 + (int)dt + (128 + LD - NA) * TAU_K / 16 - LEAD0) | 1u); } }
        FENCE();
        if constexpr (n < 32) T[n] = ld128<0>(ro, vo, (((n >> 3) * 32 + (n & 7)) ^ rot) * 1024);
        FENCE();
        if constexpr (n >= DO && ((n - DO) & 7) == 7) { constexpr int s = (n - DO) >> 3;
          static_for<8>([&](auto J) { constexpr int j = J; const v4u f = bfrag<j>(T + 8 * s);
            if constexpr (s & 1) Bf[s >> 1][j].hi = f; else Bf[s >> 1][j].lo = f;
            PIN(Bf[s >> 1][j]); }); }
        FENCE(); });
    }

    const int cons = (blk & 7) * 8 + (idx & 7), col0 = 32 * (cons >> 2) + 4 * (cons & 1) + (cons & 2);
    const unsigned co = (lane & 1) * 32 + (r >> 1) * 16 + (r & 1) * 2;
    if (a == 3) {
      unsigned mb;
#pragma unroll 1
      for (unsigned spin = 0; spin < CAP_LDS; ++spin) {
        mb = flag_get(L_M + 4 * (lane & 3));
        if (__all(mb != SENT)) break;
        __builtin_amdgcn_s_sleep(4);
      }
      ORDER();
      const float m4 = __uint_as_float(mb);
      float M = fmaxf(m4, dpp<XOR1>(m4)); M = fmaxf(M, dpp<XOR2>(M));
      float L = 0.f;
#pragma unroll
      for (int i = 0; i < 8; ++i) { const f32x4 s4 = *(const f32x4*)(lds + L_S + i * 1024 + l16);
#pragma unroll
        for (int j = 0; j < 4; ++j) L += exp2_(s4[j] - M); }
      L = wave_sum(L);
      const float lse = M + __builtin_amdgcn_logf(L);
      const unsigned tk = flag_get(L_T);
      st128(v4u{__float_as_uint(lse), tag, tk, tag}, rws, lane == 0 ? WS_LS + (b * 64 + prod) * 16 : OOB);
      const unsigned long long need = blk == 0 ? ~0ull : 1ull << (prod ^ 1);
      float other; unsigned mean;
#pragma unroll 1
      for (unsigned spin = 0; spin < CAP_SLOW; ++spin) {
        __builtin_amdgcn_s_sleep(80);
        const v4u m = ld128<SC1>(rws, l16, WS_LS + b * 1024);
        const bool ok = ((m[1] ^ tag) | (m[3] ^ tag)) == 0u;
        const unsigned tn = clock10ns();
        const float dt = __builtin_amdgcn_fmed3f(wave_sum((float)(int)((ok ? m[2] : tn) - tk)) * (1.0f / 64), -1000.f, 1000.f);
        mean = __builtin_amdgcn_readfirstlane(tk + (int)dt);
        flag_set(L_REF, (mean - LEAD1) | 1u);
        other = __uint_as_float(__builtin_amdgcn_readlane(m[0], prod ^ 1));
        if ((__ballot(ok) & need) == need) break;
        ORDER();
      }
      *(float*)(lds + L_F + 4 * (lane & 3)) = exp2_(m4 - M) * rcp_(L) * rcp_(1.0f + exp2_(other - lse));
      __builtin_amdgcn_raw_buffer_store_b32(tag, rws, (blk == 0 && lane == 0) ? WS_E : OOB, 0, SC1);
      while ((int)(clock10ns() - mean) < T_LAST) __builtin_amdgcn_s_sleep(2);
      st64(v2u{flag_get(L_T), tag}, rws, ck_st + 512);
      __builtin_amdgcn_s_sleep(36);
      const v2u ck = ld64<SC1>(rws, lane * 8, WS_CK + 512);
      const bool ok = ck[1] == tag;
      const int dt = (int)(wave_sum(ok ? (float)(int)(ck[0] - mean) : 0.f) * rcp_(fmaxf(wave_sum(ok ? 1.f : 0.f), 1.f)));
      flag_set(L_REF, (mean + dt - LEAD2) | 1u);
    } else if (a == 2) {
      const unsigned b2v = __builtin_amdgcn_raw_buffer_load_b16(mk_rsrc(p.b2, DM * 2), co, col0 * 2, 0);
      const unsigned xv = __builtin_amdgcn_raw_buffer_load_b16(rx, co, (b * DM + col0) * 2, 0);
      const unsigned mo = lane < 28 ? (unsigned)((lane >> 1) * 8192 + (lane & 1) * 64) : OOB, ms = WS_MP + (col0 >> 6) * 1024 + b * 256 + (col0 & 63) * 4;
      f32x4 m4;
#pragma unroll 1
      for (unsigned spin = 0; spin < CAP_SLOW; ++spin) {
        __builtin_amdgcn_s_sleep(64);
        const v2u G0 = ld64<SC1>(rws, mo, ms), G1 = ld64<SC1>(rws, mo, ms + 32);
        unsigned bad = 0;
        m4 = untag4(v4u{G0[0], G0[1], G1[0], G1[1]}, lane < 28 ? tag : 0u, bad);
        if (all_fresh(bad)) break;
        ORDER();
      }
      const float m = rbf(rbf(sum4_parity(m4)) + bflo(b2v));
      *(f32x2*)(lds + L_GX + (r * 2 + (lane & 1)) * 8) = f32x2{rbf(m * rcp_(1.0f + exp2_(-LOG2E * m))), bflo(xv)};
    }

    unsigned a_off = L_O + c * O_ROW + r * 16, sidx = (lane & 2) ? 0xeeeeu : 0x4444u;
    unsigned tb_off = WS_TB + (b * 64 + prod) * 2048 + a * 512 + (c >> 2) * 128 + r * 32 + (c & 3) * 8;
    unsigned go = (lane >> 1) * 2048 + (lane & 1) * 16, out_off = (lane & 14) == 0 ? (b * DM + col0) * 2 + co : OOB;
    f32x4 d[8];
#pragma unroll
    for (int j = 0; j < 8; ++j) { d[j] = f32x4{0.f, 0.f, 0.f, 0.f}; PIN(d[j]); }
    PIN(a_off); PIN(sidx); PIN(tb_off); PIN(go); PIN(out_off);
    asm volatile("s_branch 1f\n\t.p2align 11\n1:");
    WG_BARRIER();
    const v4u A0 = *(const v4u*)(lds + a_off), A1 = *(const v4u*)(lds + a_off + 64);
    const float f = *(const float*)(lds + L_F + r * 4);
    FENCE();
    float t[8];
    auto mul = [&](int j) { d[j] = smfma(A0, Bf[0][j], d[j], sidx); d[j + 4] = smfma(A0, Bf[0][j + 4], d[j + 4], sidx);
      d[j] = smfma(A1, Bf[1][j], d[j], sidx); d[j + 4] = smfma(A1, Bf[1][j + 4], d[j + 4], sidx); };
    auto sum = [&](int j) { const f32x2 u2 = d[j].lo + d[j].hi; t[j] = (u2[0] + u2[1]) * f; PIN(t[j]); };
    mul(0); mul(2); mul(1); mul(3);
    sum(0); sum(4); sum(2); sum(6);
    unsigned w0 = tag1(tr32(tr16(t[0], t[4]), tr16(t[2], t[6])), tag); PIN(w0);
    sum(1); sum(5);
    float x1 = tr16(t[1], t[5]); PIN(x1);
    sum(3); sum(7);
    const unsigned w1 = tag1(tr32(x1, tr16(t[3], t[7])), tag >> 12);
    __builtin_amdgcn_sched_group_barrier(0x8, 7, 0);
#pragma unroll
    for (int k = 0; k < 9; ++k) { __builtin_amdgcn_sched_group_barrier(0x2, 3, 0); __builtin_amdgcn_sched_group_barrier(0x8, 1, 0); }
    FENCE();
    st64(v2u{w0, w1}, rws, tb_off);
    if (a == 0) {
      const f32x2 gx = *(const f32x2*)(lds + L_GX + (r * 2 + (lane & 1)) * 8);
      const unsigned t1 = clock10ns();
      const int wait = (int)(__builtin_amdgcn_readfirstlane(flag_get(L_REF)) + LEAD2 + 8 * TAU_V + T_LOOK - t1);
      __builtin_amdgcn_s_sleep(4);
      while ((int)(clock10ns() - t1) < (wait < 120 ? wait : 120)) {}
      const rsrc_t rout = mk_rsrc(p.out, NB * DM * 2);
      unsigned o;
#pragma unroll 1
      for (unsigned spin = 0; spin < CAP_FAST; ++spin) {
        const v4u G0 = ld128<SC1>(rws, go, WS_TB + b * 131072 + cons * 32), G1 = ld128<SC1>(rws, go, WS_TB + b * 131072 + 65536 + cons * 32);
        unsigned bad = 0;
        f32x4 s4 = untag4(G0, tag, bad); s4 += untag4(G1, tag, bad);
        o = bf_bits(gx[1] + rbf(rbf(sum4_parity(s4)) * gx[0]));
        PIN(o);
        if (all_fresh(bad)) break;
        ORDER();
      }
      __builtin_amdgcn_raw_buffer_store_b16((unsigned short)o, rout, out_off, 0, 0);
    }
    return;
  }

  constexpr unsigned KVLEN = 2048 * DH * 2;
  const char* kp = (const char*)p.k + kvoff; const char* vp = (const char*)p.v + kvoff;
  const rsrc_t rk = mk_rsrc(kp, KVLEN);
  auto kvload = [&](rsrc_t rs, unsigned o, int i) { return ld128<NT>(rs, l16 + (i & 3) * 1024, o ^ (((i >> 2) & 1) * 4096 + (i >> 3) * 32768)); };
  float* const scs = (float*)(lds + L_S + w * 2048) + lane;
  v4u S[NU + LD], q;
  unsigned now, refv;
  unsigned mask = 0x1249u, org = 0;
  auto pace = [&](int sched) {
    const unsigned rf = __builtin_amdgcn_readfirstlane(refv);
    org = now - sched + ((mask & 1) ? 0 : TAU_V / 4);
    int lv = (int)(org - rf) >> 5;
    asm volatile("" : "+s"(lv));
    lv = rf ? lv : 3;
    unsigned m = 0xffffu; m = lv > 0 ? 0x5555u : m; m = lv > 1 ? 0x1111u : m; m = lv > 2 ? 0x1249u : m;
    m = lv > 3 ? 0x0421u : m; m = lv > 4 ? 0x0101u : m; m = lv > 5 ? 0u : m;
    mask = __builtin_amdgcn_readfirstlane(m); };
  float ka[8], mw = -INFINITY;
  auto kdot = [&](v4u t, int i) {
    float d = dot2(t[0], q[0], 0.f); d = dot2(t[1], q[1], d); d = dot2(t[2], q[2], d); d = dot2(t[3], q[3], d);
    if (i >= 8) { i -= 8; d = trd<XOR8>(ka[i], d, lane & 8);
      if (i >= 4) { i -= 4; d = trd<XOR7>(ka[i], d, lane & 4);
        if (i >= 2) { i -= 2; d = trd<XOR2>(ka[i], d, lane & 2);
          if (i >= 1) { i -= 1; d = trd<XOR1>(ka[i], d, lane & 1); } } } }
    PIN(d); ka[i] = d; };
  auto kdone = [&](int g) { const float s = ka[0] * C2; scs[g * 64] = s; mw = fmaxf(mw, s); PIN(mw); };

  static_for<NU + LD>([&](auto N) {
    constexpr int n = N, dn = n < N0 ? D0 : D1;
    if constexpr (n >= dn) TOUCH(S[n - dn]);
    FENCE();
    S[n] = kvload(rk, kx, n);
    if constexpr (n == 0) __builtin_amdgcn_s_setprio(2);
    if constexpr (n == NS0) { FENCE(); WG_BARRIER(); }
    FENCE();
    if constexpr (n == NA - 1) {
#pragma unroll 1
      for (unsigned spin = 0; spin < CAP_LDS; ++spin) {
        const unsigned rdy = flag_get(L_RDY);
        ORDER();
        q = *(const v4u*)(lds + L_Q + c * 16);
        if (__builtin_amdgcn_readfirstlane(rdy)) break;
        __builtin_amdgcn_s_sleep(1);
      }
      now = clock10ns();
    }
    if constexpr (n == NA) flag_set(L_T0, now | 1u);
    if constexpr (n >= NA) {
      constexpr int hi = (n - NA + 1) * 4 < n - LD + 1 ? (n - NA + 1) * 4 : n - LD + 1;
      constexpr int lo = (n - NA) * 4 < n - LD ? (n - NA) * 4 : n - LD;
      static_for<hi - lo>([&](auto J) { constexpr int i = lo + J; kdot(S[i], i & 15); if constexpr ((i & 15) == 15) kdone(i >> 4); });
    }
    FENCE();
  });
  v4u R[16];
#pragma unroll
  for (int i = 0; i < LD; ++i) R[i] = S[NU + i];
  float snext = scs[0];
  v4u sel;
#pragma unroll
  for (int j = 0; j < 4; ++j) { const unsigned pick = (c & 8) ? 0x0302u : 0x0100u;
    sel[j] = ((c >> 1) & 3) != j ? 0x0c0c0c0cu : (c & 1) ? (pick << 16) | 0x0c0cu : 0x0c0c0000u | pick; }
  unsigned o_off = L_O + (4 * w + (r & 2)) * O_ROW + ((c * 8 + (r & 1) * 4) ^ rot);
  PIN(o_off); PIN(sel);
#pragma unroll 1
  for (int g = NU / 16; g < 8; ++g) {
    const rsrc_t rn = mk_rsrc(g == 7 ? vp : kp, KVLEN);
    const unsigned o0 = (g * 65536) ^ kx, o1 = (((g + 1) & 7) * 65536) ^ kx;
    static_for<16>([&](auto I) { constexpr int i = I;
      TOUCH(R[i]); THROTTLE(mask, i); FENCE();
      R[(i + LD) & 15] = i + LD < 16 ? kvload(rk, o0, i + LD) : kvload(rn, o1, i + LD - 16);
      if constexpr (i == 0) { now = clock10ns(); refv = flag_get(L_REF); }
      if constexpr (i == 2) pace((g - 8) * TAU_K);
      FENCE();
      kdot(R[i], i);
      FENCE(); });
    kdone(g);
  }
  mw = wave_max(mw);
  flag_set(L_T + 4 * w, org);
  ORDER();
  flag_set(L_M + 4 * w, min(__float_as_uint(mw), SENT - 1u));
  f32x4 acc = {0.f, 0.f, 0.f, 0.f};
  const rsrc_t rv = mk_rsrc(vp, KVLEN);
#pragma unroll 1
  for (int g = 0; g < 8; ++g) {
    const rsrc_t rn = mk_rsrc(vp, g == 7 ? 0u : KVLEN);
    const unsigned o0 = (g * 65536) ^ kx, o1 = (((g + 1) & 7) * 65536) ^ kx;
    const float pg = exp2_(snext - mw), ph = rbf(pg);
    const unsigned pp = pk_bf16(ph, pg - ph);
    snext = scs[((g + 1) & 7) * 64];
    static_for<16>([&](auto I) { constexpr int i = I;
      const unsigned pb = __builtin_amdgcn_mov_dpp(pp, BCAST + i, 0xf, 0xf, true);
      v4u A;
#pragma unroll
      for (int j = 0; j < 4; ++j) A[j] = __builtin_amdgcn_perm(pb, pb, sel[j]);
      PIN(A);
      FENCE();
      TOUCH(R[i]); THROTTLE(mask, i); FENCE();
      R[(i + LD) & 15] = i + LD < 16 ? kvload(rv, o0, i + LD) : kvload(rn, o1, i + LD - 16);
      if constexpr (i == 0) { now = clock10ns(); refv = flag_get(L_REF); }
      if constexpr (i == 2) { pace(g * TAU_V); mask &= g == 7 ? 0xffu : 0xffffu; flag_set(L_T + 4 * w, org); }
      FENCE();
      acc = mfma(A, R[i], acc);
      FENCE(); });
    const float e0 = tr32(acc[0], acc[2]), e1 = tr32(acc[1], acc[3]);
    const unsigned hi = pk_bf16(e0, e1), lo = pk_bf16(e0 - bflo(hi), e1 - bfhi(hi));
    *(unsigned*)(lds + o_off) = hi;
    *(unsigned*)(lds + o_off + O_ROW) = lo;
    ORDER();
  }
  WG_BARRIER();
}

extern "C" void kernel_launch(void* const* d_in, const int* in_sizes, int n_in, void* d_out, int out_size, void* d_ws, size_t ws_size,
                              hipStream_t stream) {
  Params p;
  p.x = (const unsigned short*)d_in[0]; p.k = (const unsigned short*)d_in[1]; p.v = (const unsigned short*)d_in[2];
  p.wq = (const unsigned short*)d_in[3]; p.wo = (const unsigned short*)d_in[4];
  p.w1 = (const unsigned short*)d_in[6]; p.b1 = (const unsigned short*)d_in[7]; p.w2 = (const unsigned short*)d_in[8]; p.b2 = (const unsigned short*)d_in[9];
  p.out = (unsigned short*)d_out;
  p.ws = (unsigned char*)d_ws;
  k_fused<<<256, 512, 0, stream>>>(p);
}
```

```cpp
#include <hip/hip_runtime.h>
#include <hip/hip_bf16.h>
#include <stdint.h>
#include <utility>

typedef unsigned v2u __attribute__((ext_vector_type(2)));
typedef unsigned v4u __attribute__((ext_vector_type(4)));
typedef unsigned v8u __attribute__((ext_vector_type(8)));
typedef float f32x2 __attribute__((ext_vector_type(2)));
typedef float f32x4 __attribute__((ext_vector_type(4)));
typedef __bf16 bf16x2 __attribute__((ext_vector_type(2)));
typedef __bf16 bf16x8 __attribute__((ext_vector_type(8)));
typedef __bf16 bf16x16 __attribute__((ext_vector_type(16)));
typedef __amdgpu_buffer_rsrc_t rsrc_t;
typedef __attribute__((address_space(3))) unsigned lds_u32;

constexpr int NB = 4, DM = 512, NH = 32, SK = 4096, DH = 128, FF = 1792;
constexpr int NT = 2, SC1 = 16;
constexpr unsigned SENT = 0xFFFFFFFFu, OOB = 0x80000000u;
constexpr unsigned CAP_FAST = 1u << 19, CAP_SLOW = 1u << 16, CAP_LDS = 1u << 22;
constexpr float LOG2E = 1.4426950408889634f, C2 = 0.088388347648318440f * LOG2E;

constexpr int D0 = 7, N0 = 10, D1 = 5;
constexpr int NS0 = 18;
constexpr int NA = 40;
constexpr int NU = 48;
constexpr int LD = 8;
constexpr int DO = 3;
static_assert((NU + LD - NA) * 4 >= NU, "the backlog is gone before the loop starts");
constexpr int TAU_K = 228, TAU_V = 215;
constexpr int LEAD0 = 15, LEAD1 = 110, LEAD2 = 125;
constexpr int T_LAST = 1100;
constexpr int T_LOOK = 20;

template <class F, int... I> __device__ __forceinline__ void static_for_impl(F&& f, std::integer_sequence<int, I...>) { (f(std::integral_constant<int, I>{}), ...); }
template <int N, class F> __device__ __forceinline__ void static_for(F&& f) { static_for_impl(f, std::make_integer_sequence<int, N>{}); }

#define TOUCH(x) asm volatile("" :: "v"(x))
#define PIN(x) asm volatile("" : "+v"(x))
#define ORDER() asm volatile("" ::: "memory")
#define FENCE() __builtin_amdgcn_sched_barrier(0)
#define WG_BARRIER() asm volatile("s_waitcnt lgkmcnt(0)\n\ts_barrier" ::: "memory")
#define THROTTLE(mask, i) asm volatile("s_bitcmp1_b32 %0, %1\n\ts_cbranch_scc0 1f\n\ts_waitcnt vmcnt(3)\n1:" :: "s"(mask), "n"(i) : "scc")

__device__ __forceinline__ unsigned clock10ns() { return (unsigned)__builtin_amdgcn_s_memrealtime(); }
__device__ __forceinline__ float dot2(unsigned a, unsigned b, float acc) {
  return __builtin_amdgcn_fdot2_f32_bf16(__builtin_bit_cast(bf16x2, a), __builtin_bit_cast(bf16x2, b), acc, false);
}
__device__ __forceinline__ f32x4 mfma(v4u a, v4u b, f32x4 c) {
  return __builtin_amdgcn_mfma_f32_16x16x32_bf16(__builtin_bit_cast(bf16x8, a), __builtin_bit_cast(bf16x8, b), c, 0, 0, 0);
}
__device__ __forceinline__ f32x4 smfma(v4u a, v8u b, f32x4 c, unsigned idx) {
  return __builtin_amdgcn_smfmac_f32_16x16x64_bf16(__builtin_bit_cast(bf16x8, a), __builtin_bit_cast(bf16x16, b), c, (int)idx, 0, 0);
}
__device__ __forceinline__ float bflo(unsigned u) { return __uint_as_float(u << 16); }
__device__ __forceinline__ float bfhi(unsigned u) { return __uint_as_float(u & 0xffff0000u); }
__device__ __forceinline__ unsigned pk_bf16(float lo, float hi) { bf16x2 r; r.x = (__bf16)lo; r.y = (__bf16)hi; return __builtin_bit_cast(unsigned, r); }
__device__ __forceinline__ unsigned bf_bits(float x) { return (unsigned)__builtin_bit_cast(unsigned short, (__bf16)x); }
__device__ __forceinline__ float rbf(float x) { return bflo(bf_bits(x)); }
__device__ __forceinline__ float exp2_(float x) { return __builtin_amdgcn_exp2f(x); }
__device__ __forceinline__ float rcp_(float x) { return __builtin_amdgcn_rcpf(x); }

template <int CTRL> __device__ __forceinline__ float dpp(float x) { return __int_as_float(__builtin_amdgcn_mov_dpp(__float_as_int(x), CTRL, 0xf, 0xf, true)); }
constexpr int XOR1 = 0xB1, XOR2 = 0x4E, XOR7 = 0x141, XOR8 = 0x128, ROR4 = 0x124, BCAST = 0x150;
__device__ __forceinline__ float tr32(float a, float b) {
  auto s = __builtin_amdgcn_permlane32_swap(__float_as_uint(a), __float_as_uint(b), false, false);
  return __uint_as_float(s[0]) + __uint_as_float(s[1]);
}
__device__ __forceinline__ float tr16(float a, float b) {
  auto s = __builtin_amdgcn_permlane16_swap(__float_as_uint(a), __float_as_uint(b), false, false);
  return __uint_as_float(s[0]) + __uint_as_float(s[1]);
}
template <int CTRL> __device__ __forceinline__ float trd(float lo, float hi, bool up) {
  const float t1 = lo + dpp<CTRL>(lo), t2 = hi + dpp<CTRL>(hi);
  return up ? t2 : t1;
}
__device__ __forceinline__ float wave_sum(float x) {
  x += dpp<XOR1>(x); x += dpp<XOR2>(x); x += dpp<XOR7>(x); x += dpp<XOR8>(x);
  x = tr16(x, x);
  return tr32(x, x);
}
__device__ __forceinline__ float wave_max(float x) {
  x = fmaxf(x, dpp<XOR1>(x)); x = fmaxf(x, dpp<XOR2>(x)); x = fmaxf(x, dpp<XOR7>(x)); x = fmaxf(x, dpp<XOR8>(x));
  auto s = __builtin_amdgcn_permlane16_swap(__float_as_uint(x), __float_as_uint(x), false, false);
  x = fmaxf(__uint_as_float(s[0]), __uint_as_float(s[1]));
  auto t = __builtin_amdgcn_permlane32_swap(__float_as_uint(x), __float_as_uint(x), false, false);
  return fmaxf(__uint_as_float(t[0]), __uint_as_float(t[1]));
}
__device__ __forceinline__ float sum4_parity(f32x4 v) {
  float s = tr16(tr32(v[0], v[2]), tr32(v[1], v[3]));
  s += dpp<XOR8>(s); s += dpp<ROR4>(s); s += dpp<XOR2>(s);
  return s;
}

__device__ __forceinline__ rsrc_t mk_rsrc(const void* p, unsigned bytes) { return __builtin_amdgcn_make_buffer_rsrc(const_cast<void*>(p), 0, bytes, 0x00020000); }
template <int AUX> __device__ __forceinline__ v4u ld128(rsrc_t r, unsigned v, unsigned s) { return __builtin_amdgcn_raw_buffer_load_b128(r, v, s, AUX); }
template <int AUX> __device__ __forceinline__ v2u ld64(rsrc_t r, unsigned v, unsigned s) { return __builtin_amdgcn_raw_buffer_load_b64(r, v, s, AUX); }
__device__ __forceinline__ void st128(v4u d, rsrc_t r, unsigned off) { __builtin_amdgcn_raw_buffer_store_b128(d, r, off, 0, SC1); }
__device__ __forceinline__ void st64(v2u d, rsrc_t r, unsigned off) { __builtin_amdgcn_raw_buffer_store_b64(d, r, off, 0, SC1); }
__device__ __forceinline__ unsigned tag1(float v, unsigned tag) { return ((__float_as_uint(v) + 0x800u) & 0xfffff000u) | (tag & 0xfffu); }
__device__ __forceinline__ v4u tag4(f32x4 v, unsigned tag) { return v4u{tag1(v[0], tag), tag1(v[1], tag >> 12), tag1(v[2], tag), tag1(v[3], tag >> 12)}; }
__device__ __forceinline__ f32x4 untag4(v4u g, unsigned tag, unsigned& bad) {
  const unsigned lo = tag & 0xfffu, hi = (tag >> 12) & 0xfffu;
  g ^= v4u{lo, hi, lo, hi};
  bad |= (g[0] | g[1]) | (g[2] | g[3]);
  return __builtin_bit_cast(f32x4, g);
}
__device__ __forceinline__ bool all_fresh(unsigned bad) { return !__any((bad & 0xfffu) != 0u); }

__device__ __forceinline__ float row_sum(float x) { x += dpp<XOR1>(x); x += dpp<XOR2>(x); x = tr16(x, x); return tr32(x, x); }
__device__ __forceinline__ void ln_stats(const v4u (&x)[4], float& mean, float& inv) {
  float s = 0.f;
#pragma unroll
  for (int t = 0; t < 4; ++t)
#pragma unroll
    for (int j = 0; j < 4; ++j) s += bflo(x[t][j]) + bfhi(x[t][j]);
  const float mu = row_sum(s) * (1.0f / DM);
  float q = 0.f;
#pragma unroll
  for (int t = 0; t < 4; ++t)
#pragma unroll
    for (int j = 0; j < 4; ++j) { const float d0 = bflo(x[t][j]) - mu, d1 = bfhi(x[t][j]) - mu; q = fmaf(d0, d0, q); q = fmaf(d1, d1, q); }
  const float var = row_sum(q) * (1.0f / DM);
  mean = rbf(mu);
  inv = rcp_(rbf(__builtin_amdgcn_sqrtf(rbf(rbf(var) + 1e-5f))));
}
__device__ __forceinline__ v4u ln_apply(v4u x, float mean, float inv) {
  v4u o;
#pragma unroll
  for (int j = 0; j < 4; ++j) { const unsigned d = pk_bf16(bflo(x[j]) - mean, bfhi(x[j]) - mean); o[j] = pk_bf16(bflo(d) * inv, bfhi(d) * inv); }
  return o;
}

template <int J, class T> __device__ __forceinline__ v4u bfrag(const T* t) {
  v4u f;
#pragma unroll
  for (int d = 0; d < 4; ++d) f[d] = __builtin_amdgcn_perm(t[2 * d + 1][J >> 1], t[2 * d][J >> 1], (J & 1) ? 0x07060302u : 0x05040100u);
  return f;
}
template <int LO, int HI> __device__ __forceinline__ void tile_load(v2u (&u)[32], rsrc_t r, unsigned voff, unsigned stride) {
#pragma unroll
  for (int n = LO; n < HI; ++n) u[n] = ld64<NT>(r, voff, n * stride);
  FENCE();
}
__device__ __forceinline__ f32x4 tile_mul(v4u a, const v2u (&u)[32]) {
  f32x4 o = {0.f, 0.f, 0.f, 0.f};
  static_for<16>([&](auto N) { constexpr int s = N >> 2, j = N & 3; o[j] += mfma(a, bfrag<j>(u + 8 * s), f32x4{0.f, 0.f, 0.f, 0.f})[s]; });
  return o;
}

constexpr unsigned WS_E = 0;
constexpr unsigned WS_LS = 4096;
constexpr unsigned WS_CK = 32768;
constexpr unsigned WS_X1 = 65536;
constexpr unsigned WS_MP = WS_X1 + 32 * 4096;
constexpr unsigned WS_TB = WS_MP + 16 * 8192;
constexpr unsigned WS_QX = WS_TB + 4 * 64 * 2048;
constexpr unsigned WS_BYTES = WS_QX + 32 * 8192;
constexpr int L_Q = 0, L_M = 256, L_F = 272, L_TAG = 288, L_REF = 292, L_RDY = 296, L_T0 = 300, L_T = 304, L_GX = 320,
              L_H = 1024, L_O = 2048, O_ROW = 144, L_S = L_O + 16 * O_ROW, L_BYTES = L_S + 8192;

typedef const unsigned short* cptr;
struct Params { cptr wo, wq; unsigned char* ws; cptr k, v, x, w2, w1, b1, b2; unsigned short* out; };

__global__ void __launch_bounds__(512, 2) k_fused(Params) {
  const __attribute__((address_space(4))) Params* ap = (const __attribute__((address_space(4))) Params*)__builtin_amdgcn_kernarg_segment_ptr();
  Params p; p.k = ap->k; p.v = ap->v; p.x = ap->x; p.wq = ap->wq; p.ws = ap->ws; p.wo = ap->wo; p.w1 = ap->w1; p.w2 = ap->w2;
  FENCE();
  __shared__ __attribute__((aligned(16))) unsigned char lds[L_BYTES];
  auto flag_get = [&](int off) { return __hip_atomic_load((lds_u32*)lds + off / 4, __ATOMIC_RELAXED, __HIP_MEMORY_SCOPE_WORKGROUP); };
  auto flag_set = [&](int off, unsigned v) { __hip_atomic_store((lds_u32*)lds + off / 4, v, __ATOMIC_RELAXED, __HIP_MEMORY_SCOPE_WORKGROUP); };
  const int blk = blockIdx.x, lane = threadIdx.x & 63, w = __builtin_amdgcn_readfirstlane(threadIdx.x >> 6);
  const int idx = blk >> 3, h = (blk & 7) * 4 + (idx & 3), bs = idx >> 2, b = bs >> 1, sp = bs & 1;
  const int r = lane >> 4, c = lane & 15;
  const unsigned l16 = lane * 16;
  unsigned kvoff = ((b * NH + h) * SK + sp * 2048) * (DH * 2), kx = (w & 3) * 8192 + bs * 65536;
  asm volatile("" : "+s"(kvoff), "+s"(kx));
  asm volatile("" :: "s"(p.x), "s"(p.k), "s"(p.v), "s"(p.wq), "s"(p.wo), "s"(p.w1), "s"(p.w2), "s"(p.ws));
  const unsigned rot = b * 32 + sp * 4;

  if (w >= 4) {
    const int a = w - 4, prod = 2 * h + sp;
    *(v4u*)(lds + L_M) = v4u{SENT, SENT, SENT, SENT}; *(v4u*)(lds + L_TAG) = v4u{0u, 0u, 0u, 0u};
    if (a != 0) WG_BARRIER();
    FENCE();
    const rsrc_t rws = mk_rsrc(p.ws, WS_BYTES), rx = mk_rsrc(p.x, NB * DM * 2);
    const unsigned xoff = ((lane >> 2) & 3) * (DM * 2) + ((lane & 3) + 4 * r) * 16;
    if (a == 0) {
      v4u xs[4]; v2u U[32];
#pragma unroll
      for (int t = 0; t < 4; ++t) xs[t] = ld128<0>(rx, xoff, ((t + b) & 3) * 256);
      FENCE();
      tile_load<0, 32>(U, mk_rsrc(p.wq + (size_t)b * 128 * (NH * DH) + h * DH + sp * 64, 128 * NH * DH * 2), r * 32 * 8192 + c * 8, 8192);
      const unsigned ep = __builtin_amdgcn_raw_buffer_load_b32(rws, 0, WS_E, SC1);
      FENCE();
      WG_BARRIER();
      float mean, inv;
      ln_stats(xs, mean, inv);
      const f32x4 g = tile_mul(ln_apply(xs[0], mean, inv), U);
      FENCE();
      const unsigned tg = __builtin_amdgcn_readfirstlane(ep) * 0x9E3779B1u + 0x7F4A7C15u;
      st128(tag4(g, tg), rws, WS_QX + h * 8192 + bs * 1024 + l16);
      asm volatile("s_waitcnt vmcnt(0)" ::: "memory");
      __builtin_amdgcn_s_sleep(16);
      f32x4 q4;
#pragma unroll 1
      for (unsigned spin = 0; spin < CAP_FAST; ++spin) {
        const v4u G0 = ld128<SC1>(rws, r * 1024 + c * 16, WS_QX + h * 8192 + b * 256), G1 = ld128<SC1>(rws, r * 1024 + c * 16, WS_QX + h * 8192 + b * 256 + 4096);
        unsigned bad = 0;
        q4 = untag4(G0, tg, bad); q4 += untag4(G1, tg, bad);
        if (all_fresh(bad)) break;
        ORDER();
      }
      *(unsigned*)(lds + L_Q + (lane & 31) * 8 + (lane >> 5) * 4) = pk_bf16(tr32(q4[0], q4[2]), tr32(q4[1], q4[3]));
      *(unsigned*)(lds + L_TAG) = tg;
      ORDER();
      flag_set(L_RDY, 1u);
    }
#pragma unroll 1
    for (unsigned spin = 0; spin < CAP_LDS; ++spin) {
      if (__builtin_amdgcn_readfirstlane(flag_get(L_RDY))) break;
      __builtin_amdgcn_s_sleep(4);
    }
    ORDER();
    const unsigned tag = __builtin_amdgcn_readfirstlane(*(const unsigned*)(lds + L_TAG));
    asm volatile("" : "+s"(ap));
    p.b1 = ap->b1; p.b2 = ap->b2; p.out = ap->out;
    asm volatile("" :: "s"(p.b1), "s"(p.b2), "s"(p.out));

    if (a == 2 && h < 28) {
      const int cg = (h & 1) * 4 + b;
      const unsigned stride = sp ? DM * 2 : FF * 2;
      const rsrc_t rt = mk_rsrc(sp ? p.w2 + (size_t)(h >> 1) * 128 * DM + cg * 64 : p.w1 + (size_t)b * 128 * FF + h * 64, 128 * stride);
      v2u U[32]; v4u A;
      tile_load<0, 16>(U, rt, r * 32 * stride + c * 8, stride);
      TOUCH(U[15]); FENCE();
      tile_load<16, 32>(U, rt, r * 32 * stride + c * 8, stride);
      if (sp == 0) {
        v4u xs[4];
#pragma unroll
        for (int t = 0; t < 4; ++t) xs[t] = ld128<0>(rx, xoff, ((t + b) & 3) * 256);
#pragma unroll 1
        for (int it = 0; it < 2; ++it) {
          float mean, inv;
          ln_stats(xs, mean, inv);
#pragma unroll
          for (int t = 0; t < 4; ++t) xs[t] = ln_apply(xs[t], mean, inv);
        }
        A = xs[0];
      } else {
        v2u b1v[2];
#pragma unroll
        for (int k = 0; k < 2; ++k) b1v[k] = ld64<0>(mk_rsrc(p.b1 + (h >> 1) * 128, 256), c * 8, k * 128);
        f32x4 z4[2];
#pragma unroll 1
        for (unsigned spin = 0; spin < CAP_SLOW; ++spin) {
          __builtin_amdgcn_s_sleep(112); __builtin_amdgcn_s_sleep(112);
          v4u G[8];
#pragma unroll
          for (int j = 0; j < 8; ++j) G[j] = ld128<SC1>(rws, l16, WS_X1 + (h >> 1) * 8192 + j * 1024);
          unsigned bad = 0;
#pragma unroll
          for (int j = 0; j < 8; ++j) { const f32x4 v = untag4(G[j], tag, bad); z4[j >> 2] = (j & 3) ? z4[j >> 2] + v : v; }
          if (all_fresh(bad)) break;
          ORDER();
        }
#pragma unroll
        for (int k = 0; k < 2; ++k) {
          float hv[4];
#pragma unroll
          for (int j = 0; j < 4; ++j) {
            const float z = rbf(rbf(z4[k][j]) + ((j & 1) ? bfhi(b1v[k][j >> 1]) : bflo(b1v[k][j >> 1])));
            hv[j] = z * rcp_(1.0f + exp2_((-2.0f * 0.7978845608028654f * LOG2E) * (z + 0.044715f * z * z * z)));
          }
          *(v2u*)(lds + L_H + r * 256 + k * 128 + c * 8) = v2u{pk_bf16(hv[0], hv[1]), pk_bf16(hv[2], hv[3])};
        }
        asm volatile("s_waitcnt lgkmcnt(0)" ::: "memory");
        A = *(const v4u*)(lds + L_H + ((lane >> 2) & 3) * 256 + ((lane & 3) + 4 * r) * 16);
      }
      st128(tag4(tile_mul(A, U), tag), rws, (sp ? WS_MP + (h >> 1) * 8192 + cg * 1024 : WS_X1 + h * 4096 + b * 1024) + l16);
    }

    const unsigned ck_st = (lane == 0 && (idx & 3) == 0) ? WS_CK + (bs * 8 + (blk & 7)) * 8 : OOB;
    unsigned t0 = 0;
    if (a == 3) {
#pragma unroll 1
      for (unsigned spin = 0; spin < CAP_LDS; ++spin) {
        t0 = __builtin_amdgcn_readfirstlane(flag_get(L_T0));
        if (t0) break;
        __builtin_amdgcn_s_sleep(4);
      }
      st64(v2u{t0, tag}, rws, ck_st);
    }

    v8u Bf[2][8];
    {
      const rsrc_t ro = mk_rsrc(p.wo + (size_t)h * DH * DM + a * 128, DH * DM * 2);
      const unsigned vo = r * 8 * 1024 + c * 16;
      v4u T[32]; v2u ck;
      static_for<32 + DO>([&](auto N) { constexpr int n = N;
        if constexpr (n >= DO) TOUCH(T[n - DO]);
        FENCE();
        if constexpr (n == 10) ck = ld64<SC1>(rws, a == 3 ? lane * 8 : OOB, WS_CK);
        if constexpr (n == 10 + DO) { TOUCH(ck);
          if (a == 3) {
            const unsigned tn = clock10ns();
            const float dt = wave_sum((float)(int)((ck[1] == tag ? ck[0] : tn) - t0)) * (1.0f / 64);
            flag_set(L_REF, (t0 + (int)dt + (128 + LD - NA) * TAU_K / 16 - LEAD0) | 1u); } }
        FENCE();
        if constexpr (n < 32) T[n] = ld128<0>(ro, vo, (((n >> 3) * 32 + (n & 7)) ^ rot) * 1024);
        FENCE();
        if constexpr (n >= DO && ((n - DO) & 7) == 7) { constexpr int s = (n - DO) >> 3;
          static_for<8>([&](auto J) { constexpr int j = J; const v4u f = bfrag<j>(T + 8 * s);
            if constexpr (s & 1) Bf[s >> 1][j].hi = f; else Bf[s >> 1][j].lo = f;
            PIN(Bf[s >> 1][j]); }); }
        FENCE(); });
    }

    const int cons = (blk & 7) * 8 + (idx & 7), col0 = 32 * (cons >> 2) + 4 * (cons & 1) + (cons & 2);
    const unsigned co = (lane & 1) * 32 + (r >> 1) * 16 + (r & 1) * 2;
    if (a == 3) {
      unsigned mb;
#pragma unroll 1
      for (unsigned spin = 0; spin < CAP_LDS; ++spin) {
        mb = flag_get(L_M + 4 * (lane & 3));
        if (__all(mb != SENT)) break;
        __builtin_amdgcn_s_sleep(4);
      }
      ORDER();
      const float m4 = __uint_as_float(mb);
      float M = fmaxf(m4, dpp<XOR1>(m4)); M = fmaxf(M, dpp<XOR2>(M));
      float L = 0.f;
#pragma unroll
      for (int i = 0; i < 8; ++i) { const f32x4 s4 = *(const f32x4*)(lds + L_S + i * 1024 + l16);
#pragma unroll
        for (int j = 0; j < 4; ++j) L += exp2_(s4[j] - M); }
      L = wave_sum(L);
      const float lse = M + __builtin_amdgcn_logf(L);
      const unsigned tk = flag_get(L_T);
      st128(v4u{__float_as_uint(lse), tag, tk, tag}, rws, lane == 0 ? WS_LS + (b * 64 + prod) * 16 : OOB);
      const unsigned long long need = blk == 0 ? ~0ull : 1ull << (prod ^ 1);
      float other; unsigned mean;
#pragma unroll 1
      for (unsigned spin = 0; spin < CAP_SLOW; ++spin) {
        __builtin_amdgcn_s_sleep(80);
        const v4u m = ld128<SC1>(rws, l16, WS_LS + b * 1024);
        const bool ok = ((m[1] ^ tag) | (m[3] ^ tag)) == 0u;
        const unsigned tn = clock10ns();
        const float dt = __builtin_amdgcn_fmed3f(wave_sum((float)(int)((ok ? m[2] : tn) - tk)) * (1.0f / 64), -1000.f, 1000.f);
        mean = __builtin_amdgcn_readfirstlane(tk + (int)dt);
        flag_set(L_REF, (mean - LEAD1) | 1u);
        other = __uint_as_float(__builtin_amdgcn_readlane(m[0], prod ^ 1));
        if ((__ballot(ok) & need) == need) break;
        ORDER();
      }
      *(float*)(lds + L_F + 4 * (lane & 3)) = exp2_(m4 - M) * rcp_(L) * rcp_(1.0f + exp2_(other - lse));
      __builtin_amdgcn_raw_buffer_store_b32(tag, rws, (blk == 0 && lane == 0) ? WS_E : OOB, 0, SC1);
      while ((int)(clock10ns() - mean) < T_LAST) __builtin_amdgcn_s_sleep(2);
      st64(v2u{flag_get(L_T), tag}, rws, ck_st + 512);
      __builtin_amdgcn_s_sleep(36);
      const v2u ck = ld64<SC1>(rws, lane * 8, WS_CK + 512);
      const bool ok = ck[1] == tag;
      const int dt = (int)(wave_sum(ok ? (float)(int)(ck[0] - mean) : 0.f) * rcp_(fmaxf(wave_sum(ok ? 1.f : 0.f), 1.f)));
      flag_set(L_REF, (mean + dt - LEAD2) | 1u);
    } else if (a == 2) {
      const unsigned b2v = __builtin_amdgcn_raw_buffer_load_b16(mk_rsrc(p.b2, DM * 2), co, col0 * 2, 0);
      const unsigned xv = __builtin_amdgcn_raw_buffer_load_b16(rx, co, (b * DM + col0) * 2, 0);
      const unsigned mo = lane < 28 ? (unsigned)((lane >> 1) * 8192 + (lane & 1) * 64) : OOB, ms = WS_MP + (col0 >> 6) * 1024 + b * 256 + (col0 & 63) * 4;
      f32x4 m4;
#pragma unroll 1
      for (unsigned spin = 0; spin < CAP_SLOW; ++spin) {
        __builtin_amdgcn_s_sleep(64);
        const v2u G0 = ld64<SC1>(rws, mo, ms), G1 = ld64<SC1>(rws, mo, ms + 32);
        unsigned bad = 0;
        m4 = untag4(v4u{G0[0], G0[1], G1[0], G1[1]}, lane < 28 ? tag : 0u, bad);
        if (all_fresh(bad)) break;
        ORDER();
      }
      const float m = rbf(rbf(sum4_parity(m4)) + bflo(b2v));
      *(f32x2*)(lds + L_GX + (r * 2 + (lane & 1)) * 8) = f32x2{rbf(m * rcp_(1.0f + exp2_(-LOG2E * m))), bflo(xv)};
    }

    unsigned a_off = L_O + c * O_ROW + r * 16, sidx = (lane & 2) ? 0xeeeeu : 0x4444u;
    unsigned tb_off = WS_TB + (b * 64 + prod) * 2048 + a * 512 + (c >> 2) * 128 + r * 32 + (c & 3) * 8;
    unsigned go = (lane >> 1) * 2048 + (lane & 1) * 16, out_off = (lane & 14) == 0 ? (b * DM + col0) * 2 + co : OOB;
    f32x4 d[8];
#pragma unroll
    for (int j = 0; j < 8; ++j) { d[j] = f32x4{0.f, 0.f, 0.f, 0.f}; PIN(d[j]); }
    PIN(a_off); PIN(sidx); PIN(tb_off); PIN(go); PIN(out_off);
    asm volatile("s_branch 1f\n\t.p2align 11\n1:");
    WG_BARRIER();
    const v4u A0 = *(const v4u*)(lds + a_off), A1 = *(const v4u*)(lds + a_off + 64);
    const float f = *(const float*)(lds + L_F + r * 4);
    FENCE();
    float t[8];
    auto mul = [&](int j) { d[j] = smfma(A0, Bf[0][j], d[j], sidx); d[j + 4] = smfma(A0, Bf[0][j + 4], d[j + 4], sidx);
      d[j] = smfma(A1, Bf[1][j], d[j], sidx); d[j + 4] = smfma(A1, Bf[1][j + 4], d[j + 4], sidx); };
    auto sum = [&](int j) { const f32x2 u2 = d[j].lo + d[j].hi; t[j] = (u2[0] + u2[1]) * f; PIN(t[j]); };
    mul(0); mul(2); mul(1); mul(3);
    sum(0); sum(4); sum(2); sum(6);
    unsigned w0 = tag1(tr32(tr16(t[0], t[4]), tr16(t[2], t[6])), tag); PIN(w0);
    sum(1); sum(5);
    float x1 = tr16(t[1], t[5]); PIN(x1);
    sum(3); sum(7);
    const unsigned w1 = tag1(tr32(x1, tr16(t[3], t[7])), tag >> 12);
    __builtin_amdgcn_sched_group_barrier(0x8, 7, 0);
#pragma unroll
    for (int k = 0; k < 9; ++k) { __builtin_amdgcn_sched_group_barrier(0x2, 3, 0); __builtin_amdgcn_sched_group_barrier(0x8, 1, 0); }
    FENCE();
    st64(v2u{w0, w1}, rws, tb_off);
    if (a == 0) {
      const f32x2 gx = *(const f32x2*)(lds + L_GX + (r * 2 + (lane & 1)) * 8);
      const unsigned t1 = clock10ns();
      const int wait = (int)(__builtin_amdgcn_readfirstlane(flag_get(L_REF)) + LEAD2 + 8 * TAU_V + T_LOOK - t1);
      __builtin_amdgcn_s_sleep(4);
      while ((int)(clock10ns() - t1) < (wait < 120 ? wait : 120)) {}
      const rsrc_t rout = mk_rsrc(p.out, NB * DM * 2);
      unsigned o;
#pragma unroll 1
      for (unsigned spin = 0; spin < CAP_FAST; ++spin) {
        const v4u G0 = ld128<SC1>(rws, go, WS_TB + b * 131072 + cons * 32), G1 = ld128<SC1>(rws, go, WS_TB + b * 131072 + 65536 + cons * 32);
        unsigned bad = 0;
        f32x4 s4 = untag4(G0, tag, bad); s4 += untag4(G1, tag, bad);
        o = bf_bits(gx[1] + rbf(rbf(sum4_parity(s4)) * gx[0]));
        PIN(o);
        if (all_fresh(bad)) break;
        ORDER();
      }
      __builtin_amdgcn_raw_buffer_store_b16((unsigned short)o, rout, out_off, 0, 0);
    }
    return;
  }

  constexpr unsigned KVLEN = 2048 * DH * 2;
  const char* kp = (const char*)p.k + kvoff; const char* vp = (const char*)p.v + kvoff;
  const rsrc_t rk = mk_rsrc(kp, KVLEN);
  auto kvload = [&](rsrc_t rs, unsigned o, int i) { return ld128<NT>(rs, l16 + (i & 3) * 1024, o ^ (((i >> 2) & 1) * 4096 + (i >> 3) * 32768)); };
  float* const scs = (float*)(lds + L_S + w * 2048) + lane;
  v4u S[NU + LD], q;
  unsigned now, refv;
  unsigned mask = 0x1249u, org = 0;
  auto pace = [&](int sched) {
    const unsigned rf = __builtin_amdgcn_readfirstlane(refv);
    org = now - sched + ((mask & 1) ? 0 : TAU_V / 4);
    int lv = (int)(org - rf) >> 5;
    asm volatile("" : "+s"(lv));
    lv = rf ? lv : 3;
    unsigned m = 0xffffu; m = lv > 0 ? 0x5555u : m; m = lv > 1 ? 0x1111u : m; m = lv > 2 ? 0x1249u : m;
    m = lv > 3 ? 0x0421u : m; m = lv > 4 ? 0x0101u : m; m = lv > 5 ? 0u : m;
    mask = __builtin_amdgcn_readfirstlane(m); };
  float ka[8], mw = -INFINITY;
  auto kdot = [&](v4u t, int i) {
    float d = dot2(t[0], q[0], 0.f); d = dot2(t[1], q[1], d); d = dot2(t[2], q[2], d); d = dot2(t[3], q[3], d);
    if (i >= 8) { i -= 8; d = trd<XOR8>(ka[i], d, lane & 8);
      if (i >= 4) { i -= 4; d = trd<XOR7>(ka[i], d, lane & 4);
        if (i >= 2) { i -= 2; d = trd<XOR2>(ka[i], d, lane & 2);
          if (i >= 1) { i -= 1; d = trd<XOR1>(ka[i], d, lane & 1); } } } }
    PIN(d); ka[i] = d; };
  auto kdone = [&](int g) { const float s = ka[0] * C2; scs[g * 64] = s; mw = fmaxf(mw, s); PIN(mw); };

  static_for<NU + LD>([&](auto N) {
    constexpr int n = N, dn = n < N0 ? D0 : D1;
    if constexpr (n >= dn) TOUCH(S[n - dn]);
    FENCE();
    S[n] = kvload(rk, kx, n);
    if constexpr (n == 0) __builtin_amdgcn_s_setprio(2);
    if constexpr (n == NS0) { FENCE(); WG_BARRIER(); }
    FENCE();
    if constexpr (n == NA - 1) {
#pragma unroll 1
      for (unsigned spin = 0; spin < CAP_LDS; ++spin) {
        const unsigned rdy = flag_get(L_RDY);
        ORDER();
        q = *(const v4u*)(lds + L_Q + c * 16);
        if (__builtin_amdgcn_readfirstlane(rdy)) break;
        __builtin_amdgcn_s_sleep(1);
      }
      now = clock10ns();
    }
    if constexpr (n == NA) flag_set(L_T0, now | 1u);
    if constexpr (n >= NA) {
      constexpr int hi = (n - NA + 1) * 4 < n - LD + 1 ? (n - NA + 1) * 4 : n - LD + 1;
      constexpr int lo = (n - NA) * 4 < n - LD ? (n - NA) * 4 : n - LD;
      static_for<hi - lo>([&](auto J) { constexpr int i = lo + J; kdot(S[i], i & 15); if constexpr ((i & 15) == 15) kdone(i >> 4); });
    }
    FENCE();
  });
  v4u R[16];
#pragma unroll
  for (int i = 0; i < LD; ++i) R[i] = S[NU + i];
  float snext = scs[0];
  v4u sel;
#pragma unroll
  for (int j = 0; j < 4; ++j) { const unsigned pick = (c & 8) ? 0x0302u : 0x0100u;
    sel[j] = ((c >> 1) & 3) != j ? 0x0c0c0c0cu : (c & 1) ? (pick << 16) | 0x0c0cu : 0x0c0c0000u | pick; }
  unsigned o_off = L_O + (4 * w + (r & 2)) * O_ROW + ((c * 8 + (r & 1) * 4) ^ rot);
  PIN(o_off); PIN(sel);
#pragma unroll 1
  for (int g = NU / 16; g < 8; ++g) {
    const rsrc_t rn = mk_rsrc(g == 7 ? vp : kp, KVLEN);
    const unsigned o0 = (g * 65536) ^ kx, o1 = (((g + 1) & 7) * 65536) ^ kx;
    static_for<16>([&](auto I) { constexpr int i = I;
      TOUCH(R[i]); THROTTLE(mask, i); FENCE();
      R[(i + LD) & 15] = i + LD < 16 ? kvload(rk, o0, i + LD) : kvload(rn, o1, i + LD - 16);
      if constexpr (i == 0) { now = clock10ns(); refv = flag_get(L_REF); }
      if constexpr (i == 2) pace((g - 8) * TAU_K);
      FENCE();
      kdot(R[i], i);
      FENCE(); });
    kdone(g);
  }
  mw = wave_max(mw);
  flag_set(L_T + 4 * w, org);
  ORDER();
  flag_set(L_M + 4 * w, min(__float_as_uint(mw), SENT - 1u));
  f32x4 acc = {0.f, 0.f, 0.f, 0.f};
  const rsrc_t rv = mk_rsrc(vp, KVLEN);
#pragma unroll 1
  for (int g = 0; g < 8; ++g) {
    const rsrc_t rn = mk_rsrc(vp, g == 7 ? 0u : KVLEN);
    const unsigned o0 = (g * 65536) ^ kx, o1 = (((g + 1) & 7) * 65536) ^ kx;
    const float pg = exp2_(snext - mw), ph = rbf(pg);
    const unsigned pp = pk_bf16(ph, pg - ph);
    snext = scs[((g + 1) & 7) * 64];
    static_for<16>([&](auto I) { constexpr int i = I;
      const unsigned pb = __builtin_amdgcn_mov_dpp(pp, BCAST + i, 0xf, 0xf, true);
      v4u A;
#pragma unroll
      for (int j = 0; j < 4; ++j) A[j] = __builtin_amdgcn_perm(pb, pb, sel[j]);
      PIN(A);
      FENCE();
      TOUCH(R[i]); THROTTLE(mask, i); FENCE();
      R[(i + LD) & 15] = i + LD < 16 ? kvload(rv, o0, i + LD) : kvload(rn, o1, i + LD - 16);
      if constexpr (i == 0) { now = clock10ns(); refv = flag_get(L_REF); }
      if constexpr (i == 2) { pace(g * TAU_V); mask &= g == 7 ? 0xffu : 0xffffu; flag_set(L_T + 4 * w, org); }
      FENCE();
      acc = mfma(A, R[i], acc);
      FENCE(); });
    const float e0 = tr32(acc[0], acc[2]), e1 = tr32(acc[1], acc[3]);
    const unsigned hi = pk_bf16(e0, e1), lo = pk_bf16(e0 - bflo(hi), e1 - bfhi(hi));
    *(unsigned*)(lds + o_off) = hi;
    *(unsigned*)(lds + o_off + O_ROW) = lo;
    ORDER();
  }
  WG_BARRIER();
}

extern "C" void kernel_launch(void* const* d_in, const int* in_sizes, int n_in, void* d_out, int out_size, void* d_ws, size_t ws_size,
                              hipStream_t stream) {
  Params p;
  p.x = (const unsigned short*)d_in[0]; p.k = (const unsigned short*)d_in[1]; p.v = (const unsigned short*)d_in[2];
  p.wq = (const unsigned short*)d_in[3]; p.wo = (const unsigned short*)d_in[4];
  p.w1 = (const unsigned short*)d_in[6]; p.b1 = (const unsigned short*)d_in[7]; p.w2 = (const unsigned short*)d_in[8]; p.b2 = (const unsigned short*)d_in[9];
  p.out = (unsigned short*)d_out;
  p.ws = (unsigned char*)d_ws;
  k_fused<<<256, 512, 0, stream>>>(p);
}
```
